```python
import torch

def _softmax(x, axis=-1):
    return torch.softmax(x, dim=axis)
def _gelu(x):
    return torch.nn.functional.gelu(x, approximate="tanh")
def _silu(x):
    return torch.nn.functional.silu(x)
def _sigmoid(x):
    return torch.sigmoid(x)
def _layernorm(x, axis=-1, eps=1e-5):
    m = x.mean(dim=axis, keepdim=True)
    v = x.var(dim=axis, keepdim=True, unbiased=False)
    return (x - m) / torch.sqrt(v + eps)
def _scatter_add(tgt, idx, src, axis=0):
    return tgt.clone().index_add_(axis, idx, src)
def _segment_sum(src, seg, n):
    out = torch.zeros((n,) + src.shape[1:], dtype=src.dtype)
    return out.index_add_(0, seg, src)
def _pad(x, pw):
    flat = tuple(v for pair in reversed(pw) for v in pair)
    return torch.nn.functional.pad(x, flat)

kernel_name = 'interleave_layers__mha_block__dense_activation_relu__prompt'

def setup_inputs():

    _w = (lambda a: (lambda t: t.to(torch.bfloat16) if t.is_floating_point() else t)(torch.as_tensor(a)))
    def _A_setup():
        B, S, D, H = 1, 2048, 512, 32
        return {
            "x": _w((lambda *s: torch.randn(*s, dtype=torch.bfloat16) * 0.05)(B, S, D)),
            "wqkv": _w((lambda *s: torch.randn(*s, dtype=torch.bfloat16) * 0.05)(D, 3 * D)),
            "wo": _w((lambda *s: torch.randn(*s, dtype=torch.bfloat16) * 0.05)(D, D)),
        }
    _A_in = _A_setup()

    def _B_setup():
        B, S, D, Dff = 1, 2048, 512, 1792
        return {
            "x": _w((lambda *s: torch.randn(*s, dtype=torch.bfloat16) * 0.05)(B, S, D)),
            "w1": _w((lambda *s: torch.randn(*s, dtype=torch.bfloat16) * 0.05)(D, Dff)),
            "w2": _w((lambda *s: torch.randn(*s, dtype=torch.bfloat16) * 0.05)(Dff, D)),
        }
    _B_in = _B_setup()

    return {
        "A_x": _A_in["x"],
        "A_wqkv": _A_in["wqkv"],
        "A_wo": _A_in["wo"],
        "B_x": _B_in["x"],
        "B_w1": _B_in["w1"],
        "B_w2": _B_in["w2"],
    }

def reference(A_x, A_wqkv, A_wo, B_x, B_w1, B_w2):
    def _A_ref(x, wqkv, wo):
        B, S, D = x.shape
        H = 32
        Dh = D // H
        qkv = (lambda x, *d: x.permute(*d))((x @ wqkv).reshape(B, S, 3, H, Dh), 2, 0, 3, 1, 4)
        q, k, v = qkv[0], qkv[1], qkv[2]
        scores = torch.einsum("bhqd,bhkd->bhqk", q, k) * (Dh ** -0.5)
        p = _softmax(scores, axis=-1)
        o = torch.einsum("bhqk,bhkd->bhqd", p, v)
        return (lambda x, *d: x.permute(*d))(o, 0, 2, 1, 3).reshape(B, S, D) @ wo

    def _B_ref(x, w1, w2):
        h = torch.einsum("bsd,df->bsf", x, w1)
        zero = torch.zeros_like(h)
        h = torch.maximum(h, zero)
        return torch.einsum("bsf,fd->bsd", h, w2)

    x = A_x
    x = x + _A_ref(_layernorm(x, axis=-1), A_wqkv, A_wo)
    x = x + _B_ref(_layernorm(x, axis=-1), B_w1, B_w2)
    x = x + _A_ref(_layernorm(x, axis=-1), A_wqkv, A_wo)
    x = x + _B_ref(_layernorm(x, axis=-1), B_w1, B_w2)
    return x
```

```cpp
#include <hip/hip_runtime.h>
#include <hip/hip_bf16.h>
#include <stdint.h>

typedef unsigned short u16;
__device__ __forceinline__ float b2f(u16 v){ return __uint_as_float(((unsigned)v)<<16); }
__device__ __forceinline__ u16 f2b(float f){ unsigned u=__float_as_uint(f); u += 0x7FFF + ((u>>16)&1); return (u16)(u>>16); }

constexpr int S=2048,D=512,H=32,DH=16,FF=1792;

template<bool BF>
__global__ void ln_k(const void* xin, u16* xn){
  int row = blockIdx.x*4 + (threadIdx.x>>6); int lane=threadIdx.x&63;
  float v[8];
  for(int i=0;i<8;i++){ v[i] = BF ? b2f(((const u16*)xin)[row*D+lane*8+i]) : ((const float*)xin)[row*D+lane*8+i]; }
  float s=0; for(int i=0;i<8;i++) s+=v[i];
  for(int o=32;o>0;o>>=1) s+=__shfl_xor(s,o);
  float m=s/D; float q=0; for(int i=0;i<8;i++){ float d=v[i]-m; q+=d*d; }
  for(int o=32;o>0;o>>=1) q+=__shfl_xor(q,o);
  float r=1.0f/sqrtf(q/D+1e-5f);
  for(int i=0;i<8;i++) xn[row*D+lane*8+i]=f2b((v[i]-m)*r);
}
__global__ void gemm_k(const u16* A,const u16* B,void* C,const void* res,int N,int K,int mode){
  int col = blockIdx.x*256+threadIdx.x; int row=blockIdx.y;
  float acc=0;
  for(int k=0;k<K;k++) acc += b2f(A[row*K+k])*b2f(B[(long)k*N+col]);
  long o=(long)row*N+col;
  if(mode==0) ((u16*)C)[o]=f2b(acc);
  else if(mode==1) ((u16*)C)[o]=f2b(fmaxf(acc,0.f));
  else if(mode==2) ((float*)C)[o]=b2f(((const u16*)res)[o])+acc;
  else if(mode==3) ((float*)C)[o]=((const float*)res)[o]+acc;
  else ((u16*)C)[o]=f2b(((const float*)res)[o]+acc);
}
__global__ void attn_k(const u16* qkv,u16* O){
  int h=blockIdx.y; int q=blockIdx.x*64+threadIdx.x;
  float qv[16]; for(int d=0;d<16;d++) qv[d]=b2f(qkv[q*1536+h*16+d])*0.25f;
  float m=-1e30f,l=0,o[16]; for(int d=0;d<16;d++) o[d]=0;
  for(int j=0;j<S;j++){
    float s=0; for(int d=0;d<16;d++) s+=qv[d]*b2f(qkv[j*1536+512+h*16+d]);
    float mn=fmaxf(m,s); float a=__expf(m-mn); float p=__expf(s-mn); m=mn;
    l=l*a+p; float pb=b2f(f2b(p));
    for(int d=0;d<16;d++) o[d]=o[d]*a+pb*b2f(qkv[j*1536+1024+h*16+d]);
  }
  for(int d=0;d<16;d++) O[q*512+h*16+d]=f2b(o[d]/l);
}

extern "C" void kernel_launch(void* const* d_in, const int* in_sizes, int n_in,
                              void* d_out, int out_size, void* d_ws, size_t ws_size,
                              hipStream_t stream) {
  const u16* x=(const u16*)d_in[0]; const u16* wqkv=(const u16*)d_in[1]; const u16* wo=(const u16*)d_in[2];
  const u16* w1=(const u16*)d_in[4]; const u16* w2=(const u16*)d_in[5];
  char* ws=(char*)d_ws;
  u16* xn=(u16*)ws; u16* qkv=(u16*)(ws+(4<<20)); u16* O=(u16*)(ws+(12<<20)); u16* hh=(u16*)(ws+(16<<20));
  float* xa=(float*)(ws+(32<<20)); float* xb=(float*)(ws+(40<<20));
  for(int layer=0;layer<2;layer++){
    if(layer==0) ln_k<true><<<S/4,256,0,stream>>>(x,xn); else ln_k<false><<<S/4,256,0,stream>>>(xb,xn);
    gemm_k<<<dim3(1536/256,S),256,0,stream>>>(xn,wqkv,qkv,nullptr,1536,512,0);
    attn_k<<<dim3(S/64,H),64,0,stream>>>(qkv,O);
    if(layer==0) gemm_k<<<dim3(512/256,S),256,0,stream>>>(O,wo,xa,x,512,512,2);
    else gemm_k<<<dim3(512/256,S),256,0,stream>>>(O,wo,xa,xb,512,512,3);
    ln_k<false><<<S/4,256,0,stream>>>(xa,xn);
    gemm_k<<<dim3(1792/256,S),256,0,stream>>>(xn,w1,hh,nullptr,1792,512,1);
    if(layer==0) gemm_k<<<dim3(512/256,S),256,0,stream>>>(hh,w2,xb,xa,512,1792,3);
    else gemm_k<<<dim3(512/256,S),256,0,stream>>>(hh,w2,d_out,xa,512,1792,4);
  }
}
```

```cpp
#include <hip/hip_runtime.h>
#include <hip/hip_bf16.h>
#include <stdint.h>

typedef unsigned short u16;
typedef unsigned int u32;
typedef unsigned long long u64;
typedef u32 u32x4 __attribute__((ext_vector_type(4)));
typedef __amdgpu_buffer_rsrc_t rsrc_t;
#define DI __device__ __forceinline__
#define LAS __attribute__((address_space(3)))
#define GAS __attribute__((address_space(1)))

#define ATTN_ASM \
  "ds_read_b128 v[232:235], %[ka]\n\t" \
  "ds_read_b128 v[244:247], %[ka] offset:1024\n\t" \
  "ds_read_b128 v[236:239], %[a0]\n\t" \
  "ds_read_b128 v[240:243], %[a1]\n\t" \
  "s_waitcnt lgkmcnt(2)\n\t" \
  "v_mfma_f32_32x32x16_bf16 v[192:207], v[232:235], v[112:115], 0\n\t" \
  "s_waitcnt lgkmcnt(0)\n\t" \
  "s_nop 7\n\t" \
  "s_nop 2\n\t" \
  "v_exp_f32 v192, v192\n\t" \
  "v_cvt_pk_bf16_f32 v231, v222, v223\n\t" \
  "v_exp_f32 v193, v193\n\t" \
  "v_exp_f32 v194, v194\n\t" \
  "v_mfma_f32_32x32x16_bf16 v[96:111], v[252:255], v[228:231], v[96:111]\n\t" \
  "ds_read_b128 v[252:255], %[a1] offset:1056\n\t" \
  "v_cvt_pk_bf16_f32 v224, v192, v193\n\t" \
  "v_exp_f32 v195, v195\n\t" \
  "v_exp_f32 v196, v196\n\t" \
  "v_cvt_pk_bf16_f32 v225, v194, v195\n\t" \
  "v_exp_f32 v197, v197\n\t" \
  "v_mfma_f32_32x32x16_bf16 v[208:223], v[232:235], v[116:119], 0\n\t" \
  "ds_read_b128 v[232:235], %[ka] offset:2048\n\t" \
  "v_exp_f32 v198, v198\n\t" \
  "v_cvt_pk_bf16_f32 v226, v196, v197\n\t" \
  "v_exp_f32 v199, v199\n\t" \
  "v_exp_f32 v200, v200\n\t" \
  "v_cvt_pk_bf16_f32 v227, v198, v199\n\t" \
  "v_exp_f32 v201, v201\n\t" \
  "v_exp_f32 v202, v202\n\t" \
  "v_cvt_pk_bf16_f32 v228, v200, v201\n\t" \
  "v_exp_f32 v203, v203\n\t" \
  "v_mfma_f32_32x32x16_bf16 v[80:95], v[236:239], v[224:227], v[80:95]\n\t" \
  "ds_read_b128 v[248:251], %[a0] offset:1056\n\t" \
  "v_exp_f32 v204, v204\n\t" \
  "v_cvt_pk_bf16_f32 v229, v202, v203\n\t" \
  "v_exp_f32 v205, v205\n\t" \
  "v_exp_f32 v206, v206\n\t" \
  "v_cvt_pk_bf16_f32 v230, v204, v205\n\t" \
  "v_exp_f32 v207, v207\n\t" \
  "v_exp_f32 v208, v208\n\t" \
  "v_cvt_pk_bf16_f32 v231, v206, v207\n\t" \
  "v_exp_f32 v209, v209\n\t" \
  "v_exp_f32 v210, v210\n\t" \
  "s_mov_b32 m0, %[mk]\n\t" \
  "s_nop 0\n\t" \
  "v_mfma_f32_32x32x16_bf16 v[80:95], v[240:243], v[228:231], v[80:95]\n\t" \
  "buffer_load_dwordx4 %[l16], %[rs], %[gk] offen sc1 lds\n\t" \
  "v_cvt_pk_bf16_f32 v224, v208, v209\n\t" \
  "v_exp_f32 v211, v211\n\t" \
  "v_exp_f32 v212, v212\n\t" \
  "v_cvt_pk_bf16_f32 v225, v210, v211\n\t" \
  "v_exp_f32 v213, v213\n\t" \
  "s_add_u32 m0, m0, 4096\n\t" \
  "s_add_u32 %[gk], %[gk], 4096\n\t" \
  "v_mfma_f32_32x32x16_bf16 v[192:207], v[244:247], v[112:115], 0\n\t" \
  "buffer_load_dwordx4 %[l16], %[rs], %[gk] offen sc1 lds\n\t" \
  "v_exp_f32 v214, v214\n\t" \
  "v_cvt_pk_bf16_f32 v226, v212, v213\n\t" \
  "v_exp_f32 v215, v215\n\t" \
  "v_exp_f32 v216, v216\n\t" \
  "v_cvt_pk_bf16_f32 v227, v214, v215\n\t" \
  "v_exp_f32 v217, v217\n\t" \
  "v_exp_f32 v218, v218\n\t" \
  "v_cvt_pk_bf16_f32 v228, v216, v217\n\t" \
  "v_exp_f32 v219, v219\n\t" \
  "v_mfma_f32_32x32x16_bf16 v[96:111], v[236:239], v[224:227], v[96:111]\n\t" \
  "s_waitcnt lgkmcnt(0)\n\t" \
  "v_exp_f32 v220, v220\n\t" \
  "v_cvt_pk_bf16_f32 v229, v218, v219\n\t" \
  "v_exp_f32 v221, v221\n\t" \
  "v_exp_f32 v222, v222\n\t" \
  "v_cvt_pk_bf16_f32 v230, v220, v221\n\t" \
  "v_exp_f32 v223, v223\n\t" \
  "v_exp_f32 v192, v192\n\t" \
  "v_cvt_pk_bf16_f32 v231, v222, v223\n\t" \
  "v_exp_f32 v193, v193\n\t" \
  "v_exp_f32 v194, v194\n\t" \
  "v_mfma_f32_32x32x16_bf16 v[96:111], v[240:243], v[228:231], v[96:111]\n\t" \
  "ds_read_b128 v[240:243], %[a1] offset:2112\n\t" \
  "v_cvt_pk_bf16_f32 v224, v192, v193\n\t" \
  "v_exp_f32 v195, v195\n\t" \
  "v_exp_f32 v196, v196\n\t" \
  "v_cvt_pk_bf16_f32 v225, v194, v195\n\t" \
  "v_exp_f32 v197, v197\n\t" \
  "v_mfma_f32_32x32x16_bf16 v[208:223], v[244:247], v[116:119], 0\n\t" \
  "ds_read_b128 v[244:247], %[ka] offset:3072\n\t" \
  "v_exp_f32 v198, v198\n\t" \
  "v_cvt_pk_bf16_f32 v226, v196, v197\n\t" \
  "v_exp_f32 v199, v199\n\t" \
  "v_exp_f32 v200, v200\n\t" \
  "v_cvt_pk_bf16_f32 v227, v198, v199\n\t" \
  "v_exp_f32 v201, v201\n\t" \
  "v_exp_f32 v202, v202\n\t" \
  "v_cvt_pk_bf16_f32 v228, v200, v201\n\t" \
  "v_exp_f32 v203, v203\n\t" \
  "v_mfma_f32_32x32x16_bf16 v[80:95], v[248:251], v[224:227], v[80:95]\n\t" \
  "ds_read_b128 v[236:239], %[a0] offset:2112\n\t" \
  "v_exp_f32 v204, v204\n\t" \
  "v_cvt_pk_bf16_f32 v229, v202, v203\n\t" \
  "v_exp_f32 v205, v205\n\t" \
  "v_exp_f32 v206, v206\n\t" \
  "v_cvt_pk_bf16_f32 v230, v204, v205\n\t" \
  "v_exp_f32 v207, v207\n\t" \
  "v_exp_f32 v208, v208\n\t" \
  "v_cvt_pk_bf16_f32 v231, v206, v207\n\t" \
  "v_exp_f32 v209, v209\n\t" \
  "v_exp_f32 v210, v210\n\t" \
  "s_add_u32 m0, m0, 4096\n\t" \
  "s_add_u32 %[gk], %[gk], 4096\n\t" \
  "v_mfma_f32_32x32x16_bf16 v[80:95], v[252:255], v[228:231], v[80:95]\n\t" \
  "buffer_load_dwordx4 %[l16], %[rs], %[gk] offen sc1 lds\n\t" \
  "v_cvt_pk_bf16_f32 v224, v208, v209\n\t" \
  "v_exp_f32 v211, v211\n\t" \
  "v_exp_f32 v212, v212\n\t" \
  "v_cvt_pk_bf16_f32 v225, v210, v211\n\t" \
  "v_exp_f32 v213, v213\n\t" \
  "s_add_u32 m0, m0, 4096\n\t" \
  "s_add_u32 %[gk], %[gk], 4096\n\t" \
  "v_mfma_f32_32x32x16_bf16 v[192:207], v[232:235], v[112:115], 0\n\t" \
  "buffer_load_dwordx4 %[l16], %[rs], %[gk] offen sc1 lds\n\t" \
  "v_exp_f32 v214, v214\n\t" \
  "v_cvt_pk_bf16_f32 v226, v212, v213\n\t" \
  "v_exp_f32 v215, v215\n\t" \
  "v_exp_f32 v216, v216\n\t" \
  "v_cvt_pk_bf16_f32 v227, v214, v215\n\t" \
  "v_exp_f32 v217, v217\n\t" \
  "v_exp_f32 v218, v218\n\t" \
  "v_cvt_pk_bf16_f32 v228, v216, v217\n\t" \
  "v_exp_f32 v219, v219\n\t" \
  "v_mfma_f32_32x32x16_bf16 v[96:111], v[248:251], v[224:227], v[96:111]\n\t" \
  "s_waitcnt lgkmcnt(0)\n\t" \
  "v_exp_f32 v220, v220\n\t" \
  "v_cvt_pk_bf16_f32 v229, v218, v219\n\t" \
  "v_exp_f32 v221, v221\n\t" \
  "v_exp_f32 v222, v222\n\t" \
  "v_cvt_pk_bf16_f32 v230, v220, v221\n\t" \
  "v_exp_f32 v223, v223\n\t" \
  "v_exp_f32 v192, v192\n\t" \
  "v_cvt_pk_bf16_f32 v231, v222, v223\n\t" \
  "v_exp_f32 v193, v193\n\t" \
  "v_exp_f32 v194, v194\n\t" \
  "v_mfma_f32_32x32x16_bf16 v[96:111], v[252:255], v[228:231], v[96:111]\n\t" \
  "ds_read_b128 v[252:255], %[a1] offset:3168\n\t" \
  "v_cvt_pk_bf16_f32 v224, v192, v193\n\t" \
  "v_exp_f32 v195, v195\n\t" \
  "v_exp_f32 v196, v196\n\t" \
  "v_cvt_pk_bf16_f32 v225, v194, v195\n\t" \
  "v_exp_f32 v197, v197\n\t" \
  "v_mfma_f32_32x32x16_bf16 v[208:223], v[232:235], v[116:119], 0\n\t" \
  "ds_read_b128 v[232:235], %[ka] offset:4096\n\t" \
  "v_exp_f32 v198, v198\n\t" \
  "v_cvt_pk_bf16_f32 v226, v196, v197\n\t" \
  "v_exp_f32 v199, v199\n\t" \
  "v_exp_f32 v200, v200\n\t" \
  "v_cvt_pk_bf16_f32 v227, v198, v199\n\t" \
  "v_exp_f32 v201, v201\n\t" \
  "v_exp_f32 v202, v202\n\t" \
  "v_cvt_pk_bf16_f32 v228, v200, v201\n\t" \
  "v_exp_f32 v203, v203\n\t" \
  "v_mfma_f32_32x32x16_bf16 v[80:95], v[236:239], v[224:227], v[80:95]\n\t" \
  "ds_read_b128 v[248:251], %[a0] offset:3168\n\t" \
  "v_exp_f32 v204, v204\n\t" \
  "v_cvt_pk_bf16_f32 v229, v202, v203\n\t" \
  "v_exp_f32 v205, v205\n\t" \
  "v_exp_f32 v206, v206\n\t" \
  "v_cvt_pk_bf16_f32 v230, v204, v205\n\t" \
  "v_exp_f32 v207, v207\n\t" \
  "v_exp_f32 v208, v208\n\t" \
  "v_cvt_pk_bf16_f32 v231, v206, v207\n\t" \
  "v_exp_f32 v209, v209\n\t" \
  "v_exp_f32 v210, v210\n\t" \
  "s_add_u32 m0, m0, 4096\n\t" \
  "s_add_u32 %[gk], %[gk], 4096\n\t" \
  "v_mfma_f32_32x32x16_bf16 v[80:95], v[240:243], v[228:231], v[80:95]\n\t" \
  "buffer_load_dwordx4 %[l16], %[rs], %[gk] offen sc1 lds\n\t" \
  "v_cvt_pk_bf16_f32 v224, v208, v209\n\t" \
  "v_exp_f32 v211, v211\n\t" \
  "v_exp_f32 v212, v212\n\t" \
  "v_cvt_pk_bf16_f32 v225, v210, v211\n\t" \
  "v_exp_f32 v213, v213\n\t" \
  "s_add_u32 m0, m0, 4096\n\t" \
  "s_add_u32 %[gk], %[gk], 4096\n\t" \
  "v_mfma_f32_32x32x16_bf16 v[192:207], v[244:247], v[112:115], 0\n\t" \
  "buffer_load_dwordx4 %[l16], %[rs], %[gk] offen sc1 lds\n\t" \
  "v_exp_f32 v214, v214\n\t" \
  "v_cvt_pk_bf16_f32 v226, v212, v213\n\t" \
  "v_exp_f32 v215, v215\n\t" \
  "v_exp_f32 v216, v216\n\t" \
  "v_cvt_pk_bf16_f32 v227, v214, v215\n\t" \
  "v_exp_f32 v217, v217\n\t" \
  "v_exp_f32 v218, v218\n\t" \
  "v_cvt_pk_bf16_f32 v228, v216, v217\n\t" \
  "v_exp_f32 v219, v219\n\t" \
  "v_mfma_f32_32x32x16_bf16 v[96:111], v[236:239], v[224:227], v[96:111]\n\t" \
  "s_waitcnt lgkmcnt(0)\n\t" \
  "v_exp_f32 v220, v220\n\t" \
  "v_cvt_pk_bf16_f32 v229, v218, v219\n\t" \
  "v_exp_f32 v221, v221\n\t" \
  "v_exp_f32 v222, v222\n\t" \
  "v_cvt_pk_bf16_f32 v230, v220, v221\n\t" \
  "v_exp_f32 v223, v223\n\t" \
  "v_exp_f32 v192, v192\n\t" \
  "v_cvt_pk_bf16_f32 v231, v222, v223\n\t" \
  "v_exp_f32 v193, v193\n\t" \
  "v_exp_f32 v194, v194\n\t" \
  "v_mfma_f32_32x32x16_bf16 v[96:111], v[240:243], v[228:231], v[96:111]\n\t" \
  "ds_read_b128 v[240:243], %[a1] offset:4224\n\t" \
  "v_cvt_pk_bf16_f32 v224, v192, v193\n\t" \
  "v_exp_f32 v195, v195\n\t" \
  "v_exp_f32 v196, v196\n\t" \
  "v_cvt_pk_bf16_f32 v225, v194, v195\n\t" \
  "v_exp_f32 v197, v197\n\t" \
  "v_mfma_f32_32x32x16_bf16 v[208:223], v[244:247], v[116:119], 0\n\t" \
  "ds_read_b128 v[244:247], %[ka] offset:5120\n\t" \
  "v_exp_f32 v198, v198\n\t" \
  "v_cvt_pk_bf16_f32 v226, v196, v197\n\t" \
  "v_exp_f32 v199, v199\n\t" \
  "v_exp_f32 v200, v200\n\t" \
  "v_cvt_pk_bf16_f32 v227, v198, v199\n\t" \
  "v_exp_f32 v201, v201\n\t" \
  "v_exp_f32 v202, v202\n\t" \
  "v_cvt_pk_bf16_f32 v228, v200, v201\n\t" \
  "v_exp_f32 v203, v203\n\t" \
  "v_mfma_f32_32x32x16_bf16 v[80:95], v[248:251], v[224:227], v[80:95]\n\t" \
  "ds_read_b128 v[236:239], %[a0] offset:4224\n\t" \
  "v_exp_f32 v204, v204\n\t" \
  "v_cvt_pk_bf16_f32 v229, v202, v203\n\t" \
  "v_exp_f32 v205, v205\n\t" \
  "v_exp_f32 v206, v206\n\t" \
  "v_cvt_pk_bf16_f32 v230, v204, v205\n\t" \
  "v_exp_f32 v207, v207\n\t" \
  "v_exp_f32 v208, v208\n\t" \
  "v_cvt_pk_bf16_f32 v231, v206, v207\n\t" \
  "v_exp_f32 v209, v209\n\t" \
  "v_exp_f32 v210, v210\n\t" \
  "s_add_u32 m0, m0, 4096\n\t" \
  "s_add_u32 %[gk], %[gk], 4096\n\t" \
  "v_mfma_f32_32x32x16_bf16 v[80:95], v[252:255], v[228:231], v[80:95]\n\t" \
  "buffer_load_dwordx4 %[l16], %[rs], %[gk] offen sc1 lds\n\t" \
  "v_cvt_pk_bf16_f32 v224, v208, v209\n\t" \
  "v_exp_f32 v211, v211\n\t" \
  "v_exp_f32 v212, v212\n\t" \
  "v_cvt_pk_bf16_f32 v225, v210, v211\n\t" \
  "v_exp_f32 v213, v213\n\t" \
  "s_add_u32 m0, m0, 4096\n\t" \
  "s_add_u32 %[gk], %[gk], 4096\n\t" \
  "v_mfma_f32_32x32x16_bf16 v[192:207], v[232:235], v[112:115], 0\n\t" \
  "buffer_load_dwordx4 %[l16], %[rs], %[gk] offen sc1 lds\n\t" \
  "v_exp_f32 v214, v214\n\t" \
  "v_cvt_pk_bf16_f32 v226, v212, v213\n\t" \
  "v_exp_f32 v215, v215\n\t" \
  "v_exp_f32 v216, v216\n\t" \
  "v_cvt_pk_bf16_f32 v227, v214, v215\n\t" \
  "v_exp_f32 v217, v217\n\t" \
  "v_exp_f32 v218, v218\n\t" \
  "v_cvt_pk_bf16_f32 v228, v216, v217\n\t" \
  "v_exp_f32 v219, v219\n\t" \
  "v_mfma_f32_32x32x16_bf16 v[96:111], v[248:251], v[224:227], v[96:111]\n\t" \
  "s_waitcnt lgkmcnt(0)\n\t" \
  "v_exp_f32 v220, v220\n\t" \
  "v_cvt_pk_bf16_f32 v229, v218, v219\n\t" \
  "v_exp_f32 v221, v221\n\t" \
  "v_exp_f32 v222, v222\n\t" \
  "v_cvt_pk_bf16_f32 v230, v220, v221\n\t" \
  "v_exp_f32 v223, v223\n\t" \
  "v_exp_f32 v192, v192\n\t" \
  "v_cvt_pk_bf16_f32 v231, v222, v223\n\t" \
  "v_exp_f32 v193, v193\n\t" \
  "v_exp_f32 v194, v194\n\t" \
  "v_mfma_f32_32x32x16_bf16 v[96:111], v[252:255], v[228:231], v[96:111]\n\t" \
  "ds_read_b128 v[252:255], %[a1] offset:5280\n\t" \
  "v_cvt_pk_bf16_f32 v224, v192, v193\n\t" \
  "v_exp_f32 v195, v195\n\t" \
  "v_exp_f32 v196, v196\n\t" \
  "v_cvt_pk_bf16_f32 v225, v194, v195\n\t" \
  "v_exp_f32 v197, v197\n\t" \
  "v_mfma_f32_32x32x16_bf16 v[208:223], v[232:235], v[116:119], 0\n\t" \
  "ds_read_b128 v[232:235], %[ka] offset:6144\n\t" \
  "v_exp_f32 v198, v198\n\t" \
  "v_cvt_pk_bf16_f32 v226, v196, v197\n\t" \
  "v_exp_f32 v199, v199\n\t" \
  "v_exp_f32 v200, v200\n\t" \
  "v_cvt_pk_bf16_f32 v227, v198, v199\n\t" \
  "v_exp_f32 v201, v201\n\t" \
  "v_exp_f32 v202, v202\n\t" \
  "v_cvt_pk_bf16_f32 v228, v200, v201\n\t" \
  "v_exp_f32 v203, v203\n\t" \
  "v_mfma_f32_32x32x16_bf16 v[80:95], v[236:239], v[224:227], v[80:95]\n\t" \
  "ds_read_b128 v[248:251], %[a0] offset:5280\n\t" \
  "v_exp_f32 v204, v204\n\t" \
  "v_cvt_pk_bf16_f32 v229, v202, v203\n\t" \
  "v_exp_f32 v205, v205\n\t" \
  "v_exp_f32 v206, v206\n\t" \
  "v_cvt_pk_bf16_f32 v230, v204, v205\n\t" \
  "v_exp_f32 v207, v207\n\t" \
  "v_exp_f32 v208, v208\n\t" \
  "v_cvt_pk_bf16_f32 v231, v206, v207\n\t" \
  "v_exp_f32 v209, v209\n\t" \
  "v_exp_f32 v210, v210\n\t" \
  "s_add_u32 m0, m0, 4096\n\t" \
  "s_add_u32 %[gk], %[gk], 4096\n\t" \
  "v_mfma_f32_32x32x16_bf16 v[80:95], v[240:243], v[228:231], v[80:95]\n\t" \
  "buffer_load_dwordx4 %[l16], %[rs], %[gk] offen sc1 lds\n\t" \
  "v_cvt_pk_bf16_f32 v224, v208, v209\n\t" \
  "v_exp_f32 v211, v211\n\t" \
  "v_exp_f32 v212, v212\n\t" \
  "v_cvt_pk_bf16_f32 v225, v210, v211\n\t" \
  "v_exp_f32 v213, v213\n\t" \
  "s_add_u32 m0, m0, 4096\n\t" \
  "s_add_u32 %[gk], %[gk], 4096\n\t" \
  "v_mfma_f32_32x32x16_bf16 v[192:207], v[244:247], v[112:115], 0\n\t" \
  "buffer_load_dwordx4 %[l16], %[rs], %[gk] offen sc1 lds\n\t" \
  "v_exp_f32 v214, v214\n\t" \
  "v_cvt_pk_bf16_f32 v226, v212, v213\n\t" \
  "v_exp_f32 v215, v215\n\t" \
  "v_exp_f32 v216, v216\n\t" \
  "v_cvt_pk_bf16_f32 v227, v214, v215\n\t" \
  "v_exp_f32 v217, v217\n\t" \
  "v_exp_f32 v218, v218\n\t" \
  "v_cvt_pk_bf16_f32 v228, v216, v217\n\t" \
  "v_exp_f32 v219, v219\n\t" \
  "v_mfma_f32_32x32x16_bf16 v[96:111], v[236:239], v[224:227], v[96:111]\n\t" \
  "s_waitcnt lgkmcnt(0)\n\t" \
  "v_exp_f32 v220, v220\n\t" \
  "v_cvt_pk_bf16_f32 v229, v218, v219\n\t" \
  "v_exp_f32 v221, v221\n\t" \
  "v_exp_f32 v222, v222\n\t" \
  "v_cvt_pk_bf16_f32 v230, v220, v221\n\t" \
  "v_exp_f32 v223, v223\n\t" \
  "v_exp_f32 v192, v192\n\t" \
  "v_cvt_pk_bf16_f32 v231, v222, v223\n\t" \
  "v_exp_f32 v193, v193\n\t" \
  "v_exp_f32 v194, v194\n\t" \
  "v_mfma_f32_32x32x16_bf16 v[96:111], v[240:243], v[228:231], v[96:111]\n\t" \
  "ds_read_b128 v[240:243], %[a1] offset:6336\n\t" \
  "v_cvt_pk_bf16_f32 v224, v192, v193\n\t" \
  "v_exp_f32 v195, v195\n\t" \
  "v_exp_f32 v196, v196\n\t" \
  "v_cvt_pk_bf16_f32 v225, v194, v195\n\t" \
  "v_exp_f32 v197, v197\n\t" \
  "v_mfma_f32_32x32x16_bf16 v[208:223], v[244:247], v[116:119], 0\n\t" \
  "ds_read_b128 v[244:247], %[ka] offset:7168\n\t" \
  "v_exp_f32 v198, v198\n\t" \
  "v_cvt_pk_bf16_f32 v226, v196, v197\n\t" \
  "v_exp_f32 v199, v199\n\t" \
  "v_exp_f32 v200, v200\n\t" \
  "v_cvt_pk_bf16_f32 v227, v198, v199\n\t" \
  "v_exp_f32 v201, v201\n\t" \
  "v_exp_f32 v202, v202\n\t" \
  "v_cvt_pk_bf16_f32 v228, v200, v201\n\t" \
  "v_exp_f32 v203, v203\n\t" \
  "v_mfma_f32_32x32x16_bf16 v[80:95], v[248:251], v[224:227], v[80:95]\n\t" \
  "ds_read_b128 v[236:239], %[a0] offset:6336\n\t" \
  "v_exp_f32 v204, v204\n\t" \
  "v_cvt_pk_bf16_f32 v229, v202, v203\n\t" \
  "v_exp_f32 v205, v205\n\t" \
  "v_exp_f32 v206, v206\n\t" \
  "v_cvt_pk_bf16_f32 v230, v204, v205\n\t" \
  "v_exp_f32 v207, v207\n\t" \
  "v_exp_f32 v208, v208\n\t" \
  "v_cvt_pk_bf16_f32 v231, v206, v207\n\t" \
  "v_exp_f32 v209, v209\n\t" \
  "v_exp_f32 v210, v210\n\t" \
  "s_add_u32 m0, m0, 4096\n\t" \
  "s_add_u32 %[gk], %[gk], 4096\n\t" \
  "v_mfma_f32_32x32x16_bf16 v[80:95], v[252:255], v[228:231], v[80:95]\n\t" \
  "buffer_load_dwordx4 %[l16], %[rs], %[gk] offen sc1 lds\n\t" \
  "v_cvt_pk_bf16_f32 v224, v208, v209\n\t" \
  "v_exp_f32 v211, v211\n\t" \
  "v_exp_f32 v212, v212\n\t" \
  "v_cvt_pk_bf16_f32 v225, v210, v211\n\t" \
  "v_exp_f32 v213, v213\n\t" \
  "s_mov_b32 m0, %[mv]\n\t" \
  "s_nop 0\n\t" \
  "v_mfma_f32_32x32x16_bf16 v[192:207], v[232:235], v[112:115], 0\n\t" \
  "buffer_load_dwordx4 %[l16], %[rs], %[gv] offen sc1 lds\n\t" \
  "v_exp_f32 v214, v214\n\t" \
  "v_cvt_pk_bf16_f32 v226, v212, v213\n\t" \
  "v_exp_f32 v215, v215\n\t" \
  "v_exp_f32 v216, v216\n\t" \
  "v_cvt_pk_bf16_f32 v227, v214, v215\n\t" \
  "v_exp_f32 v217, v217\n\t" \
  "v_exp_f32 v218, v218\n\t" \
  "v_cvt_pk_bf16_f32 v228, v216, v217\n\t" \
  "v_exp_f32 v219, v219\n\t" \
  "v_mfma_f32_32x32x16_bf16 v[96:111], v[248:251], v[224:227], v[96:111]\n\t" \
  "s_waitcnt lgkmcnt(0)\n\t" \
  "v_exp_f32 v220, v220\n\t" \
  "v_cvt_pk_bf16_f32 v229, v218, v219\n\t" \
  "v_exp_f32 v221, v221\n\t" \
  "v_exp_f32 v222, v222\n\t" \
  "v_cvt_pk_bf16_f32 v230, v220, v221\n\t" \
  "v_exp_f32 v223, v223\n\t" \
  "v_exp_f32 v192, v192\n\t" \
  "v_cvt_pk_bf16_f32 v231, v222, v223\n\t" \
  "v_exp_f32 v193, v193\n\t" \
  "v_exp_f32 v194, v194\n\t" \
  "v_mfma_f32_32x32x16_bf16 v[96:111], v[252:255], v[228:231], v[96:111]\n\t" \
  "ds_read_b128 v[252:255], %[a1] offset:7392\n\t" \
  "v_cvt_pk_bf16_f32 v224, v192, v193\n\t" \
  "v_exp_f32 v195, v195\n\t" \
  "v_exp_f32 v196, v196\n\t" \
  "v_cvt_pk_bf16_f32 v225, v194, v195\n\t" \
  "v_exp_f32 v197, v197\n\t" \
  "v_mfma_f32_32x32x16_bf16 v[208:223], v[232:235], v[116:119], 0\n\t" \
  "ds_read_b128 v[232:235], %[ka] offset:8192\n\t" \
  "v_exp_f32 v198, v198\n\t" \
  "v_cvt_pk_bf16_f32 v226, v196, v197\n\t" \
  "v_exp_f32 v199, v199\n\t" \
  "v_exp_f32 v200, v200\n\t" \
  "v_cvt_pk_bf16_f32 v227, v198, v199\n\t" \
  "v_exp_f32 v201, v201\n\t" \
  "v_exp_f32 v202, v202\n\t" \
  "v_cvt_pk_bf16_f32 v228, v200, v201\n\t" \
  "v_exp_f32 v203, v203\n\t" \
  "v_mfma_f32_32x32x16_bf16 v[80:95], v[236:239], v[224:227], v[80:95]\n\t" \
  "ds_read_b128 v[248:251], %[a0] offset:7392\n\t" \
  "v_exp_f32 v204, v204\n\t" \
  "v_cvt_pk_bf16_f32 v229, v202, v203\n\t" \
  "v_exp_f32 v205, v205\n\t" \
  "v_exp_f32 v206, v206\n\t" \
  "v_cvt_pk_bf16_f32 v230, v204, v205\n\t" \
  "v_exp_f32 v207, v207\n\t" \
  "v_exp_f32 v208, v208\n\t" \
  "v_cvt_pk_bf16_f32 v231, v206, v207\n\t" \
  "v_exp_f32 v209, v209\n\t" \
  "v_exp_f32 v210, v210\n\t" \
  "s_add_u32 m0, m0, 4224\n\t" \
  "s_add_u32 %[gv], %[gv], 4096\n\t" \
  "v_mfma_f32_32x32x16_bf16 v[80:95], v[240:243], v[228:231], v[80:95]\n\t" \
  "buffer_load_dwordx4 %[l16], %[rs], %[gv] offen sc1 lds\n\t" \
  "v_cvt_pk_bf16_f32 v224, v208, v209\n\t" \
  "v_exp_f32 v211, v211\n\t" \
  "v_exp_f32 v212, v212\n\t" \
  "v_cvt_pk_bf16_f32 v225, v210, v211\n\t" \
  "v_exp_f32 v213, v213\n\t" \
  "s_add_u32 m0, m0, 4224\n\t" \
  "s_add_u32 %[gv], %[gv], 4096\n\t" \
  "v_mfma_f32_32x32x16_bf16 v[192:207], v[244:247], v[112:115], 0\n\t" \
  "buffer_load_dwordx4 %[l16], %[rs], %[gv] offen sc1 lds\n\t" \
  "v_exp_f32 v214, v214\n\t" \
  "v_cvt_pk_bf16_f32 v226, v212, v213\n\t" \
  "v_exp_f32 v215, v215\n\t" \
  "v_exp_f32 v216, v216\n\t" \
  "v_cvt_pk_bf16_f32 v227, v214, v215\n\t" \
  "v_exp_f32 v217, v217\n\t" \
  "v_exp_f32 v218, v218\n\t" \
  "v_cvt_pk_bf16_f32 v228, v216, v217\n\t" \
  "v_exp_f32 v219, v219\n\t" \
  "v_mfma_f32_32x32x16_bf16 v[96:111], v[236:239], v[224:227], v[96:111]\n\t" \
  "s_waitcnt lgkmcnt(0)\n\t" \
  "v_exp_f32 v220, v220\n\t" \
  "v_cvt_pk_bf16_f32 v229, v218, v219\n\t" \
  "v_exp_f32 v221, v221\n\t" \
  "v_exp_f32 v222, v222\n\t" \
  "v_cvt_pk_bf16_f32 v230, v220, v221\n\t" \
  "v_exp_f32 v223, v223\n\t" \
  "v_exp_f32 v192, v192\n\t" \
  "v_cvt_pk_bf16_f32 v231, v222, v223\n\t" \
  "v_exp_f32 v193, v193\n\t" \
  "v_exp_f32 v194, v194\n\t" \
  "v_mfma_f32_32x32x16_bf16 v[96:111], v[240:243], v[228:231], v[96:111]\n\t" \
  "ds_read_b128 v[240:243], %[a1] offset:8448\n\t" \
  "v_cvt_pk_bf16_f32 v224, v192, v193\n\t" \
  "v_exp_f32 v195, v195\n\t" \
  "v_exp_f32 v196, v196\n\t" \
  "v_cvt_pk_bf16_f32 v225, v194, v195\n\t" \
  "v_exp_f32 v197, v197\n\t" \
  "v_mfma_f32_32x32x16_bf16 v[208:223], v[244:247], v[116:119], 0\n\t" \
  "ds_read_b128 v[244:247], %[ka] offset:9216\n\t" \
  "v_exp_f32 v198, v198\n\t" \
  "v_cvt_pk_bf16_f32 v226, v196, v197\n\t" \
  "v_exp_f32 v199, v199\n\t" \
  "v_exp_f32 v200, v200\n\t" \
  "v_cvt_pk_bf16_f32 v227, v198, v199\n\t" \
  "v_exp_f32 v201, v201\n\t" \
  "v_exp_f32 v202, v202\n\t" \
  "v_cvt_pk_bf16_f32 v228, v200, v201\n\t" \
  "v_exp_f32 v203, v203\n\t" \
  "v_mfma_f32_32x32x16_bf16 v[80:95], v[248:251], v[224:227], v[80:95]\n\t" \
  "ds_read_b128 v[236:239], %[a0] offset:8448\n\t" \
  "v_exp_f32 v204, v204\n\t" \
  "v_cvt_pk_bf16_f32 v229, v202, v203\n\t" \
  "v_exp_f32 v205, v205\n\t" \
  "v_exp_f32 v206, v206\n\t" \
  "v_cvt_pk_bf16_f32 v230, v204, v205\n\t" \
  "v_exp_f32 v207, v207\n\t" \
  "v_exp_f32 v208, v208\n\t" \
  "v_cvt_pk_bf16_f32 v231, v206, v207\n\t" \
  "v_exp_f32 v209, v209\n\t" \
  "v_exp_f32 v210, v210\n\t" \
  "s_add_u32 m0, m0, 4224\n\t" \
  "s_add_u32 %[gv], %[gv], 4096\n\t" \
  "v_mfma_f32_32x32x16_bf16 v[80:95], v[252:255], v[228:231], v[80:95]\n\t" \
  "buffer_load_dwordx4 %[l16], %[rs], %[gv] offen sc1 lds\n\t" \
  "v_cvt_pk_bf16_f32 v224, v208, v209\n\t" \
  "v_exp_f32 v211, v211\n\t" \
  "v_exp_f32 v212, v212\n\t" \
  "v_cvt_pk_bf16_f32 v225, v210, v211\n\t" \
  "v_exp_f32 v213, v213\n\t" \
  "s_add_u32 m0, m0, 4224\n\t" \
  "s_add_u32 %[gv], %[gv], 4096\n\t" \
  "v_mfma_f32_32x32x16_bf16 v[192:207], v[232:235], v[112:115], 0\n\t" \
  "buffer_load_dwordx4 %[l16], %[rs], %[gv] offen sc1 lds\n\t" \
  "v_exp_f32 v214, v214\n\t" \
  "v_cvt_pk_bf16_f32 v226, v212, v213\n\t" \
  "v_exp_f32 v215, v215\n\t" \
  "v_exp_f32 v216, v216\n\t" \
  "v_cvt_pk_bf16_f32 v227, v214, v215\n\t" \
  "v_exp_f32 v217, v217\n\t" \
  "v_exp_f32 v218, v218\n\t" \
  "v_cvt_pk_bf16_f32 v228, v216, v217\n\t" \
  "v_exp_f32 v219, v219\n\t" \
  "v_mfma_f32_32x32x16_bf16 v[96:111], v[248:251], v[224:227], v[96:111]\n\t" \
  "s_waitcnt lgkmcnt(0)\n\t" \
  "v_exp_f32 v220, v220\n\t" \
  "v_cvt_pk_bf16_f32 v229, v218, v219\n\t" \
  "v_exp_f32 v221, v221\n\t" \
  "v_exp_f32 v222, v222\n\t" \
  "v_cvt_pk_bf16_f32 v230, v220, v221\n\t" \
  "v_exp_f32 v223, v223\n\t" \
  "v_add_u32 %[ka], 8192, %[ka]\n\t" \
  "v_add_u32 %[a0], 8448, %[a0]\n\t" \
  "v_add_u32 %[a1], 8448, %[a1]\n\t" \
  "v_exp_f32 v192, v192\n\t" \
  "v_cvt_pk_bf16_f32 v231, v222, v223\n\t" \
  "v_exp_f32 v193, v193\n\t" \
  "v_exp_f32 v194, v194\n\t" \
  "v_mfma_f32_32x32x16_bf16 v[96:111], v[252:255], v[228:231], v[96:111]\n\t" \
  "ds_read_b128 v[252:255], %[a1] offset:1056\n\t" \
  "v_cvt_pk_bf16_f32 v224, v192, v193\n\t" \
  "v_exp_f32 v195, v195\n\t" \
  "v_exp_f32 v196, v196\n\t" \
  "v_cvt_pk_bf16_f32 v225, v194, v195\n\t" \
  "v_exp_f32 v197, v197\n\t" \
  "v_mfma_f32_32x32x16_bf16 v[208:223], v[232:235], v[116:119], 0\n\t" \
  "ds_read_b128 v[232:235], %[ka] offset:2048\n\t" \
  "v_exp_f32 v198, v198\n\t" \
  "v_cvt_pk_bf16_f32 v226, v196, v197\n\t" \
  "v_exp_f32 v199, v199\n\t" \
  "v_exp_f32 v200, v200\n\t" \
  "v_cvt_pk_bf16_f32 v227, v198, v199\n\t" \
  "v_exp_f32 v201, v201\n\t" \
  "v_exp_f32 v202, v202\n\t" \
  "v_cvt_pk_bf16_f32 v228, v200, v201\n\t" \
  "v_exp_f32 v203, v203\n\t" \
  "v_mfma_f32_32x32x16_bf16 v[80:95], v[236:239], v[224:227], v[80:95]\n\t" \
  "ds_read_b128 v[248:251], %[a0] offset:1056\n\t" \
  "v_exp_f32 v204, v204\n\t" \
  "v_cvt_pk_bf16_f32 v229, v202, v203\n\t" \
  "v_exp_f32 v205, v205\n\t" \
  "v_exp_f32 v206, v206\n\t" \
  "v_cvt_pk_bf16_f32 v230, v204, v205\n\t" \
  "v_exp_f32 v207, v207\n\t" \
  "v_exp_f32 v208, v208\n\t" \
  "v_cvt_pk_bf16_f32 v231, v206, v207\n\t" \
  "v_exp_f32 v209, v209\n\t" \
  "v_exp_f32 v210, v210\n\t" \
  "s_add_u32 m0, m0, 4224\n\t" \
  "s_add_u32 %[gv], %[gv], 4096\n\t" \
  "v_mfma_f32_32x32x16_bf16 v[80:95], v[240:243], v[228:231], v[80:95]\n\t" \
  "buffer_load_dwordx4 %[l16], %[rs], %[gv] offen sc1 lds\n\t" \
  "v_cvt_pk_bf16_f32 v224, v208, v209\n\t" \
  "v_exp_f32 v211, v211\n\t" \
  "v_exp_f32 v212, v212\n\t" \
  "v_cvt_pk_bf16_f32 v225, v210, v211\n\t" \
  "v_exp_f32 v213, v213\n\t" \
  "s_add_u32 m0, m0, 4224\n\t" \
  "s_add_u32 %[gv], %[gv], 4096\n\t" \
  "v_mfma_f32_32x32x16_bf16 v[192:207], v[244:247], v[112:115], 0\n\t" \
  "buffer_load_dwordx4 %[l16], %[rs], %[gv] offen sc1 lds\n\t" \
  "v_exp_f32 v214, v214\n\t" \
  "v_cvt_pk_bf16_f32 v226, v212, v213\n\t" \
  "v_exp_f32 v215, v215\n\t" \
  "v_exp_f32 v216, v216\n\t" \
  "v_cvt_pk_bf16_f32 v227, v214, v215\n\t" \
  "v_exp_f32 v217, v217\n\t" \
  "v_exp_f32 v218, v218\n\t" \
  "v_cvt_pk_bf16_f32 v228, v216, v217\n\t" \
  "v_exp_f32 v219, v219\n\t" \
  "v_mfma_f32_32x32x16_bf16 v[96:111], v[236:239], v[224:227], v[96:111]\n\t" \
  "s_waitcnt lgkmcnt(0)\n\t" \
  "v_exp_f32 v220, v220\n\t" \
  "v_cvt_pk_bf16_f32 v229, v218, v219\n\t" \
  "v_exp_f32 v221, v221\n\t" \
  "v_exp_f32 v222, v222\n\t" \
  "v_cvt_pk_bf16_f32 v230, v220, v221\n\t" \
  "v_exp_f32 v223, v223\n\t" \
  "v_exp_f32 v192, v192\n\t" \
  "v_cvt_pk_bf16_f32 v231, v222, v223\n\t" \
  "v_exp_f32 v193, v193\n\t" \
  "v_exp_f32 v194, v194\n\t" \
  "v_mfma_f32_32x32x16_bf16 v[96:111], v[240:243], v[228:231], v[96:111]\n\t" \
  "ds_read_b128 v[240:243], %[a1] offset:2112\n\t" \
  "v_cvt_pk_bf16_f32 v224, v192, v193\n\t" \
  "v_exp_f32 v195, v195\n\t" \
  "v_exp_f32 v196, v196\n\t" \
  "v_cvt_pk_bf16_f32 v225, v194, v195\n\t" \
  "v_exp_f32 v197, v197\n\t" \
  "v_mfma_f32_32x32x16_bf16 v[208:223], v[244:247], v[116:119], 0\n\t" \
  "ds_read_b128 v[244:247], %[ka] offset:3072\n\t" \
  "v_exp_f32 v198, v198\n\t" \
  "v_cvt_pk_bf16_f32 v226, v196, v197\n\t" \
  "v_exp_f32 v199, v199\n\t" \
  "v_exp_f32 v200, v200\n\t" \
  "v_cvt_pk_bf16_f32 v227, v198, v199\n\t" \
  "v_exp_f32 v201, v201\n\t" \
  "v_exp_f32 v202, v202\n\t" \
  "v_cvt_pk_bf16_f32 v228, v200, v201\n\t" \
  "v_exp_f32 v203, v203\n\t" \
  "v_mfma_f32_32x32x16_bf16 v[80:95], v[248:251], v[224:227], v[80:95]\n\t" \
  "ds_read_b128 v[236:239], %[a0] offset:2112\n\t" \
  "v_exp_f32 v204, v204\n\t" \
  "v_cvt_pk_bf16_f32 v229, v202, v203\n\t" \
  "v_exp_f32 v205, v205\n\t" \
  "v_exp_f32 v206, v206\n\t" \
  "v_cvt_pk_bf16_f32 v230, v204, v205\n\t" \
  "v_exp_f32 v207, v207\n\t" \
  "v_exp_f32 v208, v208\n\t" \
  "v_cvt_pk_bf16_f32 v231, v206, v207\n\t" \
  "v_exp_f32 v209, v209\n\t" \
  "v_exp_f32 v210, v210\n\t" \
  "s_add_u32 m0, m0, 4224\n\t" \
  "s_add_u32 %[gv], %[gv], 4096\n\t" \
  "v_mfma_f32_32x32x16_bf16 v[80:95], v[252:255], v[228:231], v[80:95]\n\t" \
  "buffer_load_dwordx4 %[l16], %[rs], %[gv] offen sc1 lds\n\t" \
  "v_cvt_pk_bf16_f32 v224, v208, v209\n\t" \
  "v_exp_f32 v211, v211\n\t" \
  "v_exp_f32 v212, v212\n\t" \
  "v_cvt_pk_bf16_f32 v225, v210, v211\n\t" \
  "v_exp_f32 v213, v213\n\t" \
  "s_add_u32 m0, m0, 4224\n\t" \
  "s_add_u32 %[gv], %[gv], 4096\n\t" \
  "v_mfma_f32_32x32x16_bf16 v[192:207], v[232:235], v[112:115], 0\n\t" \
  "buffer_load_dwordx4 %[l16], %[rs], %[gv] offen sc1 lds\n\t" \
  "v_exp_f32 v214, v214\n\t" \
  "v_cvt_pk_bf16_f32 v226, v212, v213\n\t" \
  "v_exp_f32 v215, v215\n\t" \
  "v_exp_f32 v216, v216\n\t" \
  "v_cvt_pk_bf16_f32 v227, v214, v215\n\t" \
  "v_exp_f32 v217, v217\n\t" \
  "v_exp_f32 v218, v218\n\t" \
  "v_cvt_pk_bf16_f32 v228, v216, v217\n\t" \
  "v_exp_f32 v219, v219\n\t" \
  "v_mfma_f32_32x32x16_bf16 v[96:111], v[248:251], v[224:227], v[96:111]\n\t" \
  "s_waitcnt lgkmcnt(0)\n\t" \
  "v_exp_f32 v220, v220\n\t" \
  "v_cvt_pk_bf16_f32 v229, v218, v219\n\t" \
  "v_exp_f32 v221, v221\n\t" \
  "v_exp_f32 v222, v222\n\t" \
  "v_cvt_pk_bf16_f32 v230, v220, v221\n\t" \
  "v_exp_f32 v223, v223\n\t" \
  "v_exp_f32 v192, v192\n\t" \
  "v_cvt_pk_bf16_f32 v231, v222, v223\n\t" \
  "v_exp_f32 v193, v193\n\t" \
  "v_exp_f32 v194, v194\n\t" \
  "v_mfma_f32_32x32x16_bf16 v[96:111], v[252:255], v[228:231], v[96:111]\n\t" \
  "ds_read_b128 v[252:255], %[a1] offset:3168\n\t" \
  "v_cvt_pk_bf16_f32 v224, v192, v193\n\t" \
  "v_exp_f32 v195, v195\n\t" \
  "v_exp_f32 v196, v196\n\t" \
  "v_cvt_pk_bf16_f32 v225, v194, v195\n\t" \
  "v_exp_f32 v197, v197\n\t" \
  "v_mfma_f32_32x32x16_bf16 v[208:223], v[232:235], v[116:119], 0\n\t" \
  "ds_read_b128 v[232:235], %[ka] offset:4096\n\t" \
  "v_exp_f32 v198, v198\n\t" \
  "v_cvt_pk_bf16_f32 v226, v196, v197\n\t" \
  "v_exp_f32 v199, v199\n\t" \
  "v_exp_f32 v200, v200\n\t" \
  "v_cvt_pk_bf16_f32 v227, v198, v199\n\t" \
  "v_exp_f32 v201, v201\n\t" \
  "v_exp_f32 v202, v202\n\t" \
  "v_cvt_pk_bf16_f32 v228, v200, v201\n\t" \
  "v_exp_f32 v203, v203\n\t" \
  "v_mfma_f32_32x32x16_bf16 v[80:95], v[236:239], v[224:227], v[80:95]\n\t" \
  "ds_read_b128 v[248:251], %[a0] offset:3168\n\t" \
  "v_exp_f32 v204, v204\n\t" \
  "v_cvt_pk_bf16_f32 v229, v202, v203\n\t" \
  "v_exp_f32 v205, v205\n\t" \
  "v_exp_f32 v206, v206\n\t" \
  "v_cvt_pk_bf16_f32 v230, v204, v205\n\t" \
  "v_exp_f32 v207, v207\n\t" \
  "v_exp_f32 v208, v208\n\t" \
  "v_cvt_pk_bf16_f32 v231, v206, v207\n\t" \
  "v_exp_f32 v209, v209\n\t" \
  "v_exp_f32 v210, v210\n\t" \
  "s_add_u32 m0, m0, 4224\n\t" \
  "s_add_u32 %[gv], %[gv], 4096\n\t" \
  "v_mfma_f32_32x32x16_bf16 v[80:95], v[240:243], v[228:231], v[80:95]\n\t" \
  "buffer_load_dwordx4 %[l16], %[rs], %[gv] offen sc1 lds\n\t" \
  "v_cvt_pk_bf16_f32 v224, v208, v209\n\t" \
  "v_exp_f32 v211, v211\n\t" \
  "v_exp_f32 v212, v212\n\t" \
  "v_cvt_pk_bf16_f32 v225, v210, v211\n\t" \
  "v_exp_f32 v213, v213\n\t" \
  "s_add_u32 m0, m0, 4224\n\t" \
  "s_add_u32 %[gv], %[gv], 4096\n\t" \
  "v_mfma_f32_32x32x16_bf16 v[192:207], v[244:247], v[112:115], 0\n\t" \
  "buffer_load_dwordx4 %[l16], %[rs], %[gv] offen sc1 lds\n\t" \
  "v_exp_f32 v214, v214\n\t" \
  "v_cvt_pk_bf16_f32 v226, v212, v213\n\t" \
  "v_exp_f32 v215, v215\n\t" \
  "v_exp_f32 v216, v216\n\t" \
  "v_cvt_pk_bf16_f32 v227, v214, v215\n\t" \
  "v_exp_f32 v217, v217\n\t" \
  "v_exp_f32 v218, v218\n\t" \
  "v_cvt_pk_bf16_f32 v228, v216, v217\n\t" \
  "v_exp_f32 v219, v219\n\t" \
  "v_mfma_f32_32x32x16_bf16 v[96:111], v[236:239], v[224:227], v[96:111]\n\t" \
  "s_waitcnt lgkmcnt(0)\n\t" \
  "v_exp_f32 v220, v220\n\t" \
  "v_cvt_pk_bf16_f32 v229, v218, v219\n\t" \
  "v_exp_f32 v221, v221\n\t" \
  "v_exp_f32 v222, v222\n\t" \
  "v_cvt_pk_bf16_f32 v230, v220, v221\n\t" \
  "v_exp_f32 v223, v223\n\t" \
  "v_exp_f32 v192, v192\n\t" \
  "v_cvt_pk_bf16_f32 v231, v222, v223\n\t" \
  "v_exp_f32 v193, v193\n\t" \
  "v_exp_f32 v194, v194\n\t" \
  "v_mfma_f32_32x32x16_bf16 v[96:111], v[240:243], v[228:231], v[96:111]\n\t" \
  "ds_read_b128 v[240:243], %[a1] offset:4224\n\t" \
  "v_cvt_pk_bf16_f32 v224, v192, v193\n\t" \
  "v_exp_f32 v195, v195\n\t" \
  "v_exp_f32 v196, v196\n\t" \
  "v_cvt_pk_bf16_f32 v225, v194, v195\n\t" \
  "v_exp_f32 v197, v197\n\t" \
  "v_mfma_f32_32x32x16_bf16 v[208:223], v[244:247], v[116:119], 0\n\t" \
  "ds_read_b128 v[244:247], %[ka] offset:5120\n\t" \
  "v_exp_f32 v198, v198\n\t" \
  "v_cvt_pk_bf16_f32 v226, v196, v197\n\t" \
  "v_exp_f32 v199, v199\n\t" \
  "v_exp_f32 v200, v200\n\t" \
  "v_cvt_pk_bf16_f32 v227, v198, v199\n\t" \
  "v_exp_f32 v201, v201\n\t" \
  "v_exp_f32 v202, v202\n\t" \
  "v_cvt_pk_bf16_f32 v228, v200, v201\n\t" \
  "v_exp_f32 v203, v203\n\t" \
  "v_mfma_f32_32x32x16_bf16 v[80:95], v[248:251], v[224:227], v[80:95]\n\t" \
  "ds_read_b128 v[236:239], %[a0] offset:4224\n\t" \
  "v_exp_f32 v204, v204\n\t" \
  "v_cvt_pk_bf16_f32 v229, v202, v203\n\t" \
  "v_exp_f32 v205, v205\n\t" \
  "v_exp_f32 v206, v206\n\t" \
  "v_cvt_pk_bf16_f32 v230, v204, v205\n\t" \
  "v_exp_f32 v207, v207\n\t" \
  "v_exp_f32 v208, v208\n\t" \
  "v_cvt_pk_bf16_f32 v231, v206, v207\n\t" \
  "v_exp_f32 v209, v209\n\t" \
  "v_exp_f32 v210, v210\n\t" \
  "v_mfma_f32_32x32x16_bf16 v[80:95], v[252:255], v[228:231], v[80:95]\n\t" \
  "buffer_load_dwordx4 a[128:131], %[l16], %[rs], %[gwoa] offen sc1\n\t" \
  "v_cvt_pk_bf16_f32 v224, v208, v209\n\t" \
  "v_exp_f32 v211, v211\n\t" \
  "v_exp_f32 v212, v212\n\t" \
  "v_cvt_pk_bf16_f32 v225, v210, v211\n\t" \
  "v_exp_f32 v213, v213\n\t" \
  "v_mfma_f32_32x32x16_bf16 v[192:207], v[232:235], v[112:115], 0\n\t" \
  "buffer_load_dwordx4 a[132:135], %[l16], %[rs], %[gwob] offen sc1\n\t" \
  "v_exp_f32 v214, v214\n\t" \
  "v_cvt_pk_bf16_f32 v226, v212, v213\n\t" \
  "v_exp_f32 v215, v215\n\t" \
  "v_exp_f32 v216, v216\n\t" \
  "v_cvt_pk_bf16_f32 v227, v214, v215\n\t" \
  "v_exp_f32 v217, v217\n\t" \
  "v_exp_f32 v218, v218\n\t" \
  "v_cvt_pk_bf16_f32 v228, v216, v217\n\t" \
  "v_exp_f32 v219, v219\n\t" \
  "v_mfma_f32_32x32x16_bf16 v[96:111], v[248:251], v[224:227], v[96:111]\n\t" \
  "s_waitcnt lgkmcnt(0)\n\t" \
  "v_exp_f32 v220, v220\n\t" \
  "v_cvt_pk_bf16_f32 v229, v218, v219\n\t" \
  "v_exp_f32 v221, v221\n\t" \
  "v_exp_f32 v222, v222\n\t" \
  "v_cvt_pk_bf16_f32 v230, v220, v221\n\t" \
  "v_exp_f32 v223, v223\n\t" \
  "v_exp_f32 v192, v192\n\t" \
  "v_cvt_pk_bf16_f32 v231, v222, v223\n\t" \
  "v_exp_f32 v193, v193\n\t" \
  "v_exp_f32 v194, v194\n\t" \
  "v_mfma_f32_32x32x16_bf16 v[96:111], v[252:255], v[228:231], v[96:111]\n\t" \
  "ds_read_b128 v[252:255], %[a1] offset:5280\n\t" \
  "v_cvt_pk_bf16_f32 v224, v192, v193\n\t" \
  "v_exp_f32 v195, v195\n\t" \
  "v_exp_f32 v196, v196\n\t" \
  "v_cvt_pk_bf16_f32 v225, v194, v195\n\t" \
  "v_exp_f32 v197, v197\n\t" \
  "v_mfma_f32_32x32x16_bf16 v[208:223], v[232:235], v[116:119], 0\n\t" \
  "ds_read_b128 v[232:235], %[ka] offset:6144\n\t" \
  "v_exp_f32 v198, v198\n\t" \
  "v_cvt_pk_bf16_f32 v226, v196, v197\n\t" \
  "v_exp_f32 v199, v199\n\t" \
  "v_exp_f32 v200, v200\n\t" \
  "v_cvt_pk_bf16_f32 v227, v198, v199\n\t" \
  "v_exp_f32 v201, v201\n\t" \
  "v_exp_f32 v202, v202\n\t" \
  "v_cvt_pk_bf16_f32 v228, v200, v201\n\t" \
  "v_exp_f32 v203, v203\n\t" \
  "v_mfma_f32_32x32x16_bf16 v[80:95], v[236:239], v[224:227], v[80:95]\n\t" \
  "ds_read_b128 v[248:251], %[a0] offset:5280\n\t" \
  "v_exp_f32 v204, v204\n\t" \
  "v_cvt_pk_bf16_f32 v229, v202, v203\n\t" \
  "v_exp_f32 v205, v205\n\t" \
  "v_exp_f32 v206, v206\n\t" \
  "v_cvt_pk_bf16_f32 v230, v204, v205\n\t" \
  "v_exp_f32 v207, v207\n\t" \
  "v_exp_f32 v208, v208\n\t" \
  "v_cvt_pk_bf16_f32 v231, v206, v207\n\t" \
  "v_exp_f32 v209, v209\n\t" \
  "v_exp_f32 v210, v210\n\t" \
  "v_mfma_f32_32x32x16_bf16 v[80:95], v[240:243], v[228:231], v[80:95]\n\t" \
  "buffer_load_dwordx4 a[136:139], %[l16], %[rs], %[gwoa] offen offset:1024 sc1\n\t" \
  "v_cvt_pk_bf16_f32 v224, v208, v209\n\t" \
  "v_exp_f32 v211, v211\n\t" \
  "v_exp_f32 v212, v212\n\t" \
  "v_cvt_pk_bf16_f32 v225, v210, v211\n\t" \
  "v_exp_f32 v213, v213\n\t" \
  "v_mfma_f32_32x32x16_bf16 v[192:207], v[244:247], v[112:115], 0\n\t" \
  "buffer_load_dwordx4 a[140:143], %[l16], %[rs], %[gwob] offen offset:1024 sc1\n\t" \
  "v_exp_f32 v214, v214\n\t" \
  "v_cvt_pk_bf16_f32 v226, v212, v213\n\t" \
  "v_exp_f32 v215, v215\n\t" \
  "v_exp_f32 v216, v216\n\t" \
  "v_cvt_pk_bf16_f32 v227, v214, v215\n\t" \
  "v_exp_f32 v217, v217\n\t" \
  "v_exp_f32 v218, v218\n\t" \
  "v_cvt_pk_bf16_f32 v228, v216, v217\n\t" \
  "v_exp_f32 v219, v219\n\t" \
  "v_mfma_f32_32x32x16_bf16 v[96:111], v[236:239], v[224:227], v[96:111]\n\t" \
  "s_waitcnt lgkmcnt(0)\n\t" \
  "v_exp_f32 v220, v220\n\t" \
  "v_cvt_pk_bf16_f32 v229, v218, v219\n\t" \
  "v_exp_f32 v221, v221\n\t" \
  "v_exp_f32 v222, v222\n\t" \
  "v_cvt_pk_bf16_f32 v230, v220, v221\n\t" \
  "v_exp_f32 v223, v223\n\t" \
  "v_exp_f32 v192, v192\n\t" \
  "v_cvt_pk_bf16_f32 v231, v222, v223\n\t" \
  "v_exp_f32 v193, v193\n\t" \
  "v_exp_f32 v194, v194\n\t" \
  "v_mfma_f32_32x32x16_bf16 v[96:111], v[240:243], v[228:231], v[96:111]\n\t" \
  "ds_read_b128 v[240:243], %[a1] offset:6336\n\t" \
  "v_cvt_pk_bf16_f32 v224, v192, v193\n\t" \
  "v_exp_f32 v195, v195\n\t" \
  "v_exp_f32 v196, v196\n\t" \
  "v_cvt_pk_bf16_f32 v225, v194, v195\n\t" \
  "v_exp_f32 v197, v197\n\t" \
  "v_mfma_f32_32x32x16_bf16 v[208:223], v[244:247], v[116:119], 0\n\t" \
  "ds_read_b128 v[244:247], %[ka] offset:7168\n\t" \
  "v_exp_f32 v198, v198\n\t" \
  "v_cvt_pk_bf16_f32 v226, v196, v197\n\t" \
  "v_exp_f32 v199, v199\n\t" \
  "v_exp_f32 v200, v200\n\t" \
  "v_cvt_pk_bf16_f32 v227, v198, v199\n\t" \
  "v_exp_f32 v201, v201\n\t" \
  "v_exp_f32 v202, v202\n\t" \
  "v_cvt_pk_bf16_f32 v228, v200, v201\n\t" \
  "v_exp_f32 v203, v203\n\t" \
  "v_mfma_f32_32x32x16_bf16 v[80:95], v[248:251], v[224:227], v[80:95]\n\t" \
  "ds_read_b128 v[236:239], %[a0] offset:6336\n\t" \
  "v_exp_f32 v204, v204\n\t" \
  "v_cvt_pk_bf16_f32 v229, v202, v203\n\t" \
  "v_exp_f32 v205, v205\n\t" \
  "v_exp_f32 v206, v206\n\t" \
  "v_cvt_pk_bf16_f32 v230, v204, v205\n\t" \
  "v_exp_f32 v207, v207\n\t" \
  "v_exp_f32 v208, v208\n\t" \
  "v_cvt_pk_bf16_f32 v231, v206, v207\n\t" \
  "v_exp_f32 v209, v209\n\t" \
  "v_exp_f32 v210, v210\n\t" \
  "v_mfma_f32_32x32x16_bf16 v[80:95], v[252:255], v[228:231], v[80:95]\n\t" \
  "buffer_load_dwordx4 a[144:147], %[l16], %[rs], %[gwoa] offen offset:2048 sc1\n\t" \
  "v_cvt_pk_bf16_f32 v224, v208, v209\n\t" \
  "v_exp_f32 v211, v211\n\t" \
  "v_exp_f32 v212, v212\n\t" \
  "v_cvt_pk_bf16_f32 v225, v210, v211\n\t" \
  "v_exp_f32 v213, v213\n\t" \
  "v_mfma_f32_32x32x16_bf16 v[192:207], v[232:235], v[112:115], 0\n\t" \
  "buffer_load_dwordx4 a[148:151], %[l16], %[rs], %[gwob] offen offset:2048 sc1\n\t" \
  "v_exp_f32 v214, v214\n\t" \
  "v_cvt_pk_bf16_f32 v226, v212, v213\n\t" \
  "v_exp_f32 v215, v215\n\t" \
  "v_exp_f32 v216, v216\n\t" \
  "v_cvt_pk_bf16_f32 v227, v214, v215\n\t" \
  "v_exp_f32 v217, v217\n\t" \
  "v_exp_f32 v218, v218\n\t" \
  "v_cvt_pk_bf16_f32 v228, v216, v217\n\t" \
  "v_exp_f32 v219, v219\n\t" \
  "v_mfma_f32_32x32x16_bf16 v[96:111], v[248:251], v[224:227], v[96:111]\n\t" \
  "s_waitcnt lgkmcnt(0)\n\t" \
  "v_exp_f32 v220, v220\n\t" \
  "v_cvt_pk_bf16_f32 v229, v218, v219\n\t" \
  "v_exp_f32 v221, v221\n\t" \
  "v_exp_f32 v222, v222\n\t" \
  "v_cvt_pk_bf16_f32 v230, v220, v221\n\t" \
  "v_exp_f32 v223, v223\n\t" \
  "v_exp_f32 v192, v192\n\t" \
  "v_cvt_pk_bf16_f32 v231, v222, v223\n\t" \
  "v_exp_f32 v193, v193\n\t" \
  "v_exp_f32 v194, v194\n\t" \
  "v_mfma_f32_32x32x16_bf16 v[96:111], v[252:255], v[228:231], v[96:111]\n\t" \
  "ds_read_b128 v[252:255], %[a1] offset:7392\n\t" \
  "v_cvt_pk_bf16_f32 v224, v192, v193\n\t" \
  "v_exp_f32 v195, v195\n\t" \
  "v_exp_f32 v196, v196\n\t" \
  "v_cvt_pk_bf16_f32 v225, v194, v195\n\t" \
  "v_exp_f32 v197, v197\n\t" \
  "v_mfma_f32_32x32x16_bf16 v[208:223], v[232:235], v[116:119], 0\n\t" \
  "ds_read_b128 v[232:235], %[ka] offset:8192\n\t" \
  "v_exp_f32 v198, v198\n\t" \
  "v_cvt_pk_bf16_f32 v226, v196, v197\n\t" \
  "v_exp_f32 v199, v199\n\t" \
  "v_exp_f32 v200, v200\n\t" \
  "v_cvt_pk_bf16_f32 v227, v198, v199\n\t" \
  "v_exp_f32 v201, v201\n\t" \
  "v_exp_f32 v202, v202\n\t" \
  "v_cvt_pk_bf16_f32 v228, v200, v201\n\t" \
  "v_exp_f32 v203, v203\n\t" \
  "v_mfma_f32_32x32x16_bf16 v[80:95], v[236:239], v[224:227], v[80:95]\n\t" \
  "ds_read_b128 v[248:251], %[a0] offset:7392\n\t" \
  "v_exp_f32 v204, v204\n\t" \
  "v_cvt_pk_bf16_f32 v229, v202, v203\n\t" \
  "v_exp_f32 v205, v205\n\t" \
  "v_exp_f32 v206, v206\n\t" \
  "v_cvt_pk_bf16_f32 v230, v204, v205\n\t" \
  "v_exp_f32 v207, v207\n\t" \
  "v_exp_f32 v208, v208\n\t" \
  "v_cvt_pk_bf16_f32 v231, v206, v207\n\t" \
  "v_exp_f32 v209, v209\n\t" \
  "v_exp_f32 v210, v210\n\t" \
  "v_mfma_f32_32x32x16_bf16 v[80:95], v[240:243], v[228:231], v[80:95]\n\t" \
  "buffer_load_dwordx4 a[152:155], %[l16], %[rs], %[gwoa] offen offset:3072 sc1\n\t" \
  "v_cvt_pk_bf16_f32 v224, v208, v209\n\t" \
  "v_exp_f32 v211, v211\n\t" \
  "v_exp_f32 v212, v212\n\t" \
  "v_cvt_pk_bf16_f32 v225, v210, v211\n\t" \
  "v_exp_f32 v213, v213\n\t" \
  "v_mfma_f32_32x32x16_bf16 v[192:207], v[244:247], v[112:115], 0\n\t" \
  "buffer_load_dwordx4 a[156:159], %[l16], %[rs], %[gwob] offen offset:3072 sc1\n\t" \
  "v_exp_f32 v214, v214\n\t" \
  "v_cvt_pk_bf16_f32 v226, v212, v213\n\t" \
  "v_exp_f32 v215, v215\n\t" \
  "v_exp_f32 v216, v216\n\t" \
  "v_cvt_pk_bf16_f32 v227, v214, v215\n\t" \
  "v_exp_f32 v217, v217\n\t" \
  "v_exp_f32 v218, v218\n\t" \
  "v_cvt_pk_bf16_f32 v228, v216, v217\n\t" \
  "v_exp_f32 v219, v219\n\t" \
  "v_mfma_f32_32x32x16_bf16 v[96:111], v[236:239], v[224:227], v[96:111]\n\t" \
  "s_waitcnt lgkmcnt(0)\n\t" \
  "v_exp_f32 v220, v220\n\t" \
  "v_cvt_pk_bf16_f32 v229, v218, v219\n\t" \
  "v_exp_f32 v221, v221\n\t" \
  "v_exp_f32 v222, v222\n\t" \
  "v_cvt_pk_bf16_f32 v230, v220, v221\n\t" \
  "v_exp_f32 v223, v223\n\t" \
  "v_exp_f32 v192, v192\n\t" \
  "v_cvt_pk_bf16_f32 v231, v222, v223\n\t" \
  "v_exp_f32 v193, v193\n\t" \
  "v_exp_f32 v194, v194\n\t" \
  "v_mfma_f32_32x32x16_bf16 v[96:111], v[240:243], v[228:231], v[96:111]\n\t" \
  "ds_read_b128 v[240:243], %[a1] offset:8448\n\t" \
  "v_cvt_pk_bf16_f32 v224, v192, v193\n\t" \
  "v_exp_f32 v195, v195\n\t" \
  "v_exp_f32 v196, v196\n\t" \
  "v_cvt_pk_bf16_f32 v225, v194, v195\n\t" \
  "v_exp_f32 v197, v197\n\t" \
  "v_mfma_f32_32x32x16_bf16 v[208:223], v[244:247], v[116:119], 0\n\t" \
  "ds_read_b128 v[244:247], %[ka] offset:9216\n\t" \
  "v_exp_f32 v198, v198\n\t" \
  "v_cvt_pk_bf16_f32 v226, v196, v197\n\t" \
  "v_exp_f32 v199, v199\n\t" \
  "v_exp_f32 v200, v200\n\t" \
  "v_cvt_pk_bf16_f32 v227, v198, v199\n\t" \
  "v_exp_f32 v201, v201\n\t" \
  "v_exp_f32 v202, v202\n\t" \
  "v_cvt_pk_bf16_f32 v228, v200, v201\n\t" \
  "v_exp_f32 v203, v203\n\t" \
  "v_mfma_f32_32x32x16_bf16 v[80:95], v[248:251], v[224:227], v[80:95]\n\t" \
  "ds_read_b128 v[236:239], %[a0] offset:8448\n\t" \
  "v_exp_f32 v204, v204\n\t" \
  "v_cvt_pk_bf16_f32 v229, v202, v203\n\t" \
  "v_exp_f32 v205, v205\n\t" \
  "v_exp_f32 v206, v206\n\t" \
  "v_cvt_pk_bf16_f32 v230, v204, v205\n\t" \
  "v_exp_f32 v207, v207\n\t" \
  "v_exp_f32 v208, v208\n\t" \
  "v_cvt_pk_bf16_f32 v231, v206, v207\n\t" \
  "v_exp_f32 v209, v209\n\t" \
  "v_exp_f32 v210, v210\n\t" \
  "s_add_u32 %[gwoa], %[gwoa], 4096\n\t" \
  "v_mfma_f32_32x32x16_bf16 v[80:95], v[252:255], v[228:231], v[80:95]\n\t" \
  "buffer_load_dwordx4 a[160:163], %[l16], %[rs], %[gwoa] offen sc1\n\t" \
  "v_cvt_pk_bf16_f32 v224, v208, v209\n\t" \
  "v_exp_f32 v211, v211\n\t" \
  "v_exp_f32 v212, v212\n\t" \
  "v_cvt_pk_bf16_f32 v225, v210, v211\n\t" \
  "v_exp_f32 v213, v213\n\t" \
  "s_add_u32 %[gwob], %[gwob], 4096\n\t" \
  "v_mfma_f32_32x32x16_bf16 v[192:207], v[232:235], v[112:115], 0\n\t" \
  "buffer_load_dwordx4 a[164:167], %[l16], %[rs], %[gwob] offen sc1\n\t" \
  "v_exp_f32 v214, v214\n\t" \
  "v_cvt_pk_bf16_f32 v226, v212, v213\n\t" \
  "v_exp_f32 v215, v215\n\t" \
  "v_exp_f32 v216, v216\n\t" \
  "v_cvt_pk_bf16_f32 v227, v214, v215\n\t" \
  "v_exp_f32 v217, v217\n\t" \
  "v_exp_f32 v218, v218\n\t" \
  "v_cvt_pk_bf16_f32 v228, v216, v217\n\t" \
  "v_exp_f32 v219, v219\n\t" \
  "v_mfma_f32_32x32x16_bf16 v[96:111], v[248:251], v[224:227], v[96:111]\n\t" \
  "s_waitcnt lgkmcnt(0)\n\t" \
  "v_exp_f32 v220, v220\n\t" \
  "v_cvt_pk_bf16_f32 v229, v218, v219\n\t" \
  "v_exp_f32 v221, v221\n\t" \
  "v_exp_f32 v222, v222\n\t" \
  "v_cvt_pk_bf16_f32 v230, v220, v221\n\t" \
  "v_exp_f32 v223, v223\n\t" \
  "v_add_u32 %[ka], 8192, %[ka]\n\t" \
  "v_add_u32 %[a0], 8448, %[a0]\n\t" \
  "v_add_u32 %[a1], 8448, %[a1]\n\t" \
  "s_waitcnt vmcnt(10)\n\t" \
  "s_barrier\n\t" \
  "v_exp_f32 v192, v192\n\t" \
  "v_cvt_pk_bf16_f32 v231, v222, v223\n\t" \
  "v_exp_f32 v193, v193\n\t" \
  "v_exp_f32 v194, v194\n\t" \
  "v_mfma_f32_32x32x16_bf16 v[96:111], v[252:255], v[228:231], v[96:111]\n\t" \
  "ds_read_b128 v[252:255], %[a1] offset:1056\n\t" \
  "v_cvt_pk_bf16_f32 v224, v192, v193\n\t" \
  "v_exp_f32 v195, v195\n\t" \
  "v_exp_f32 v196, v196\n\t" \
  "v_cvt_pk_bf16_f32 v225, v194, v195\n\t" \
  "v_exp_f32 v197, v197\n\t" \
  "v_mfma_f32_32x32x16_bf16 v[208:223], v[232:235], v[116:119], 0\n\t" \
  "ds_read_b128 v[232:235], %[ka] offset:2048\n\t" \
  "v_exp_f32 v198, v198\n\t" \
  "v_cvt_pk_bf16_f32 v226, v196, v197\n\t" \
  "v_exp_f32 v199, v199\n\t" \
  "v_exp_f32 v200, v200\n\t" \
  "v_cvt_pk_bf16_f32 v227, v198, v199\n\t" \
  "v_exp_f32 v201, v201\n\t" \
  "v_exp_f32 v202, v202\n\t" \
  "v_cvt_pk_bf16_f32 v228, v200, v201\n\t" \
  "v_exp_f32 v203, v203\n\t" \
  "v_mfma_f32_32x32x16_bf16 v[80:95], v[236:239], v[224:227], v[80:95]\n\t" \
  "ds_read_b128 v[248:251], %[a0] offset:1056\n\t" \
  "v_exp_f32 v204, v204\n\t" \
  "v_cvt_pk_bf16_f32 v229, v202, v203\n\t" \
  "v_exp_f32 v205, v205\n\t" \
  "v_exp_f32 v206, v206\n\t" \
  "v_cvt_pk_bf16_f32 v230, v204, v205\n\t" \
  "v_exp_f32 v207, v207\n\t" \
  "v_exp_f32 v208, v208\n\t" \
  "v_cvt_pk_bf16_f32 v231, v206, v207\n\t" \
  "v_exp_f32 v209, v209\n\t" \
  "v_exp_f32 v210, v210\n\t" \
  "v_mfma_f32_32x32x16_bf16 v[80:95], v[240:243], v[228:231], v[80:95]\n\t" \
  "buffer_load_dwordx4 a[168:171], %[l16], %[rs], %[gwoa] offen offset:1024 sc1\n\t" \
  "v_cvt_pk_bf16_f32 v224, v208, v209\n\t" \
  "v_exp_f32 v211, v211\n\t" \
  "v_exp_f32 v212, v212\n\t" \
  "v_cvt_pk_bf16_f32 v225, v210, v211\n\t" \
  "v_exp_f32 v213, v213\n\t" \
  "v_mfma_f32_32x32x16_bf16 v[192:207], v[244:247], v[112:115], 0\n\t" \
  "buffer_load_dwordx4 a[172:175], %[l16], %[rs], %[gwob] offen offset:1024 sc1\n\t" \
  "v_exp_f32 v214, v214\n\t" \
  "v_cvt_pk_bf16_f32 v226, v212, v213\n\t" \
  "v_exp_f32 v215, v215\n\t" \
  "v_exp_f32 v216, v216\n\t" \
  "v_cvt_pk_bf16_f32 v227, v214, v215\n\t" \
  "v_exp_f32 v217, v217\n\t" \
  "v_exp_f32 v218, v218\n\t" \
  "v_cvt_pk_bf16_f32 v228, v216, v217\n\t" \
  "v_exp_f32 v219, v219\n\t" \
  "v_mfma_f32_32x32x16_bf16 v[96:111], v[236:239], v[224:227], v[96:111]\n\t" \
  "s_waitcnt lgkmcnt(0)\n\t" \
  "v_exp_f32 v220, v220\n\t" \
  "v_cvt_pk_bf16_f32 v229, v218, v219\n\t" \
  "v_exp_f32 v221, v221\n\t" \
  "v_exp_f32 v222, v222\n\t" \
  "v_cvt_pk_bf16_f32 v230, v220, v221\n\t" \
  "v_exp_f32 v223, v223\n\t" \
  "v_exp_f32 v192, v192\n\t" \
  "v_cvt_pk_bf16_f32 v231, v222, v223\n\t" \
  "v_exp_f32 v193, v193\n\t" \
  "v_exp_f32 v194, v194\n\t" \
  "v_mfma_f32_32x32x16_bf16 v[96:111], v[240:243], v[228:231], v[96:111]\n\t" \
  "ds_read_b128 v[240:243], %[a1] offset:2112\n\t" \
  "v_cvt_pk_bf16_f32 v224, v192, v193\n\t" \
  "v_exp_f32 v195, v195\n\t" \
  "v_exp_f32 v196, v196\n\t" \
  "v_cvt_pk_bf16_f32 v225, v194, v195\n\t" \
  "v_exp_f32 v197, v197\n\t" \
  "v_mfma_f32_32x32x16_bf16 v[208:223], v[244:247], v[116:119], 0\n\t" \
  "ds_read_b128 v[244:247], %[ka] offset:3072\n\t" \
  "v_exp_f32 v198, v198\n\t" \
  "v_cvt_pk_bf16_f32 v226, v196, v197\n\t" \
  "v_exp_f32 v199, v199\n\t" \
  "v_exp_f32 v200, v200\n\t" \
  "v_cvt_pk_bf16_f32 v227, v198, v199\n\t" \
  "v_exp_f32 v201, v201\n\t" \
  "v_exp_f32 v202, v202\n\t" \
  "v_cvt_pk_bf16_f32 v228, v200, v201\n\t" \
  "v_exp_f32 v203, v203\n\t" \
  "v_mfma_f32_32x32x16_bf16 v[80:95], v[248:251], v[224:227], v[80:95]\n\t" \
  "ds_read_b128 v[236:239], %[a0] offset:2112\n\t" \
  "v_exp_f32 v204, v204\n\t" \
  "v_cvt_pk_bf16_f32 v229, v202, v203\n\t" \
  "v_exp_f32 v205, v205\n\t" \
  "v_exp_f32 v206, v206\n\t" \
  "v_cvt_pk_bf16_f32 v230, v204, v205\n\t" \
  "v_exp_f32 v207, v207\n\t" \
  "v_exp_f32 v208, v208\n\t" \
  "v_cvt_pk_bf16_f32 v231, v206, v207\n\t" \
  "v_exp_f32 v209, v209\n\t" \
  "v_exp_f32 v210, v210\n\t" \
  "v_mfma_f32_32x32x16_bf16 v[80:95], v[252:255], v[228:231], v[80:95]\n\t" \
  "buffer_load_dwordx4 a[176:179], %[l16], %[rs], %[gwoa] offen offset:2048 sc1\n\t" \
  "v_cvt_pk_bf16_f32 v224, v208, v209\n\t" \
  "v_exp_f32 v211, v211\n\t" \
  "v_exp_f32 v212, v212\n\t" \
  "v_cvt_pk_bf16_f32 v225, v210, v211\n\t" \
  "v_exp_f32 v213, v213\n\t" \
  "v_mfma_f32_32x32x16_bf16 v[192:207], v[232:235], v[112:115], 0\n\t" \
  "buffer_load_dwordx4 a[180:183], %[l16], %[rs], %[gwob] offen offset:2048 sc1\n\t" \
  "v_exp_f32 v214, v214\n\t" \
  "v_cvt_pk_bf16_f32 v226, v212, v213\n\t" \
  "v_exp_f32 v215, v215\n\t" \
  "v_exp_f32 v216, v216\n\t" \
  "v_cvt_pk_bf16_f32 v227, v214, v215\n\t" \
  "v_exp_f32 v217, v217\n\t" \
  "v_exp_f32 v218, v218\n\t" \
  "v_cvt_pk_bf16_f32 v228, v216, v217\n\t" \
  "v_exp_f32 v219, v219\n\t" \
  "v_mfma_f32_32x32x16_bf16 v[96:111], v[248:251], v[224:227], v[96:111]\n\t" \
  "s_waitcnt lgkmcnt(0)\n\t" \
  "v_exp_f32 v220, v220\n\t" \
  "v_cvt_pk_bf16_f32 v229, v218, v219\n\t" \
  "v_exp_f32 v221, v221\n\t" \
  "v_exp_f32 v222, v222\n\t" \
  "v_cvt_pk_bf16_f32 v230, v220, v221\n\t" \
  "v_exp_f32 v223, v223\n\t" \
  "v_exp_f32 v192, v192\n\t" \
  "v_cvt_pk_bf16_f32 v231, v222, v223\n\t" \
  "v_exp_f32 v193, v193\n\t" \
  "v_exp_f32 v194, v194\n\t" \
  "v_mfma_f32_32x32x16_bf16 v[96:111], v[252:255], v[228:231], v[96:111]\n\t" \
  "ds_read_b128 v[252:255], %[a1] offset:3168\n\t" \
  "v_cvt_pk_bf16_f32 v224, v192, v193\n\t" \
  "v_exp_f32 v195, v195\n\t" \
  "v_exp_f32 v196, v196\n\t" \
  "v_cvt_pk_bf16_f32 v225, v194, v195\n\t" \
  "v_exp_f32 v197, v197\n\t" \
  "v_mfma_f32_32x32x16_bf16 v[208:223], v[232:235], v[116:119], 0\n\t" \
  "ds_read_b128 v[232:235], %[ka] offset:4096\n\t" \
  "v_exp_f32 v198, v198\n\t" \
  "v_cvt_pk_bf16_f32 v226, v196, v197\n\t" \
  "v_exp_f32 v199, v199\n\t" \
  "v_exp_f32 v200, v200\n\t" \
  "v_cvt_pk_bf16_f32 v227, v198, v199\n\t" \
  "v_exp_f32 v201, v201\n\t" \
  "v_exp_f32 v202, v202\n\t" \
  "v_cvt_pk_bf16_f32 v228, v200, v201\n\t" \
  "v_exp_f32 v203, v203\n\t" \
  "v_mfma_f32_32x32x16_bf16 v[80:95], v[236:239], v[224:227], v[80:95]\n\t" \
  "ds_read_b128 v[248:251], %[a0] offset:3168\n\t" \
  "v_exp_f32 v204, v204\n\t" \
  "v_cvt_pk_bf16_f32 v229, v202, v203\n\t" \
  "v_exp_f32 v205, v205\n\t" \
  "v_exp_f32 v206, v206\n\t" \
  "v_cvt_pk_bf16_f32 v230, v204, v205\n\t" \
  "v_exp_f32 v207, v207\n\t" \
  "v_exp_f32 v208, v208\n\t" \
  "v_cvt_pk_bf16_f32 v231, v206, v207\n\t" \
  "v_exp_f32 v209, v209\n\t" \
  "v_exp_f32 v210, v210\n\t" \
  "v_mfma_f32_32x32x16_bf16 v[80:95], v[240:243], v[228:231], v[80:95]\n\t" \
  "buffer_load_dwordx4 a[184:187], %[l16], %[rs], %[gwoa] offen offset:3072 sc1\n\t" \
  "v_cvt_pk_bf16_f32 v224, v208, v209\n\t" \
  "v_exp_f32 v211, v211\n\t" \
  "v_exp_f32 v212, v212\n\t" \
  "v_cvt_pk_bf16_f32 v225, v210, v211\n\t" \
  "v_exp_f32 v213, v213\n\t" \
  "v_mfma_f32_32x32x16_bf16 v[192:207], v[244:247], v[112:115], 0\n\t" \
  "buffer_load_dwordx4 a[188:191], %[l16], %[rs], %[gwob] offen offset:3072 sc1\n\t" \
  "v_exp_f32 v214, v214\n\t" \
  "v_cvt_pk_bf16_f32 v226, v212, v213\n\t" \
  "v_exp_f32 v215, v215\n\t" \
  "v_exp_f32 v216, v216\n\t" \
  "v_cvt_pk_bf16_f32 v227, v214, v215\n\t" \
  "v_exp_f32 v217, v217\n\t" \
  "v_exp_f32 v218, v218\n\t" \
  "v_cvt_pk_bf16_f32 v228, v216, v217\n\t" \
  "v_exp_f32 v219, v219\n\t" \
  "v_mfma_f32_32x32x16_bf16 v[96:111], v[236:239], v[224:227], v[96:111]\n\t" \
  "s_waitcnt lgkmcnt(0)\n\t" \
  "v_exp_f32 v220, v220\n\t" \
  "v_cvt_pk_bf16_f32 v229, v218, v219\n\t" \
  "v_exp_f32 v221, v221\n\t" \
  "v_exp_f32 v222, v222\n\t" \
  "v_cvt_pk_bf16_f32 v230, v220, v221\n\t" \
  "v_exp_f32 v223, v223\n\t" \
  "v_exp_f32 v192, v192\n\t" \
  "v_cvt_pk_bf16_f32 v231, v222, v223\n\t" \
  "v_exp_f32 v193, v193\n\t" \
  "v_exp_f32 v194, v194\n\t" \
  "v_mfma_f32_32x32x16_bf16 v[96:111], v[240:243], v[228:231], v[96:111]\n\t" \
  "ds_read_b128 v[240:243], %[a1] offset:4224\n\t" \
  "v_cvt_pk_bf16_f32 v224, v192, v193\n\t" \
  "v_exp_f32 v195, v195\n\t" \
  "v_exp_f32 v196, v196\n\t" \
  "v_cvt_pk_bf16_f32 v225, v194, v195\n\t" \
  "v_exp_f32 v197, v197\n\t" \
  "v_mfma_f32_32x32x16_bf16 v[208:223], v[244:247], v[116:119], 0\n\t" \
  "ds_read_b128 v[244:247], %[ka] offset:5120\n\t" \
  "v_exp_f32 v198, v198\n\t" \
  "v_cvt_pk_bf16_f32 v226, v196, v197\n\t" \
  "v_exp_f32 v199, v199\n\t" \
  "v_exp_f32 v200, v200\n\t" \
  "v_cvt_pk_bf16_f32 v227, v198, v199\n\t" \
  "v_exp_f32 v201, v201\n\t" \
  "v_exp_f32 v202, v202\n\t" \
  "v_cvt_pk_bf16_f32 v228, v200, v201\n\t" \
  "v_exp_f32 v203, v203\n\t" \
  "v_mfma_f32_32x32x16_bf16 v[80:95], v[248:251], v[224:227], v[80:95]\n\t" \
  "ds_read_b128 v[236:239], %[a0] offset:4224\n\t" \
  "v_exp_f32 v204, v204\n\t" \
  "v_cvt_pk_bf16_f32 v229, v202, v203\n\t" \
  "v_exp_f32 v205, v205\n\t" \
  "v_exp_f32 v206, v206\n\t" \
  "v_cvt_pk_bf16_f32 v230, v204, v205\n\t" \
  "v_exp_f32 v207, v207\n\t" \
  "v_exp_f32 v208, v208\n\t" \
  "v_cvt_pk_bf16_f32 v231, v206, v207\n\t" \
  "v_exp_f32 v209, v209\n\t" \
  "v_exp_f32 v210, v210\n\t" \
  "v_mfma_f32_32x32x16_bf16 v[80:95], v[252:255], v[228:231], v[80:95]\n\t" \
  "buffer_load_dwordx4 a[0:3], %[l16], %[rs], %[gw1] offen sc1\n\t" \
  "v_cvt_pk_bf16_f32 v224, v208, v209\n\t" \
  "v_exp_f32 v211, v211\n\t" \
  "v_exp_f32 v212, v212\n\t" \
  "v_cvt_pk_bf16_f32 v225, v210, v211\n\t" \
  "v_exp_f32 v213, v213\n\t" \
  "v_mfma_f32_32x32x16_bf16 v[192:207], v[232:235], v[112:115], 0\n\t" \
  "buffer_load_dwordx4 a[4:7], %[l16], %[rs], %[gw1] offen offset:1024 sc1\n\t" \
  "v_exp_f32 v214, v214\n\t" \
  "v_cvt_pk_bf16_f32 v226, v212, v213\n\t" \
  "v_exp_f32 v215, v215\n\t" \
  "v_exp_f32 v216, v216\n\t" \
  "v_cvt_pk_bf16_f32 v227, v214, v215\n\t" \
  "v_exp_f32 v217, v217\n\t" \
  "v_exp_f32 v218, v218\n\t" \
  "v_cvt_pk_bf16_f32 v228, v216, v217\n\t" \
  "v_exp_f32 v219, v219\n\t" \
  "v_mfma_f32_32x32x16_bf16 v[96:111], v[248:251], v[224:227], v[96:111]\n\t" \
  "s_waitcnt lgkmcnt(0)\n\t" \
  "v_exp_f32 v220, v220\n\t" \
  "v_cvt_pk_bf16_f32 v229, v218, v219\n\t" \
  "v_exp_f32 v221, v221\n\t" \
  "v_exp_f32 v222, v222\n\t" \
  "v_cvt_pk_bf16_f32 v230, v220, v221\n\t" \
  "v_exp_f32 v223, v223\n\t" \
  "v_exp_f32 v192, v192\n\t" \
  "v_cvt_pk_bf16_f32 v231, v222, v223\n\t" \
  "v_exp_f32 v193, v193\n\t" \
  "v_exp_f32 v194, v194\n\t" \
  "v_mfma_f32_32x32x16_bf16 v[96:111], v[252:255], v[228:231], v[96:111]\n\t" \
  "ds_read_b128 v[252:255], %[a1] offset:5280\n\t" \
  "v_cvt_pk_bf16_f32 v224, v192, v193\n\t" \
  "v_exp_f32 v195, v195\n\t" \
  "v_exp_f32 v196, v196\n\t" \
  "v_cvt_pk_bf16_f32 v225, v194, v195\n\t" \
  "v_exp_f32 v197, v197\n\t" \
  "v_mfma_f32_32x32x16_bf16 v[208:223], v[232:235], v[116:119], 0\n\t" \
  "ds_read_b128 v[232:235], %[ka] offset:6144\n\t" \
  "v_exp_f32 v198, v198\n\t" \
  "v_cvt_pk_bf16_f32 v226, v196, v197\n\t" \
  "v_exp_f32 v199, v199\n\t" \
  "v_exp_f32 v200, v200\n\t" \
  "v_cvt_pk_bf16_f32 v227, v198, v199\n\t" \
  "v_exp_f32 v201, v201\n\t" \
  "v_exp_f32 v202, v202\n\t" \
  "v_cvt_pk_bf16_f32 v228, v200, v201\n\t" \
  "v_exp_f32 v203, v203\n\t" \
  "v_mfma_f32_32x32x16_bf16 v[80:95], v[236:239], v[224:227], v[80:95]\n\t" \
  "ds_read_b128 v[248:251], %[a0] offset:5280\n\t" \
  "v_exp_f32 v204, v204\n\t" \
  "v_cvt_pk_bf16_f32 v229, v202, v203\n\t" \
  "v_exp_f32 v205, v205\n\t" \
  "v_exp_f32 v206, v206\n\t" \
  "v_cvt_pk_bf16_f32 v230, v204, v205\n\t" \
  "v_exp_f32 v207, v207\n\t" \
  "v_exp_f32 v208, v208\n\t" \
  "v_cvt_pk_bf16_f32 v231, v206, v207\n\t" \
  "v_exp_f32 v209, v209\n\t" \
  "v_exp_f32 v210, v210\n\t" \
  "v_mfma_f32_32x32x16_bf16 v[80:95], v[240:243], v[228:231], v[80:95]\n\t" \
  "buffer_load_dwordx4 a[8:11], %[l16], %[rs], %[gw1] offen offset:2048 sc1\n\t" \
  "v_cvt_pk_bf16_f32 v224, v208, v209\n\t" \
  "v_exp_f32 v211, v211\n\t" \
  "v_exp_f32 v212, v212\n\t" \
  "v_cvt_pk_bf16_f32 v225, v210, v211\n\t" \
  "v_exp_f32 v213, v213\n\t" \
  "v_mfma_f32_32x32x16_bf16 v[192:207], v[244:247], v[112:115], 0\n\t" \
  "buffer_load_dwordx4 a[12:15], %[l16], %[rs], %[gw1] offen offset:3072 sc1\n\t" \
  "v_exp_f32 v214, v214\n\t" \
  "v_cvt_pk_bf16_f32 v226, v212, v213\n\t" \
  "v_exp_f32 v215, v215\n\t" \
  "v_exp_f32 v216, v216\n\t" \
  "v_cvt_pk_bf16_f32 v227, v214, v215\n\t" \
  "v_exp_f32 v217, v217\n\t" \
  "v_exp_f32 v218, v218\n\t" \
  "v_cvt_pk_bf16_f32 v228, v216, v217\n\t" \
  "v_exp_f32 v219, v219\n\t" \
  "v_mfma_f32_32x32x16_bf16 v[96:111], v[236:239], v[224:227], v[96:111]\n\t" \
  "s_waitcnt lgkmcnt(0)\n\t" \
  "v_exp_f32 v220, v220\n\t" \
  "v_cvt_pk_bf16_f32 v229, v218, v219\n\t" \
  "v_exp_f32 v221, v221\n\t" \
  "v_exp_f32 v222, v222\n\t" \
  "v_cvt_pk_bf16_f32 v230, v220, v221\n\t" \
  "v_exp_f32 v223, v223\n\t" \
  "v_exp_f32 v192, v192\n\t" \
  "v_cvt_pk_bf16_f32 v231, v222, v223\n\t" \
  "v_exp_f32 v193, v193\n\t" \
  "v_exp_f32 v194, v194\n\t" \
  "v_mfma_f32_32x32x16_bf16 v[96:111], v[240:243], v[228:231], v[96:111]\n\t" \
  "ds_read_b128 v[240:243], %[a1] offset:6336\n\t" \
  "v_cvt_pk_bf16_f32 v224, v192, v193\n\t" \
  "v_exp_f32 v195, v195\n\t" \
  "v_exp_f32 v196, v196\n\t" \
  "v_cvt_pk_bf16_f32 v225, v194, v195\n\t" \
  "v_exp_f32 v197, v197\n\t" \
  "v_mfma_f32_32x32x16_bf16 v[208:223], v[244:247], v[116:119], 0\n\t" \
  "ds_read_b128 v[244:247], %[ka] offset:7168\n\t" \
  "v_exp_f32 v198, v198\n\t" \
  "v_cvt_pk_bf16_f32 v226, v196, v197\n\t" \
  "v_exp_f32 v199, v199\n\t" \
  "v_exp_f32 v200, v200\n\t" \
  "v_cvt_pk_bf16_f32 v227, v198, v199\n\t" \
  "v_exp_f32 v201, v201\n\t" \
  "v_exp_f32 v202, v202\n\t" \
  "v_cvt_pk_bf16_f32 v228, v200, v201\n\t" \
  "v_exp_f32 v203, v203\n\t" \
  "v_mfma_f32_32x32x16_bf16 v[80:95], v[248:251], v[224:227], v[80:95]\n\t" \
  "ds_read_b128 v[236:239], %[a0] offset:6336\n\t" \
  "v_exp_f32 v204, v204\n\t" \
  "v_cvt_pk_bf16_f32 v229, v202, v203\n\t" \
  "v_exp_f32 v205, v205\n\t" \
  "v_exp_f32 v206, v206\n\t" \
  "v_cvt_pk_bf16_f32 v230, v204, v205\n\t" \
  "v_exp_f32 v207, v207\n\t" \
  "v_exp_f32 v208, v208\n\t" \
  "v_cvt_pk_bf16_f32 v231, v206, v207\n\t" \
  "v_exp_f32 v209, v209\n\t" \
  "v_exp_f32 v210, v210\n\t" \
  "s_add_u32 %[gw1], %[gw1], 4096\n\t" \
  "v_mfma_f32_32x32x16_bf16 v[80:95], v[252:255], v[228:231], v[80:95]\n\t" \
  "buffer_load_dwordx4 a[16:19], %[l16], %[rs], %[gw1] offen sc1\n\t" \
  "v_cvt_pk_bf16_f32 v224, v208, v209\n\t" \
  "v_exp_f32 v211, v211\n\t" \
  "v_exp_f32 v212, v212\n\t" \
  "v_cvt_pk_bf16_f32 v225, v210, v211\n\t" \
  "v_exp_f32 v213, v213\n\t" \
  "v_mfma_f32_32x32x16_bf16 v[192:207], v[232:235], v[112:115], 0\n\t" \
  "buffer_load_dwordx4 a[20:23], %[l16], %[rs], %[gw1] offen offset:1024 sc1\n\t" \
  "v_exp_f32 v214, v214\n\t" \
  "v_cvt_pk_bf16_f32 v226, v212, v213\n\t" \
  "v_exp_f32 v215, v215\n\t" \
  "v_exp_f32 v216, v216\n\t" \
  "v_cvt_pk_bf16_f32 v227, v214, v215\n\t" \
  "v_exp_f32 v217, v217\n\t" \
  "v_exp_f32 v218, v218\n\t" \
  "v_cvt_pk_bf16_f32 v228, v216, v217\n\t" \
  "v_exp_f32 v219, v219\n\t" \
  "v_mfma_f32_32x32x16_bf16 v[96:111], v[248:251], v[224:227], v[96:111]\n\t" \
  "s_waitcnt lgkmcnt(0)\n\t" \
  "v_exp_f32 v220, v220\n\t" \
  "v_cvt_pk_bf16_f32 v229, v218, v219\n\t" \
  "v_exp_f32 v221, v221\n\t" \
  "v_exp_f32 v222, v222\n\t" \
  "v_cvt_pk_bf16_f32 v230, v220, v221\n\t" \
  "v_exp_f32 v223, v223\n\t" \
  "v_exp_f32 v192, v192\n\t" \
  "v_cvt_pk_bf16_f32 v231, v222, v223\n\t" \
  "v_exp_f32 v193, v193\n\t" \
  "v_exp_f32 v194, v194\n\t" \
  "v_mfma_f32_32x32x16_bf16 v[96:111], v[252:255], v[228:231], v[96:111]\n\t" \
  "ds_read_b128 v[252:255], %[a1] offset:7392\n\t" \
  "v_cvt_pk_bf16_f32 v224, v192, v193\n\t" \
  "v_exp_f32 v195, v195\n\t" \
  "v_exp_f32 v196, v196\n\t" \
  "v_cvt_pk_bf16_f32 v225, v194, v195\n\t" \
  "v_exp_f32 v197, v197\n\t" \
  "v_mfma_f32_32x32x16_bf16 v[208:223], v[232:235], v[116:119], 0\n\t" \
  "ds_read_b128 v[232:235], %[ka] offset:8192\n\t" \
  "v_exp_f32 v198, v198\n\t" \
  "v_cvt_pk_bf16_f32 v226, v196, v197\n\t" \
  "v_exp_f32 v199, v199\n\t" \
  "v_exp_f32 v200, v200\n\t" \
  "v_cvt_pk_bf16_f32 v227, v198, v199\n\t" \
  "v_exp_f32 v201, v201\n\t" \
  "v_exp_f32 v202, v202\n\t" \
  "v_cvt_pk_bf16_f32 v228, v200, v201\n\t" \
  "v_exp_f32 v203, v203\n\t" \
  "v_mfma_f32_32x32x16_bf16 v[80:95], v[236:239], v[224:227], v[80:95]\n\t" \
  "ds_read_b128 v[248:251], %[a0] offset:7392\n\t" \
  "v_exp_f32 v204, v204\n\t" \
  "v_cvt_pk_bf16_f32 v229, v202, v203\n\t" \
  "v_exp_f32 v205, v205\n\t" \
  "v_exp_f32 v206, v206\n\t" \
  "v_cvt_pk_bf16_f32 v230, v204, v205\n\t" \
  "v_exp_f32 v207, v207\n\t" \
  "v_exp_f32 v208, v208\n\t" \
  "v_cvt_pk_bf16_f32 v231, v206, v207\n\t" \
  "v_exp_f32 v209, v209\n\t" \
  "v_exp_f32 v210, v210\n\t" \
  "v_mfma_f32_32x32x16_bf16 v[80:95], v[240:243], v[228:231], v[80:95]\n\t" \
  "buffer_load_dwordx4 a[24:27], %[l16], %[rs], %[gw1] offen offset:2048 sc1\n\t" \
  "v_cvt_pk_bf16_f32 v224, v208, v209\n\t" \
  "v_exp_f32 v211, v211\n\t" \
  "v_exp_f32 v212, v212\n\t" \
  "v_cvt_pk_bf16_f32 v225, v210, v211\n\t" \
  "v_exp_f32 v213, v213\n\t" \
  "v_mfma_f32_32x32x16_bf16 v[192:207], v[244:247], v[112:115], 0\n\t" \
  "buffer_load_dwordx4 a[28:31], %[l16], %[rs], %[gw1] offen offset:3072 sc1\n\t" \
  "v_exp_f32 v214, v214\n\t" \
  "v_cvt_pk_bf16_f32 v226, v212, v213\n\t" \
  "v_exp_f32 v215, v215\n\t" \
  "v_exp_f32 v216, v216\n\t" \
  "v_cvt_pk_bf16_f32 v227, v214, v215\n\t" \
  "v_exp_f32 v217, v217\n\t" \
  "v_exp_f32 v218, v218\n\t" \
  "v_cvt_pk_bf16_f32 v228, v216, v217\n\t" \
  "v_exp_f32 v219, v219\n\t" \
  "v_mfma_f32_32x32x16_bf16 v[96:111], v[236:239], v[224:227], v[96:111]\n\t" \
  "s_waitcnt lgkmcnt(0)\n\t" \
  "v_exp_f32 v220, v220\n\t" \
  "v_cvt_pk_bf16_f32 v229, v218, v219\n\t" \
  "v_exp_f32 v221, v221\n\t" \
  "v_exp_f32 v222, v222\n\t" \
  "v_cvt_pk_bf16_f32 v230, v220, v221\n\t" \
  "v_exp_f32 v223, v223\n\t" \
  "v_exp_f32 v192, v192\n\t" \
  "v_cvt_pk_bf16_f32 v231, v222, v223\n\t" \
  "v_exp_f32 v193, v193\n\t" \
  "v_exp_f32 v194, v194\n\t" \
  "v_mfma_f32_32x32x16_bf16 v[96:111], v[240:243], v[228:231], v[96:111]\n\t" \
  "ds_read_b128 v[240:243], %[a1] offset:8448\n\t" \
  "v_cvt_pk_bf16_f32 v224, v192, v193\n\t" \
  "v_exp_f32 v195, v195\n\t" \
  "v_exp_f32 v196, v196\n\t" \
  "v_cvt_pk_bf16_f32 v225, v194, v195\n\t" \
  "v_exp_f32 v197, v197\n\t" \
  "v_mfma_f32_32x32x16_bf16 v[208:223], v[244:247], v[116:119], 0\n\t" \
  "ds_read_b128 v[244:247], %[ka] offset:9216\n\t" \
  "v_exp_f32 v198, v198\n\t" \
  "v_cvt_pk_bf16_f32 v226, v196, v197\n\t" \
  "v_exp_f32 v199, v199\n\t" \
  "v_exp_f32 v200, v200\n\t" \
  "v_cvt_pk_bf16_f32 v227, v198, v199\n\t" \
  "v_exp_f32 v201, v201\n\t" \
  "v_exp_f32 v202, v202\n\t" \
  "v_cvt_pk_bf16_f32 v228, v200, v201\n\t" \
  "v_exp_f32 v203, v203\n\t" \
  "v_mfma_f32_32x32x16_bf16 v[80:95], v[248:251], v[224:227], v[80:95]\n\t" \
  "ds_read_b128 v[236:239], %[a0] offset:8448\n\t" \
  "v_exp_f32 v204, v204\n\t" \
  "v_cvt_pk_bf16_f32 v229, v202, v203\n\t" \
  "v_exp_f32 v205, v205\n\t" \
  "v_exp_f32 v206, v206\n\t" \
  "v_cvt_pk_bf16_f32 v230, v204, v205\n\t" \
  "v_exp_f32 v207, v207\n\t" \
  "v_exp_f32 v208, v208\n\t" \
  "v_cvt_pk_bf16_f32 v231, v206, v207\n\t" \
  "v_exp_f32 v209, v209\n\t" \
  "v_exp_f32 v210, v210\n\t" \
  "s_add_u32 %[gw1], %[gw1], 4096\n\t" \
  "v_mfma_f32_32x32x16_bf16 v[80:95], v[252:255], v[228:231], v[80:95]\n\t" \
  "buffer_load_dwordx4 a[32:35], %[l16], %[rs], %[gw1] offen sc1\n\t" \
  "v_cvt_pk_bf16_f32 v224, v208, v209\n\t" \
  "v_exp_f32 v211, v211\n\t" \
  "v_exp_f32 v212, v212\n\t" \
  "v_cvt_pk_bf16_f32 v225, v210, v211\n\t" \
  "v_exp_f32 v213, v213\n\t" \
  "v_mfma_f32_32x32x16_bf16 v[192:207], v[232:235], v[112:115], 0\n\t" \
  "buffer_load_dwordx4 a[36:39], %[l16], %[rs], %[gw1] offen offset:1024 sc1\n\t" \
  "v_exp_f32 v214, v214\n\t" \
  "v_cvt_pk_bf16_f32 v226, v212, v213\n\t" \
  "v_exp_f32 v215, v215\n\t" \
  "v_exp_f32 v216, v216\n\t" \
  "v_cvt_pk_bf16_f32 v227, v214, v215\n\t" \
  "v_exp_f32 v217, v217\n\t" \
  "v_exp_f32 v218, v218\n\t" \
  "v_cvt_pk_bf16_f32 v228, v216, v217\n\t" \
  "v_exp_f32 v219, v219\n\t" \
  "v_mfma_f32_32x32x16_bf16 v[96:111], v[248:251], v[224:227], v[96:111]\n\t" \
  "s_waitcnt lgkmcnt(0)\n\t" \
  "v_exp_f32 v220, v220\n\t" \
  "v_cvt_pk_bf16_f32 v229, v218, v219\n\t" \
  "v_exp_f32 v221, v221\n\t" \
  "v_exp_f32 v222, v222\n\t" \
  "v_cvt_pk_bf16_f32 v230, v220, v221\n\t" \
  "v_exp_f32 v223, v223\n\t" \
  "v_add_u32 %[ka], 8192, %[ka]\n\t" \
  "v_add_u32 %[a0], 8448, %[a0]\n\t" \
  "v_add_u32 %[a1], 8448, %[a1]\n\t" \
  "v_exp_f32 v192, v192\n\t" \
  "v_cvt_pk_bf16_f32 v231, v222, v223\n\t" \
  "v_exp_f32 v193, v193\n\t" \
  "v_exp_f32 v194, v194\n\t" \
  "v_mfma_f32_32x32x16_bf16 v[96:111], v[252:255], v[228:231], v[96:111]\n\t" \
  "ds_read_b128 v[252:255], %[a1] offset:1056\n\t" \
  "v_cvt_pk_bf16_f32 v224, v192, v193\n\t" \
  "v_exp_f32 v195, v195\n\t" \
  "v_exp_f32 v196, v196\n\t" \
  "v_cvt_pk_bf16_f32 v225, v194, v195\n\t" \
  "v_exp_f32 v197, v197\n\t" \
  "v_mfma_f32_32x32x16_bf16 v[208:223], v[232:235], v[116:119], 0\n\t" \
  "ds_read_b128 v[232:235], %[ka] offset:2048\n\t" \
  "v_exp_f32 v198, v198\n\t" \
  "v_cvt_pk_bf16_f32 v226, v196, v197\n\t" \
  "v_exp_f32 v199, v199\n\t" \
  "v_exp_f32 v200, v200\n\t" \
  "v_cvt_pk_bf16_f32 v227, v198, v199\n\t" \
  "v_exp_f32 v201, v201\n\t" \
  "v_exp_f32 v202, v202\n\t" \
  "v_cvt_pk_bf16_f32 v228, v200, v201\n\t" \
  "v_exp_f32 v203, v203\n\t" \
  "v_mfma_f32_32x32x16_bf16 v[80:95], v[236:239], v[224:227], v[80:95]\n\t" \
  "ds_read_b128 v[248:251], %[a0] offset:1056\n\t" \
  "v_exp_f32 v204, v204\n\t" \
  "v_cvt_pk_bf16_f32 v229, v202, v203\n\t" \
  "v_exp_f32 v205, v205\n\t" \
  "v_exp_f32 v206, v206\n\t" \
  "v_cvt_pk_bf16_f32 v230, v204, v205\n\t" \
  "v_exp_f32 v207, v207\n\t" \
  "v_exp_f32 v208, v208\n\t" \
  "v_cvt_pk_bf16_f32 v231, v206, v207\n\t" \
  "v_exp_f32 v209, v209\n\t" \
  "v_exp_f32 v210, v210\n\t" \
  "v_mfma_f32_32x32x16_bf16 v[80:95], v[240:243], v[228:231], v[80:95]\n\t" \
  "buffer_load_dwordx4 a[40:43], %[l16], %[rs], %[gw1] offen offset:2048 sc1\n\t" \
  "v_cvt_pk_bf16_f32 v224, v208, v209\n\t" \
  "v_exp_f32 v211, v211\n\t" \
  "v_exp_f32 v212, v212\n\t" \
  "v_cvt_pk_bf16_f32 v225, v210, v211\n\t" \
  "v_exp_f32 v213, v213\n\t" \
  "v_mfma_f32_32x32x16_bf16 v[192:207], v[244:247], v[112:115], 0\n\t" \
  "buffer_load_dwordx4 a[44:47], %[l16], %[rs], %[gw1] offen offset:3072 sc1\n\t" \
  "v_exp_f32 v214, v214\n\t" \
  "v_cvt_pk_bf16_f32 v226, v212, v213\n\t" \
  "v_exp_f32 v215, v215\n\t" \
  "v_exp_f32 v216, v216\n\t" \
  "v_cvt_pk_bf16_f32 v227, v214, v215\n\t" \
  "v_exp_f32 v217, v217\n\t" \
  "v_exp_f32 v218, v218\n\t" \
  "v_cvt_pk_bf16_f32 v228, v216, v217\n\t" \
  "v_exp_f32 v219, v219\n\t" \
  "v_mfma_f32_32x32x16_bf16 v[96:111], v[236:239], v[224:227], v[96:111]\n\t" \
  "s_waitcnt lgkmcnt(0)\n\t" \
  "v_exp_f32 v220, v220\n\t" \
  "v_cvt_pk_bf16_f32 v229, v218, v219\n\t" \
  "v_exp_f32 v221, v221\n\t" \
  "v_exp_f32 v222, v222\n\t" \
  "v_cvt_pk_bf16_f32 v230, v220, v221\n\t" \
  "v_exp_f32 v223, v223\n\t" \
  "v_exp_f32 v192, v192\n\t" \
  "v_cvt_pk_bf16_f32 v231, v222, v223\n\t" \
  "v_exp_f32 v193, v193\n\t" \
  "v_exp_f32 v194, v194\n\t" \
  "v_mfma_f32_32x32x16_bf16 v[96:111], v[240:243], v[228:231], v[96:111]\n\t" \
  "ds_read_b128 v[240:243], %[a1] offset:2112\n\t" \
  "v_cvt_pk_bf16_f32 v224, v192, v193\n\t" \
  "v_exp_f32 v195, v195\n\t" \
  "v_exp_f32 v196, v196\n\t" \
  "v_cvt_pk_bf16_f32 v225, v194, v195\n\t" \
  "v_exp_f32 v197, v197\n\t" \
  "v_mfma_f32_32x32x16_bf16 v[208:223], v[244:247], v[116:119], 0\n\t" \
  "ds_read_b128 v[244:247], %[ka] offset:3072\n\t" \
  "v_exp_f32 v198, v198\n\t" \
  "v_cvt_pk_bf16_f32 v226, v196, v197\n\t" \
  "v_exp_f32 v199, v199\n\t" \
  "v_exp_f32 v200, v200\n\t" \
  "v_cvt_pk_bf16_f32 v227, v198, v199\n\t" \
  "v_exp_f32 v201, v201\n\t" \
  "v_exp_f32 v202, v202\n\t" \
  "v_cvt_pk_bf16_f32 v228, v200, v201\n\t" \
  "v_exp_f32 v203, v203\n\t" \
  "v_mfma_f32_32x32x16_bf16 v[80:95], v[248:251], v[224:227], v[80:95]\n\t" \
  "ds_read_b128 v[236:239], %[a0] offset:2112\n\t" \
  "v_exp_f32 v204, v204\n\t" \
  "v_cvt_pk_bf16_f32 v229, v202, v203\n\t" \
  "v_exp_f32 v205, v205\n\t" \
  "v_exp_f32 v206, v206\n\t" \
  "v_cvt_pk_bf16_f32 v230, v204, v205\n\t" \
  "v_exp_f32 v207, v207\n\t" \
  "v_exp_f32 v208, v208\n\t" \
  "v_cvt_pk_bf16_f32 v231, v206, v207\n\t" \
  "v_exp_f32 v209, v209\n\t" \
  "v_exp_f32 v210, v210\n\t" \
  "s_add_u32 %[gw1], %[gw1], 4096\n\t" \
  "v_mfma_f32_32x32x16_bf16 v[80:95], v[252:255], v[228:231], v[80:95]\n\t" \
  "buffer_load_dwordx4 a[48:51], %[l16], %[rs], %[gw1] offen sc1\n\t" \
  "v_cvt_pk_bf16_f32 v224, v208, v209\n\t" \
  "v_exp_f32 v211, v211\n\t" \
  "v_exp_f32 v212, v212\n\t" \
  "v_cvt_pk_bf16_f32 v225, v210, v211\n\t" \
  "v_exp_f32 v213, v213\n\t" \
  "v_mfma_f32_32x32x16_bf16 v[192:207], v[232:235], v[112:115], 0\n\t" \
  "buffer_load_dwordx4 a[52:55], %[l16], %[rs], %[gw1] offen offset:1024 sc1\n\t" \
  "v_exp_f32 v214, v214\n\t" \
  "v_cvt_pk_bf16_f32 v226, v212, v213\n\t" \
  "v_exp_f32 v215, v215\n\t" \
  "v_exp_f32 v216, v216\n\t" \
  "v_cvt_pk_bf16_f32 v227, v214, v215\n\t" \
  "v_exp_f32 v217, v217\n\t" \
  "v_exp_f32 v218, v218\n\t" \
  "v_cvt_pk_bf16_f32 v228, v216, v217\n\t" \
  "v_exp_f32 v219, v219\n\t" \
  "v_mfma_f32_32x32x16_bf16 v[96:111], v[248:251], v[224:227], v[96:111]\n\t" \
  "s_waitcnt lgkmcnt(0)\n\t" \
  "v_exp_f32 v220, v220\n\t" \
  "v_cvt_pk_bf16_f32 v229, v218, v219\n\t" \
  "v_exp_f32 v221, v221\n\t" \
  "v_exp_f32 v222, v222\n\t" \
  "v_cvt_pk_bf16_f32 v230, v220, v221\n\t" \
  "v_exp_f32 v223, v223\n\t" \
  "v_exp_f32 v192, v192\n\t" \
  "v_cvt_pk_bf16_f32 v231, v222, v223\n\t" \
  "v_exp_f32 v193, v193\n\t" \
  "v_exp_f32 v194, v194\n\t" \
  "v_mfma_f32_32x32x16_bf16 v[96:111], v[252:255], v[228:231], v[96:111]\n\t" \
  "ds_read_b128 v[252:255], %[a1] offset:3168\n\t" \
  "v_cvt_pk_bf16_f32 v224, v192, v193\n\t" \
  "v_exp_f32 v195, v195\n\t" \
  "v_exp_f32 v196, v196\n\t" \
  "v_cvt_pk_bf16_f32 v225, v194, v195\n\t" \
  "v_exp_f32 v197, v197\n\t" \
  "v_mfma_f32_32x32x16_bf16 v[208:223], v[232:235], v[116:119], 0\n\t" \
  "ds_read_b128 v[232:235], %[ka] offset:4096\n\t" \
  "v_exp_f32 v198, v198\n\t" \
  "v_cvt_pk_bf16_f32 v226, v196, v197\n\t" \
  "v_exp_f32 v199, v199\n\t" \
  "v_exp_f32 v200, v200\n\t" \
  "v_cvt_pk_bf16_f32 v227, v198, v199\n\t" \
  "v_exp_f32 v201, v201\n\t" \
  "v_exp_f32 v202, v202\n\t" \
  "v_cvt_pk_bf16_f32 v228, v200, v201\n\t" \
  "v_exp_f32 v203, v203\n\t" \
  "v_mfma_f32_32x32x16_bf16 v[80:95], v[236:239], v[224:227], v[80:95]\n\t" \
  "ds_read_b128 v[248:251], %[a0] offset:3168\n\t" \
  "v_exp_f32 v204, v204\n\t" \
  "v_cvt_pk_bf16_f32 v229, v202, v203\n\t" \
  "v_exp_f32 v205, v205\n\t" \
  "v_exp_f32 v206, v206\n\t" \
  "v_cvt_pk_bf16_f32 v230, v204, v205\n\t" \
  "v_exp_f32 v207, v207\n\t" \
  "v_exp_f32 v208, v208\n\t" \
  "v_cvt_pk_bf16_f32 v231, v206, v207\n\t" \
  "v_exp_f32 v209, v209\n\t" \
  "v_exp_f32 v210, v210\n\t" \
  "v_mfma_f32_32x32x16_bf16 v[80:95], v[240:243], v[228:231], v[80:95]\n\t" \
  "buffer_load_dwordx4 a[56:59], %[l16], %[rs], %[gw1] offen offset:2048 sc1\n\t" \
  "v_cvt_pk_bf16_f32 v224, v208, v209\n\t" \
  "v_exp_f32 v211, v211\n\t" \
  "v_exp_f32 v212, v212\n\t" \
  "v_cvt_pk_bf16_f32 v225, v210, v211\n\t" \
  "v_exp_f32 v213, v213\n\t" \
  "v_mfma_f32_32x32x16_bf16 v[192:207], v[244:247], v[112:115], 0\n\t" \
  "buffer_load_dwordx4 a[60:63], %[l16], %[rs], %[gw1] offen offset:3072 sc1\n\t" \
  "v_exp_f32 v214, v214\n\t" \
  "v_cvt_pk_bf16_f32 v226, v212, v213\n\t" \
  "v_exp_f32 v215, v215\n\t" \
  "v_exp_f32 v216, v216\n\t" \
  "v_cvt_pk_bf16_f32 v227, v214, v215\n\t" \
  "v_exp_f32 v217, v217\n\t" \
  "v_exp_f32 v218, v218\n\t" \
  "v_cvt_pk_bf16_f32 v228, v216, v217\n\t" \
  "v_exp_f32 v219, v219\n\t" \
  "v_mfma_f32_32x32x16_bf16 v[96:111], v[236:239], v[224:227], v[96:111]\n\t" \
  "s_waitcnt lgkmcnt(0)\n\t" \
  "v_exp_f32 v220, v220\n\t" \
  "v_cvt_pk_bf16_f32 v229, v218, v219\n\t" \
  "v_exp_f32 v221, v221\n\t" \
  "v_exp_f32 v222, v222\n\t" \
  "v_cvt_pk_bf16_f32 v230, v220, v221\n\t" \
  "v_exp_f32 v223, v223\n\t" \
  "v_exp_f32 v192, v192\n\t" \
  "v_cvt_pk_bf16_f32 v231, v222, v223\n\t" \
  "v_exp_f32 v193, v193\n\t" \
  "v_exp_f32 v194, v194\n\t" \
  "v_mfma_f32_32x32x16_bf16 v[96:111], v[240:243], v[228:231], v[96:111]\n\t" \
  "ds_read_b128 v[240:243], %[a1] offset:4224\n\t" \
  "v_cvt_pk_bf16_f32 v224, v192, v193\n\t" \
  "v_exp_f32 v195, v195\n\t" \
  "v_exp_f32 v196, v196\n\t" \
  "v_cvt_pk_bf16_f32 v225, v194, v195\n\t" \
  "v_exp_f32 v197, v197\n\t" \
  "v_mfma_f32_32x32x16_bf16 v[208:223], v[244:247], v[116:119], 0\n\t" \
  "ds_read_b128 v[244:247], %[ka] offset:5120\n\t" \
  "v_exp_f32 v198, v198\n\t" \
  "v_cvt_pk_bf16_f32 v226, v196, v197\n\t" \
  "v_exp_f32 v199, v199\n\t" \
  "v_exp_f32 v200, v200\n\t" \
  "v_cvt_pk_bf16_f32 v227, v198, v199\n\t" \
  "v_exp_f32 v201, v201\n\t" \
  "v_exp_f32 v202, v202\n\t" \
  "v_cvt_pk_bf16_f32 v228, v200, v201\n\t" \
  "v_exp_f32 v203, v203\n\t" \
  "v_mfma_f32_32x32x16_bf16 v[80:95], v[248:251], v[224:227], v[80:95]\n\t" \
  "ds_read_b128 v[236:239], %[a0] offset:4224\n\t" \
  "v_exp_f32 v204, v204\n\t" \
  "v_cvt_pk_bf16_f32 v229, v202, v203\n\t" \
  "v_exp_f32 v205, v205\n\t" \
  "v_exp_f32 v206, v206\n\t" \
  "v_cvt_pk_bf16_f32 v230, v204, v205\n\t" \
  "v_exp_f32 v207, v207\n\t" \
  "v_exp_f32 v208, v208\n\t" \
  "v_cvt_pk_bf16_f32 v231, v206, v207\n\t" \
  "v_exp_f32 v209, v209\n\t" \
  "v_exp_f32 v210, v210\n\t" \
  "s_add_u32 %[gw1], %[gw1], 4096\n\t" \
  "v_mfma_f32_32x32x16_bf16 v[80:95], v[252:255], v[228:231], v[80:95]\n\t" \
  "buffer_load_dwordx4 a[64:67], %[l16], %[rs], %[gw1] offen sc1\n\t" \
  "v_cvt_pk_bf16_f32 v224, v208, v209\n\t" \
  "v_exp_f32 v211, v211\n\t" \
  "v_exp_f32 v212, v212\n\t" \
  "v_cvt_pk_bf16_f32 v225, v210, v211\n\t" \
  "v_exp_f32 v213, v213\n\t" \
  "v_mfma_f32_32x32x16_bf16 v[192:207], v[232:235], v[112:115], 0\n\t" \
  "buffer_load_dwordx4 a[68:71], %[l16], %[rs], %[gw1] offen offset:1024 sc1\n\t" \
  "v_exp_f32 v214, v214\n\t" \
  "v_cvt_pk_bf16_f32 v226, v212, v213\n\t" \
  "v_exp_f32 v215, v215\n\t" \
  "v_exp_f32 v216, v216\n\t" \
  "v_cvt_pk_bf16_f32 v227, v214, v215\n\t" \
  "v_exp_f32 v217, v217\n\t" \
  "v_exp_f32 v218, v218\n\t" \
  "v_cvt_pk_bf16_f32 v228, v216, v217\n\t" \
  "v_exp_f32 v219, v219\n\t" \
  "v_mfma_f32_32x32x16_bf16 v[96:111], v[248:251], v[224:227], v[96:111]\n\t" \
  "s_waitcnt lgkmcnt(0)\n\t" \
  "v_exp_f32 v220, v220\n\t" \
  "v_cvt_pk_bf16_f32 v229, v218, v219\n\t" \
  "v_exp_f32 v221, v221\n\t" \
  "v_exp_f32 v222, v222\n\t" \
  "v_cvt_pk_bf16_f32 v230, v220, v221\n\t" \
  "v_exp_f32 v223, v223\n\t" \
  "v_exp_f32 v192, v192\n\t" \
  "v_cvt_pk_bf16_f32 v231, v222, v223\n\t" \
  "v_exp_f32 v193, v193\n\t" \
  "v_exp_f32 v194, v194\n\t" \
  "v_mfma_f32_32x32x16_bf16 v[96:111], v[252:255], v[228:231], v[96:111]\n\t" \
  "ds_read_b128 v[252:255], %[a1] offset:5280\n\t" \
  "v_cvt_pk_bf16_f32 v224, v192, v193\n\t" \
  "v_exp_f32 v195, v195\n\t" \
  "v_exp_f32 v196, v196\n\t" \
  "v_cvt_pk_bf16_f32 v225, v194, v195\n\t" \
  "v_exp_f32 v197, v197\n\t" \
  "v_mfma_f32_32x32x16_bf16 v[208:223], v[232:235], v[116:119], 0\n\t" \
  "ds_read_b128 v[232:235], %[ka] offset:6144\n\t" \
  "v_exp_f32 v198, v198\n\t" \
  "v_cvt_pk_bf16_f32 v226, v196, v197\n\t" \
  "v_exp_f32 v199, v199\n\t" \
  "v_exp_f32 v200, v200\n\t" \
  "v_cvt_pk_bf16_f32 v227, v198, v199\n\t" \
  "v_exp_f32 v201, v201\n\t" \
  "v_exp_f32 v202, v202\n\t" \
  "v_cvt_pk_bf16_f32 v228, v200, v201\n\t" \
  "v_exp_f32 v203, v203\n\t" \
  "v_mfma_f32_32x32x16_bf16 v[80:95], v[236:239], v[224:227], v[80:95]\n\t" \
  "ds_read_b128 v[248:251], %[a0] offset:5280\n\t" \
  "v_exp_f32 v204, v204\n\t" \
  "v_cvt_pk_bf16_f32 v229, v202, v203\n\t" \
  "v_exp_f32 v205, v205\n\t" \
  "v_exp_f32 v206, v206\n\t" \
  "v_cvt_pk_bf16_f32 v230, v204, v205\n\t" \
  "v_exp_f32 v207, v207\n\t" \
  "v_exp_f32 v208, v208\n\t" \
  "v_cvt_pk_bf16_f32 v231, v206, v207\n\t" \
  "v_exp_f32 v209, v209\n\t" \
  "v_exp_f32 v210, v210\n\t" \
  "v_mfma_f32_32x32x16_bf16 v[80:95], v[240:243], v[228:231], v[80:95]\n\t" \
  "buffer_load_dwordx4 a[72:75], %[l16], %[rs], %[gw1] offen offset:2048 sc1\n\t" \
  "v_cvt_pk_bf16_f32 v224, v208, v209\n\t" \
  "v_exp_f32 v211, v211\n\t" \
  "v_exp_f32 v212, v212\n\t" \
  "v_cvt_pk_bf16_f32 v225, v210, v211\n\t" \
  "v_exp_f32 v213, v213\n\t" \
  "v_mfma_f32_32x32x16_bf16 v[192:207], v[244:247], v[112:115], 0\n\t" \
  "buffer_load_dwordx4 a[76:79], %[l16], %[rs], %[gw1] offen offset:3072 sc1\n\t" \
  "v_exp_f32 v214, v214\n\t" \
  "v_cvt_pk_bf16_f32 v226, v212, v213\n\t" \
  "v_exp_f32 v215, v215\n\t" \
  "v_exp_f32 v216, v216\n\t" \
  "v_cvt_pk_bf16_f32 v227, v214, v215\n\t" \
  "v_exp_f32 v217, v217\n\t" \
  "v_exp_f32 v218, v218\n\t" \
  "v_cvt_pk_bf16_f32 v228, v216, v217\n\t" \
  "v_exp_f32 v219, v219\n\t" \
  "v_mfma_f32_32x32x16_bf16 v[96:111], v[236:239], v[224:227], v[96:111]\n\t" \
  "s_waitcnt lgkmcnt(0)\n\t" \
  "v_exp_f32 v220, v220\n\t" \
  "v_cvt_pk_bf16_f32 v229, v218, v219\n\t" \
  "v_exp_f32 v221, v221\n\t" \
  "v_exp_f32 v222, v222\n\t" \
  "v_cvt_pk_bf16_f32 v230, v220, v221\n\t" \
  "v_exp_f32 v223, v223\n\t" \
  "v_exp_f32 v192, v192\n\t" \
  "v_cvt_pk_bf16_f32 v231, v222, v223\n\t" \
  "v_exp_f32 v193, v193\n\t" \
  "v_exp_f32 v194, v194\n\t" \
  "v_mfma_f32_32x32x16_bf16 v[96:111], v[240:243], v[228:231], v[96:111]\n\t" \
  "ds_read_b128 v[240:243], %[a1] offset:6336\n\t" \
  "v_cvt_pk_bf16_f32 v224, v192, v193\n\t" \
  "v_exp_f32 v195, v195\n\t" \
  "v_exp_f32 v196, v196\n\t" \
  "v_cvt_pk_bf16_f32 v225, v194, v195\n\t" \
  "v_exp_f32 v197, v197\n\t" \
  "v_mfma_f32_32x32x16_bf16 v[208:223], v[244:247], v[116:119], 0\n\t" \
  "ds_read_b128 v[244:247], %[ka] offset:7168\n\t" \
  "v_exp_f32 v198, v198\n\t" \
  "v_cvt_pk_bf16_f32 v226, v196, v197\n\t" \
  "v_exp_f32 v199, v199\n\t" \
  "v_exp_f32 v200, v200\n\t" \
  "v_cvt_pk_bf16_f32 v227, v198, v199\n\t" \
  "v_exp_f32 v201, v201\n\t" \
  "v_exp_f32 v202, v202\n\t" \
  "v_cvt_pk_bf16_f32 v228, v200, v201\n\t" \
  "v_exp_f32 v203, v203\n\t" \
  "v_mfma_f32_32x32x16_bf16 v[80:95], v[248:251], v[224:227], v[80:95]\n\t" \
  "ds_read_b128 v[236:239], %[a0] offset:6336\n\t" \
  "v_exp_f32 v204, v204\n\t" \
  "v_cvt_pk_bf16_f32 v229, v202, v203\n\t" \
  "v_exp_f32 v205, v205\n\t" \
  "v_exp_f32 v206, v206\n\t" \
  "v_cvt_pk_bf16_f32 v230, v204, v205\n\t" \
  "v_exp_f32 v207, v207\n\t" \
  "v_exp_f32 v208, v208\n\t" \
  "v_cvt_pk_bf16_f32 v231, v206, v207\n\t" \
  "v_exp_f32 v209, v209\n\t" \
  "v_exp_f32 v210, v210\n\t" \
  "s_add_u32 %[gw1], %[gw1], 4096\n\t" \
  "v_mfma_f32_32x32x16_bf16 v[80:95], v[252:255], v[228:231], v[80:95]\n\t" \
  "buffer_load_dwordx4 a[80:83], %[l16], %[rs], %[gw1] offen sc1\n\t" \
  "v_cvt_pk_bf16_f32 v224, v208, v209\n\t" \
  "v_exp_f32 v211, v211\n\t" \
  "v_exp_f32 v212, v212\n\t" \
  "v_cvt_pk_bf16_f32 v225, v210, v211\n\t" \
  "v_exp_f32 v213, v213\n\t" \
  "v_mfma_f32_32x32x16_bf16 v[192:207], v[232:235], v[112:115], 0\n\t" \
  "buffer_load_dwordx4 a[84:87], %[l16], %[rs], %[gw1] offen offset:1024 sc1\n\t" \
  "v_exp_f32 v214, v214\n\t" \
  "v_cvt_pk_bf16_f32 v226, v212, v213\n\t" \
  "v_exp_f32 v215, v215\n\t" \
  "v_exp_f32 v216, v216\n\t" \
  "v_cvt_pk_bf16_f32 v227, v214, v215\n\t" \
  "v_exp_f32 v217, v217\n\t" \
  "v_exp_f32 v218, v218\n\t" \
  "v_cvt_pk_bf16_f32 v228, v216, v217\n\t" \
  "v_exp_f32 v219, v219\n\t" \
  "v_mfma_f32_32x32x16_bf16 v[96:111], v[248:251], v[224:227], v[96:111]\n\t" \
  "s_waitcnt lgkmcnt(0)\n\t" \
  "v_exp_f32 v220, v220\n\t" \
  "v_cvt_pk_bf16_f32 v229, v218, v219\n\t" \
  "v_exp_f32 v221, v221\n\t" \
  "v_exp_f32 v222, v222\n\t" \
  "v_cvt_pk_bf16_f32 v230, v220, v221\n\t" \
  "v_exp_f32 v223, v223\n\t" \
  "v_exp_f32 v192, v192\n\t" \
  "v_cvt_pk_bf16_f32 v231, v222, v223\n\t" \
  "v_exp_f32 v193, v193\n\t" \
  "v_exp_f32 v194, v194\n\t" \
  "v_mfma_f32_32x32x16_bf16 v[96:111], v[252:255], v[228:231], v[96:111]\n\t" \
  "ds_read_b128 v[252:255], %[a1] offset:7392\n\t" \
  "v_cvt_pk_bf16_f32 v224, v192, v193\n\t" \
  "v_exp_f32 v195, v195\n\t" \
  "v_exp_f32 v196, v196\n\t" \
  "v_cvt_pk_bf16_f32 v225, v194, v195\n\t" \
  "v_exp_f32 v197, v197\n\t" \
  "v_mfma_f32_32x32x16_bf16 v[208:223], v[232:235], v[116:119], 0\n\t" \
  "ds_read_b128 v[232:235], %[ka] offset:8192\n\t" \
  "v_exp_f32 v198, v198\n\t" \
  "v_cvt_pk_bf16_f32 v226, v196, v197\n\t" \
  "v_exp_f32 v199, v199\n\t" \
  "v_exp_f32 v200, v200\n\t" \
  "v_cvt_pk_bf16_f32 v227, v198, v199\n\t" \
  "v_exp_f32 v201, v201\n\t" \
  "v_exp_f32 v202, v202\n\t" \
  "v_cvt_pk_bf16_f32 v228, v200, v201\n\t" \
  "v_exp_f32 v203, v203\n\t" \
  "v_mfma_f32_32x32x16_bf16 v[80:95], v[236:239], v[224:227], v[80:95]\n\t" \
  "ds_read_b128 v[248:251], %[a0] offset:7392\n\t" \
  "v_exp_f32 v204, v204\n\t" \
  "v_cvt_pk_bf16_f32 v229, v202, v203\n\t" \
  "v_exp_f32 v205, v205\n\t" \
  "v_exp_f32 v206, v206\n\t" \
  "v_cvt_pk_bf16_f32 v230, v204, v205\n\t" \
  "v_exp_f32 v207, v207\n\t" \
  "v_exp_f32 v208, v208\n\t" \
  "v_cvt_pk_bf16_f32 v231, v206, v207\n\t" \
  "v_exp_f32 v209, v209\n\t" \
  "v_exp_f32 v210, v210\n\t" \
  "v_mfma_f32_32x32x16_bf16 v[80:95], v[240:243], v[228:231], v[80:95]\n\t" \
  "buffer_load_dwordx4 a[88:91], %[l16], %[rs], %[gw1] offen offset:2048 sc1\n\t" \
  "v_cvt_pk_bf16_f32 v224, v208, v209\n\t" \
  "v_exp_f32 v211, v211\n\t" \
  "v_exp_f32 v212, v212\n\t" \
  "v_cvt_pk_bf16_f32 v225, v210, v211\n\t" \
  "v_exp_f32 v213, v213\n\t" \
  "v_mfma_f32_32x32x16_bf16 v[192:207], v[244:247], v[112:115], 0\n\t" \
  "buffer_load_dwordx4 a[92:95], %[l16], %[rs], %[gw1] offen offset:3072 sc1\n\t" \
  "v_exp_f32 v214, v214\n\t" \
  "v_cvt_pk_bf16_f32 v226, v212, v213\n\t" \
  "v_exp_f32 v215, v215\n\t" \
  "v_exp_f32 v216, v216\n\t" \
  "v_cvt_pk_bf16_f32 v227, v214, v215\n\t" \
  "v_exp_f32 v217, v217\n\t" \
  "v_exp_f32 v218, v218\n\t" \
  "v_cvt_pk_bf16_f32 v228, v216, v217\n\t" \
  "v_exp_f32 v219, v219\n\t" \
  "v_mfma_f32_32x32x16_bf16 v[96:111], v[236:239], v[224:227], v[96:111]\n\t" \
  "s_waitcnt lgkmcnt(0)\n\t" \
  "v_exp_f32 v220, v220\n\t" \
  "v_cvt_pk_bf16_f32 v229, v218, v219\n\t" \
  "v_exp_f32 v221, v221\n\t" \
  "v_exp_f32 v222, v222\n\t" \
  "v_cvt_pk_bf16_f32 v230, v220, v221\n\t" \
  "v_exp_f32 v223, v223\n\t" \
  "v_exp_f32 v192, v192\n\t" \
  "v_cvt_pk_bf16_f32 v231, v222, v223\n\t" \
  "v_exp_f32 v193, v193\n\t" \
  "v_exp_f32 v194, v194\n\t" \
  "v_mfma_f32_32x32x16_bf16 v[96:111], v[240:243], v[228:231], v[96:111]\n\t" \
  "ds_read_b128 v[240:243], %[a1] offset:8448\n\t" \
  "v_cvt_pk_bf16_f32 v224, v192, v193\n\t" \
  "v_exp_f32 v195, v195\n\t" \
  "v_exp_f32 v196, v196\n\t" \
  "v_cvt_pk_bf16_f32 v225, v194, v195\n\t" \
  "v_exp_f32 v197, v197\n\t" \
  "v_mfma_f32_32x32x16_bf16 v[208:223], v[244:247], v[116:119], 0\n\t" \
  "ds_read_b128 v[244:247], %[ka] offset:9216\n\t" \
  "v_exp_f32 v198, v198\n\t" \
  "v_cvt_pk_bf16_f32 v226, v196, v197\n\t" \
  "v_exp_f32 v199, v199\n\t" \
  "v_exp_f32 v200, v200\n\t" \
  "v_cvt_pk_bf16_f32 v227, v198, v199\n\t" \
  "v_exp_f32 v201, v201\n\t" \
  "v_exp_f32 v202, v202\n\t" \
  "v_cvt_pk_bf16_f32 v228, v200, v201\n\t" \
  "v_exp_f32 v203, v203\n\t" \
  "v_mfma_f32_32x32x16_bf16 v[80:95], v[248:251], v[224:227], v[80:95]\n\t" \
  "ds_read_b128 v[236:239], %[a0] offset:8448\n\t" \
  "v_exp_f32 v204, v204\n\t" \
  "v_cvt_pk_bf16_f32 v229, v202, v203\n\t" \
  "v_exp_f32 v205, v205\n\t" \
  "v_exp_f32 v206, v206\n\t" \
  "v_cvt_pk_bf16_f32 v230, v204, v205\n\t" \
  "v_exp_f32 v207, v207\n\t" \
  "v_exp_f32 v208, v208\n\t" \
  "v_cvt_pk_bf16_f32 v231, v206, v207\n\t" \
  "v_exp_f32 v209, v209\n\t" \
  "v_exp_f32 v210, v210\n\t" \
  "s_add_u32 %[gw1], %[gw1], 4096\n\t" \
  "v_mfma_f32_32x32x16_bf16 v[80:95], v[252:255], v[228:231], v[80:95]\n\t" \
  "buffer_load_dwordx4 a[96:99], %[l16], %[rs], %[gw1] offen sc1\n\t" \
  "v_cvt_pk_bf16_f32 v224, v208, v209\n\t" \
  "v_exp_f32 v211, v211\n\t" \
  "v_exp_f32 v212, v212\n\t" \
  "v_cvt_pk_bf16_f32 v225, v210, v211\n\t" \
  "v_exp_f32 v213, v213\n\t" \
  "v_mfma_f32_32x32x16_bf16 v[192:207], v[232:235], v[112:115], 0\n\t" \
  "buffer_load_dwordx4 a[100:103], %[l16], %[rs], %[gw1] offen offset:1024 sc1\n\t" \
  "v_exp_f32 v214, v214\n\t" \
  "v_cvt_pk_bf16_f32 v226, v212, v213\n\t" \
  "v_exp_f32 v215, v215\n\t" \
  "v_exp_f32 v216, v216\n\t" \
  "v_cvt_pk_bf16_f32 v227, v214, v215\n\t" \
  "v_exp_f32 v217, v217\n\t" \
  "v_exp_f32 v218, v218\n\t" \
  "v_cvt_pk_bf16_f32 v228, v216, v217\n\t" \
  "v_exp_f32 v219, v219\n\t" \
  "v_mfma_f32_32x32x16_bf16 v[96:111], v[248:251], v[224:227], v[96:111]\n\t" \
  "s_waitcnt lgkmcnt(0)\n\t" \
  "v_exp_f32 v220, v220\n\t" \
  "v_cvt_pk_bf16_f32 v229, v218, v219\n\t" \
  "v_exp_f32 v221, v221\n\t" \
  "v_exp_f32 v222, v222\n\t" \
  "v_cvt_pk_bf16_f32 v230, v220, v221\n\t" \
  "v_exp_f32 v223, v223\n\t" \
  "v_add_u32 %[ka], 8192, %[ka]\n\t" \
  "v_add_u32 %[a0], 8448, %[a0]\n\t" \
  "v_add_u32 %[a1], 8448, %[a1]\n\t" \
  "v_exp_f32 v192, v192\n\t" \
  "v_cvt_pk_bf16_f32 v231, v222, v223\n\t" \
  "v_exp_f32 v193, v193\n\t" \
  "v_exp_f32 v194, v194\n\t" \
  "v_mfma_f32_32x32x16_bf16 v[96:111], v[252:255], v[228:231], v[96:111]\n\t" \
  "ds_read_b128 v[252:255], %[a1] offset:1056\n\t" \
  "v_cvt_pk_bf16_f32 v224, v192, v193\n\t" \
  "v_exp_f32 v195, v195\n\t" \
  "v_exp_f32 v196, v196\n\t" \
  "v_cvt_pk_bf16_f32 v225, v194, v195\n\t" \
  "v_exp_f32 v197, v197\n\t" \
  "v_mfma_f32_32x32x16_bf16 v[208:223], v[232:235], v[116:119], 0\n\t" \
  "ds_read_b128 v[232:235], %[ka] offset:2048\n\t" \
  "v_exp_f32 v198, v198\n\t" \
  "v_cvt_pk_bf16_f32 v226, v196, v197\n\t" \
  "v_exp_f32 v199, v199\n\t" \
  "v_exp_f32 v200, v200\n\t" \
  "v_cvt_pk_bf16_f32 v227, v198, v199\n\t" \
  "v_exp_f32 v201, v201\n\t" \
  "v_exp_f32 v202, v202\n\t" \
  "v_cvt_pk_bf16_f32 v228, v200, v201\n\t" \
  "v_exp_f32 v203, v203\n\t" \
  "v_mfma_f32_32x32x16_bf16 v[80:95], v[236:239], v[224:227], v[80:95]\n\t" \
  "ds_read_b128 v[248:251], %[a0] offset:1056\n\t" \
  "v_exp_f32 v204, v204\n\t" \
  "v_cvt_pk_bf16_f32 v229, v202, v203\n\t" \
  "v_exp_f32 v205, v205\n\t" \
  "v_exp_f32 v206, v206\n\t" \
  "v_cvt_pk_bf16_f32 v230, v204, v205\n\t" \
  "v_exp_f32 v207, v207\n\t" \
  "v_exp_f32 v208, v208\n\t" \
  "v_cvt_pk_bf16_f32 v231, v206, v207\n\t" \
  "v_exp_f32 v209, v209\n\t" \
  "v_exp_f32 v210, v210\n\t" \
  "v_mfma_f32_32x32x16_bf16 v[80:95], v[240:243], v[228:231], v[80:95]\n\t" \
  "buffer_load_dwordx4 a[104:107], %[l16], %[rs], %[gw1] offen offset:2048 sc1\n\t" \
  "v_cvt_pk_bf16_f32 v224, v208, v209\n\t" \
  "v_exp_f32 v211, v211\n\t" \
  "v_exp_f32 v212, v212\n\t" \
  "v_cvt_pk_bf16_f32 v225, v210, v211\n\t" \
  "v_exp_f32 v213, v213\n\t" \
  "v_mfma_f32_32x32x16_bf16 v[192:207], v[244:247], v[112:115], 0\n\t" \
  "buffer_load_dwordx4 a[108:111], %[l16], %[rs], %[gw1] offen offset:3072 sc1\n\t" \
  "v_exp_f32 v214, v214\n\t" \
  "v_cvt_pk_bf16_f32 v226, v212, v213\n\t" \
  "v_exp_f32 v215, v215\n\t" \
  "v_exp_f32 v216, v216\n\t" \
  "v_cvt_pk_bf16_f32 v227, v214, v215\n\t" \
  "v_exp_f32 v217, v217\n\t" \
  "v_exp_f32 v218, v218\n\t" \
  "v_cvt_pk_bf16_f32 v228, v216, v217\n\t" \
  "v_exp_f32 v219, v219\n\t" \
  "v_mfma_f32_32x32x16_bf16 v[96:111], v[236:239], v[224:227], v[96:111]\n\t" \
  "s_waitcnt lgkmcnt(0)\n\t" \
  "v_exp_f32 v220, v220\n\t" \
  "v_cvt_pk_bf16_f32 v229, v218, v219\n\t" \
  "v_exp_f32 v221, v221\n\t" \
  "v_exp_f32 v222, v222\n\t" \
  "v_cvt_pk_bf16_f32 v230, v220, v221\n\t" \
  "v_exp_f32 v223, v223\n\t" \
  "v_exp_f32 v192, v192\n\t" \
  "v_cvt_pk_bf16_f32 v231, v222, v223\n\t" \
  "v_exp_f32 v193, v193\n\t" \
  "v_exp_f32 v194, v194\n\t" \
  "v_mfma_f32_32x32x16_bf16 v[96:111], v[240:243], v[228:231], v[96:111]\n\t" \
  "ds_read_b128 v[240:243], %[a1] offset:2112\n\t" \
  "v_cvt_pk_bf16_f32 v224, v192, v193\n\t" \
  "v_exp_f32 v195, v195\n\t" \
  "v_exp_f32 v196, v196\n\t" \
  "v_cvt_pk_bf16_f32 v225, v194, v195\n\t" \
  "v_exp_f32 v197, v197\n\t" \
  "v_mfma_f32_32x32x16_bf16 v[208:223], v[244:247], v[116:119], 0\n\t" \
  "ds_read_b128 v[244:247], %[ka] offset:3072\n\t" \
  "v_exp_f32 v198, v198\n\t" \
  "v_cvt_pk_bf16_f32 v226, v196, v197\n\t" \
  "v_exp_f32 v199, v199\n\t" \
  "v_exp_f32 v200, v200\n\t" \
  "v_cvt_pk_bf16_f32 v227, v198, v199\n\t" \
  "v_exp_f32 v201, v201\n\t" \
  "v_exp_f32 v202, v202\n\t" \
  "v_cvt_pk_bf16_f32 v228, v200, v201\n\t" \
  "v_exp_f32 v203, v203\n\t" \
  "v_mfma_f32_32x32x16_bf16 v[80:95], v[248:251], v[224:227], v[80:95]\n\t" \
  "ds_read_b128 v[236:239], %[a0] offset:2112\n\t" \
  "v_exp_f32 v204, v204\n\t" \
  "v_cvt_pk_bf16_f32 v229, v202, v203\n\t" \
  "v_exp_f32 v205, v205\n\t" \
  "v_exp_f32 v206, v206\n\t" \
  "v_cvt_pk_bf16_f32 v230, v204, v205\n\t" \
  "v_exp_f32 v207, v207\n\t" \
  "v_exp_f32 v208, v208\n\t" \
  "v_cvt_pk_bf16_f32 v231, v206, v207\n\t" \
  "v_exp_f32 v209, v209\n\t" \
  "v_exp_f32 v210, v210\n\t" \
  "s_add_u32 %[gw1], %[gw1], 4096\n\t" \
  "v_mfma_f32_32x32x16_bf16 v[80:95], v[252:255], v[228:231], v[80:95]\n\t" \
  "buffer_load_dwordx4 a[112:115], %[l16], %[rs], %[gw1] offen sc1\n\t" \
  "v_cvt_pk_bf16_f32 v224, v208, v209\n\t" \
  "v_exp_f32 v211, v211\n\t" \
  "v_exp_f32 v212, v212\n\t" \
  "v_cvt_pk_bf16_f32 v225, v210, v211\n\t" \
  "v_exp_f32 v213, v213\n\t" \
  "v_mfma_f32_32x32x16_bf16 v[192:207], v[232:235], v[112:115], 0\n\t" \
  "buffer_load_dwordx4 a[116:119], %[l16], %[rs], %[gw1] offen offset:1024 sc1\n\t" \
  "v_exp_f32 v214, v214\n\t" \
  "v_cvt_pk_bf16_f32 v226, v212, v213\n\t" \
  "v_exp_f32 v215, v215\n\t" \
  "v_exp_f32 v216, v216\n\t" \
  "v_cvt_pk_bf16_f32 v227, v214, v215\n\t" \
  "v_exp_f32 v217, v217\n\t" \
  "v_exp_f32 v218, v218\n\t" \
  "v_cvt_pk_bf16_f32 v228, v216, v217\n\t" \
  "v_exp_f32 v219, v219\n\t" \
  "v_mfma_f32_32x32x16_bf16 v[96:111], v[248:251], v[224:227], v[96:111]\n\t" \
  "s_waitcnt lgkmcnt(0)\n\t" \
  "v_exp_f32 v220, v220\n\t" \
  "v_cvt_pk_bf16_f32 v229, v218, v219\n\t" \
  "v_exp_f32 v221, v221\n\t" \
  "v_exp_f32 v222, v222\n\t" \
  "v_cvt_pk_bf16_f32 v230, v220, v221\n\t" \
  "v_exp_f32 v223, v223\n\t" \
  "v_exp_f32 v192, v192\n\t" \
  "v_cvt_pk_bf16_f32 v231, v222, v223\n\t" \
  "v_exp_f32 v193, v193\n\t" \
  "v_exp_f32 v194, v194\n\t" \
  "v_mfma_f32_32x32x16_bf16 v[96:111], v[252:255], v[228:231], v[96:111]\n\t" \
  "ds_read_b128 v[252:255], %[a1] offset:3168\n\t" \
  "v_cvt_pk_bf16_f32 v224, v192, v193\n\t" \
  "v_exp_f32 v195, v195\n\t" \
  "v_exp_f32 v196, v196\n\t" \
  "v_cvt_pk_bf16_f32 v225, v194, v195\n\t" \
  "v_exp_f32 v197, v197\n\t" \
  "v_mfma_f32_32x32x16_bf16 v[208:223], v[232:235], v[116:119], 0\n\t" \
  "ds_read_b128 v[232:235], %[ka] offset:4096\n\t" \
  "v_exp_f32 v198, v198\n\t" \
  "v_cvt_pk_bf16_f32 v226, v196, v197\n\t" \
  "v_exp_f32 v199, v199\n\t" \
  "v_exp_f32 v200, v200\n\t" \
  "v_cvt_pk_bf16_f32 v227, v198, v199\n\t" \
  "v_exp_f32 v201, v201\n\t" \
  "v_exp_f32 v202, v202\n\t" \
  "v_cvt_pk_bf16_f32 v228, v200, v201\n\t" \
  "v_exp_f32 v203, v203\n\t" \
  "v_mfma_f32_32x32x16_bf16 v[80:95], v[236:239], v[224:227], v[80:95]\n\t" \
  "ds_read_b128 v[248:251], %[a0] offset:3168\n\t" \
  "v_exp_f32 v204, v204\n\t" \
  "v_cvt_pk_bf16_f32 v229, v202, v203\n\t" \
  "v_exp_f32 v205, v205\n\t" \
  "v_exp_f32 v206, v206\n\t" \
  "v_cvt_pk_bf16_f32 v230, v204, v205\n\t" \
  "v_exp_f32 v207, v207\n\t" \
  "v_exp_f32 v208, v208\n\t" \
  "v_cvt_pk_bf16_f32 v231, v206, v207\n\t" \
  "v_exp_f32 v209, v209\n\t" \
  "v_exp_f32 v210, v210\n\t" \
  "v_mfma_f32_32x32x16_bf16 v[80:95], v[240:243], v[228:231], v[80:95]\n\t" \
  "buffer_load_dwordx4 a[120:123], %[l16], %[rs], %[gw1] offen offset:2048 sc1\n\t" \
  "v_cvt_pk_bf16_f32 v224, v208, v209\n\t" \
  "v_exp_f32 v211, v211\n\t" \
  "v_exp_f32 v212, v212\n\t" \
  "v_cvt_pk_bf16_f32 v225, v210, v211\n\t" \
  "v_exp_f32 v213, v213\n\t" \
  "v_mfma_f32_32x32x16_bf16 v[192:207], v[244:247], v[112:115], 0\n\t" \
  "buffer_load_dwordx4 a[124:127], %[l16], %[rs], %[gw1] offen offset:3072 sc1\n\t" \
  "v_exp_f32 v214, v214\n\t" \
  "v_cvt_pk_bf16_f32 v226, v212, v213\n\t" \
  "v_exp_f32 v215, v215\n\t" \
  "v_exp_f32 v216, v216\n\t" \
  "v_cvt_pk_bf16_f32 v227, v214, v215\n\t" \
  "v_exp_f32 v217, v217\n\t" \
  "v_exp_f32 v218, v218\n\t" \
  "v_cvt_pk_bf16_f32 v228, v216, v217\n\t" \
  "v_exp_f32 v219, v219\n\t" \
  "v_mfma_f32_32x32x16_bf16 v[96:111], v[236:239], v[224:227], v[96:111]\n\t" \
  "s_waitcnt lgkmcnt(0)\n\t" \
  "v_exp_f32 v220, v220\n\t" \
  "v_cvt_pk_bf16_f32 v229, v218, v219\n\t" \
  "v_exp_f32 v221, v221\n\t" \
  "v_exp_f32 v222, v222\n\t" \
  "v_cvt_pk_bf16_f32 v230, v220, v221\n\t" \
  "v_exp_f32 v223, v223\n\t" \
  "v_exp_f32 v192, v192\n\t" \
  "v_cvt_pk_bf16_f32 v231, v222, v223\n\t" \
  "v_exp_f32 v193, v193\n\t" \
  "v_exp_f32 v194, v194\n\t" \
  "v_mfma_f32_32x32x16_bf16 v[96:111], v[240:243], v[228:231], v[96:111]\n\t" \
  "ds_read_b128 v[240:243], %[a1] offset:4224\n\t" \
  "v_cvt_pk_bf16_f32 v224, v192, v193\n\t" \
  "v_exp_f32 v195, v195\n\t" \
  "v_exp_f32 v196, v196\n\t" \
  "v_cvt_pk_bf16_f32 v225, v194, v195\n\t" \
  "v_exp_f32 v197, v197\n\t" \
  "v_mfma_f32_32x32x16_bf16 v[208:223], v[244:247], v[116:119], 0\n\t" \
  "ds_read_b128 v[244:247], %[ka] offset:5120\n\t" \
  "v_exp_f32 v198, v198\n\t" \
  "v_cvt_pk_bf16_f32 v226, v196, v197\n\t" \
  "v_exp_f32 v199, v199\n\t" \
  "v_exp_f32 v200, v200\n\t" \
  "v_cvt_pk_bf16_f32 v227, v198, v199\n\t" \
  "v_exp_f32 v201, v201\n\t" \
  "v_exp_f32 v202, v202\n\t" \
  "v_cvt_pk_bf16_f32 v228, v200, v201\n\t" \
  "v_exp_f32 v203, v203\n\t" \
  "v_mfma_f32_32x32x16_bf16 v[80:95], v[248:251], v[224:227], v[80:95]\n\t" \
  "ds_read_b128 v[236:239], %[a0] offset:4224\n\t" \
  "v_exp_f32 v204, v204\n\t" \
  "v_cvt_pk_bf16_f32 v229, v202, v203\n\t" \
  "v_exp_f32 v205, v205\n\t" \
  "v_exp_f32 v206, v206\n\t" \
  "v_cvt_pk_bf16_f32 v230, v204, v205\n\t" \
  "v_exp_f32 v207, v207\n\t" \
  "v_exp_f32 v208, v208\n\t" \
  "v_cvt_pk_bf16_f32 v231, v206, v207\n\t" \
  "v_exp_f32 v209, v209\n\t" \
  "v_exp_f32 v210, v210\n\t" \
  "v_mfma_f32_32x32x16_bf16 v[80:95], v[252:255], v[228:231], v[80:95]\n\t" \
  "buffer_load_dwordx4 v[120:123], %[l16], %[rs], %[wa] offen sc1\n\t" \
  "v_cvt_pk_bf16_f32 v224, v208, v209\n\t" \
  "v_exp_f32 v211, v211\n\t" \
  "v_exp_f32 v212, v212\n\t" \
  "v_cvt_pk_bf16_f32 v225, v210, v211\n\t" \
  "v_exp_f32 v213, v213\n\t" \
  "v_mfma_f32_32x32x16_bf16 v[192:207], v[232:235], v[112:115], 0\n\t" \
  "buffer_load_dwordx4 v[124:127], %[l16], %[rs], %[wb] offen sc1\n\t" \
  "v_exp_f32 v214, v214\n\t" \
  "v_cvt_pk_bf16_f32 v226, v212, v213\n\t" \
  "v_exp_f32 v215, v215\n\t" \
  "v_exp_f32 v216, v216\n\t" \
  "v_cvt_pk_bf16_f32 v227, v214, v215\n\t" \
  "v_exp_f32 v217, v217\n\t" \
  "v_exp_f32 v218, v218\n\t" \
  "v_cvt_pk_bf16_f32 v228, v216, v217\n\t" \
  "v_exp_f32 v219, v219\n\t" \
  "v_mfma_f32_32x32x16_bf16 v[96:111], v[248:251], v[224:227], v[96:111]\n\t" \
  "s_waitcnt lgkmcnt(0)\n\t" \
  "v_exp_f32 v220, v220\n\t" \
  "v_cvt_pk_bf16_f32 v229, v218, v219\n\t" \
  "v_exp_f32 v221, v221\n\t" \
  "v_exp_f32 v222, v222\n\t" \
  "v_cvt_pk_bf16_f32 v230, v220, v221\n\t" \
  "v_exp_f32 v223, v223\n\t" \
  "v_exp_f32 v192, v192\n\t" \
  "v_cvt_pk_bf16_f32 v231, v222, v223\n\t" \
  "v_exp_f32 v193, v193\n\t" \
  "v_exp_f32 v194, v194\n\t" \
  "v_mfma_f32_32x32x16_bf16 v[96:111], v[252:255], v[228:231], v[96:111]\n\t" \
  "ds_read_b128 v[252:255], %[a1] offset:5280\n\t" \
  "v_cvt_pk_bf16_f32 v224, v192, v193\n\t" \
  "v_exp_f32 v195, v195\n\t" \
  "v_exp_f32 v196, v196\n\t" \
  "v_cvt_pk_bf16_f32 v225, v194, v195\n\t" \
  "v_exp_f32 v197, v197\n\t" \
  "v_mfma_f32_32x32x16_bf16 v[208:223], v[232:235], v[116:119], 0\n\t" \
  "ds_read_b128 v[232:235], %[ka] offset:6144\n\t" \
  "v_exp_f32 v198, v198\n\t" \
  "v_cvt_pk_bf16_f32 v226, v196, v197\n\t" \
  "v_exp_f32 v199, v199\n\t" \
  "v_exp_f32 v200, v200\n\t" \
  "v_cvt_pk_bf16_f32 v227, v198, v199\n\t" \
  "v_exp_f32 v201, v201\n\t" \
  "v_exp_f32 v202, v202\n\t" \
  "v_cvt_pk_bf16_f32 v228, v200, v201\n\t" \
  "v_exp_f32 v203, v203\n\t" \
  "v_mfma_f32_32x32x16_bf16 v[80:95], v[236:239], v[224:227], v[80:95]\n\t" \
  "ds_read_b128 v[248:251], %[a0] offset:5280\n\t" \
  "v_exp_f32 v204, v204\n\t" \
  "v_cvt_pk_bf16_f32 v229, v202, v203\n\t" \
  "v_exp_f32 v205, v205\n\t" \
  "v_exp_f32 v206, v206\n\t" \
  "v_cvt_pk_bf16_f32 v230, v204, v205\n\t" \
  "v_exp_f32 v207, v207\n\t" \
  "v_exp_f32 v208, v208\n\t" \
  "v_cvt_pk_bf16_f32 v231, v206, v207\n\t" \
  "v_exp_f32 v209, v209\n\t" \
  "v_exp_f32 v210, v210\n\t" \
  "v_mfma_f32_32x32x16_bf16 v[80:95], v[240:243], v[228:231], v[80:95]\n\t" \
  "buffer_load_dwordx4 v[128:131], %[l16], %[rs], %[wa] offen offset:1024 sc1\n\t" \
  "v_cvt_pk_bf16_f32 v224, v208, v209\n\t" \
  "v_exp_f32 v211, v211\n\t" \
  "v_exp_f32 v212, v212\n\t" \
  "v_cvt_pk_bf16_f32 v225, v210, v211\n\t" \
  "v_exp_f32 v213, v213\n\t" \
  "v_mfma_f32_32x32x16_bf16 v[192:207], v[244:247], v[112:115], 0\n\t" \
  "buffer_load_dwordx4 v[132:135], %[l16], %[rs], %[wb] offen offset:1024 sc1\n\t" \
  "v_exp_f32 v214, v214\n\t" \
  "v_cvt_pk_bf16_f32 v226, v212, v213\n\t" \
  "v_exp_f32 v215, v215\n\t" \
  "v_exp_f32 v216, v216\n\t" \
  "v_cvt_pk_bf16_f32 v227, v214, v215\n\t" \
  "v_exp_f32 v217, v217\n\t" \
  "v_exp_f32 v218, v218\n\t" \
  "v_cvt_pk_bf16_f32 v228, v216, v217\n\t" \
  "v_exp_f32 v219, v219\n\t" \
  "v_mfma_f32_32x32x16_bf16 v[96:111], v[236:239], v[224:227], v[96:111]\n\t" \
  "s_waitcnt lgkmcnt(0)\n\t" \
  "v_exp_f32 v220, v220\n\t" \
  "v_cvt_pk_bf16_f32 v229, v218, v219\n\t" \
  "v_exp_f32 v221, v221\n\t" \
  "v_exp_f32 v222, v222\n\t" \
  "v_cvt_pk_bf16_f32 v230, v220, v221\n\t" \
  "v_exp_f32 v223, v223\n\t" \
  "v_exp_f32 v192, v192\n\t" \
  "v_cvt_pk_bf16_f32 v231, v222, v223\n\t" \
  "v_exp_f32 v193, v193\n\t" \
  "v_exp_f32 v194, v194\n\t" \
  "v_mfma_f32_32x32x16_bf16 v[96:111], v[240:243], v[228:231], v[96:111]\n\t" \
  "ds_read_b128 v[240:243], %[a1] offset:6336\n\t" \
  "v_cvt_pk_bf16_f32 v224, v192, v193\n\t" \
  "v_exp_f32 v195, v195\n\t" \
  "v_exp_f32 v196, v196\n\t" \
  "v_cvt_pk_bf16_f32 v225, v194, v195\n\t" \
  "v_exp_f32 v197, v197\n\t" \
  "v_mfma_f32_32x32x16_bf16 v[208:223], v[244:247], v[116:119], 0\n\t" \
  "ds_read_b128 v[244:247], %[ka] offset:7168\n\t" \
  "v_exp_f32 v198, v198\n\t" \
  "v_cvt_pk_bf16_f32 v226, v196, v197\n\t" \
  "v_exp_f32 v199, v199\n\t" \
  "v_exp_f32 v200, v200\n\t" \
  "v_cvt_pk_bf16_f32 v227, v198, v199\n\t" \
  "v_exp_f32 v201, v201\n\t" \
  "v_exp_f32 v202, v202\n\t" \
  "v_cvt_pk_bf16_f32 v228, v200, v201\n\t" \
  "v_exp_f32 v203, v203\n\t" \
  "v_mfma_f32_32x32x16_bf16 v[80:95], v[248:251], v[224:227], v[80:95]\n\t" \
  "ds_read_b128 v[236:239], %[a0] offset:6336\n\t" \
  "v_exp_f32 v204, v204\n\t" \
  "v_cvt_pk_bf16_f32 v229, v202, v203\n\t" \
  "v_exp_f32 v205, v205\n\t" \
  "v_exp_f32 v206, v206\n\t" \
  "v_cvt_pk_bf16_f32 v230, v204, v205\n\t" \
  "v_exp_f32 v207, v207\n\t" \
  "v_exp_f32 v208, v208\n\t" \
  "v_cvt_pk_bf16_f32 v231, v206, v207\n\t" \
  "v_exp_f32 v209, v209\n\t" \
  "v_exp_f32 v210, v210\n\t" \
  "v_mfma_f32_32x32x16_bf16 v[80:95], v[252:255], v[228:231], v[80:95]\n\t" \
  "buffer_load_dwordx4 v[136:139], %[l16], %[rs], %[wa] offen offset:2048 sc1\n\t" \
  "v_cvt_pk_bf16_f32 v224, v208, v209\n\t" \
  "v_exp_f32 v211, v211\n\t" \
  "v_exp_f32 v212, v212\n\t" \
  "v_cvt_pk_bf16_f32 v225, v210, v211\n\t" \
  "v_exp_f32 v213, v213\n\t" \
  "v_mfma_f32_32x32x16_bf16 v[192:207], v[232:235], v[112:115], 0\n\t" \
  "buffer_load_dwordx4 v[140:143], %[l16], %[rs], %[wb] offen offset:2048 sc1\n\t" \
  "v_exp_f32 v214, v214\n\t" \
  "v_cvt_pk_bf16_f32 v226, v212, v213\n\t" \
  "v_exp_f32 v215, v215\n\t" \
  "v_exp_f32 v216, v216\n\t" \
  "v_cvt_pk_bf16_f32 v227, v214, v215\n\t" \
  "v_exp_f32 v217, v217\n\t" \
  "v_exp_f32 v218, v218\n\t" \
  "v_cvt_pk_bf16_f32 v228, v216, v217\n\t" \
  "v_exp_f32 v219, v219\n\t" \
  "v_mfma_f32_32x32x16_bf16 v[96:111], v[248:251], v[224:227], v[96:111]\n\t" \
  "s_waitcnt lgkmcnt(0)\n\t" \
  "v_exp_f32 v220, v220\n\t" \
  "v_cvt_pk_bf16_f32 v229, v218, v219\n\t" \
  "v_exp_f32 v221, v221\n\t" \
  "v_exp_f32 v222, v222\n\t" \
  "v_cvt_pk_bf16_f32 v230, v220, v221\n\t" \
  "v_exp_f32 v223, v223\n\t" \
  "v_exp_f32 v192, v192\n\t" \
  "v_cvt_pk_bf16_f32 v231, v222, v223\n\t" \
  "v_exp_f32 v193, v193\n\t" \
  "v_exp_f32 v194, v194\n\t" \
  "v_mfma_f32_32x32x16_bf16 v[96:111], v[252:255], v[228:231], v[96:111]\n\t" \
  "ds_read_b128 v[252:255], %[a1] offset:7392\n\t" \
  "v_cvt_pk_bf16_f32 v224, v192, v193\n\t" \
  "v_exp_f32 v195, v195\n\t" \
  "v_exp_f32 v196, v196\n\t" \
  "v_cvt_pk_bf16_f32 v225, v194, v195\n\t" \
  "v_exp_f32 v197, v197\n\t" \
  "v_mfma_f32_32x32x16_bf16 v[208:223], v[232:235], v[116:119], 0\n\t" \
  "ds_read_b128 v[232:235], %[ka] offset:8192\n\t" \
  "v_exp_f32 v198, v198\n\t" \
  "v_cvt_pk_bf16_f32 v226, v196, v197\n\t" \
  "v_exp_f32 v199, v199\n\t" \
  "v_exp_f32 v200, v200\n\t" \
  "v_cvt_pk_bf16_f32 v227, v198, v199\n\t" \
  "v_exp_f32 v201, v201\n\t" \
  "v_exp_f32 v202, v202\n\t" \
  "v_cvt_pk_bf16_f32 v228, v200, v201\n\t" \
  "v_exp_f32 v203, v203\n\t" \
  "v_mfma_f32_32x32x16_bf16 v[80:95], v[236:239], v[224:227], v[80:95]\n\t" \
  "ds_read_b128 v[248:251], %[a0] offset:7392\n\t" \
  "v_exp_f32 v204, v204\n\t" \
  "v_cvt_pk_bf16_f32 v229, v202, v203\n\t" \
  "v_exp_f32 v205, v205\n\t" \
  "v_exp_f32 v206, v206\n\t" \
  "v_cvt_pk_bf16_f32 v230, v204, v205\n\t" \
  "v_exp_f32 v207, v207\n\t" \
  "v_exp_f32 v208, v208\n\t" \
  "v_cvt_pk_bf16_f32 v231, v206, v207\n\t" \
  "v_exp_f32 v209, v209\n\t" \
  "v_exp_f32 v210, v210\n\t" \
  "v_mfma_f32_32x32x16_bf16 v[80:95], v[240:243], v[228:231], v[80:95]\n\t" \
  "buffer_load_dwordx4 v[144:147], %[l16], %[rs], %[wa] offen offset:3072 sc1\n\t" \
  "v_cvt_pk_bf16_f32 v224, v208, v209\n\t" \
  "v_exp_f32 v211, v211\n\t" \
  "v_exp_f32 v212, v212\n\t" \
  "v_cvt_pk_bf16_f32 v225, v210, v211\n\t" \
  "v_exp_f32 v213, v213\n\t" \
  "v_mfma_f32_32x32x16_bf16 v[192:207], v[244:247], v[112:115], 0\n\t" \
  "buffer_load_dwordx4 v[148:151], %[l16], %[rs], %[wb] offen offset:3072 sc1\n\t" \
  "v_exp_f32 v214, v214\n\t" \
  "v_cvt_pk_bf16_f32 v226, v212, v213\n\t" \
  "v_exp_f32 v215, v215\n\t" \
  "v_exp_f32 v216, v216\n\t" \
  "v_cvt_pk_bf16_f32 v227, v214, v215\n\t" \
  "v_exp_f32 v217, v217\n\t" \
  "v_exp_f32 v218, v218\n\t" \
  "v_cvt_pk_bf16_f32 v228, v216, v217\n\t" \
  "v_exp_f32 v219, v219\n\t" \
  "v_mfma_f32_32x32x16_bf16 v[96:111], v[236:239], v[224:227], v[96:111]\n\t" \
  "s_waitcnt lgkmcnt(0)\n\t" \
  "v_exp_f32 v220, v220\n\t" \
  "v_cvt_pk_bf16_f32 v229, v218, v219\n\t" \
  "v_exp_f32 v221, v221\n\t" \
  "v_exp_f32 v222, v222\n\t" \
  "v_cvt_pk_bf16_f32 v230, v220, v221\n\t" \
  "v_exp_f32 v223, v223\n\t" \
  "v_exp_f32 v192, v192\n\t" \
  "v_cvt_pk_bf16_f32 v231, v222, v223\n\t" \
  "v_exp_f32 v193, v193\n\t" \
  "v_exp_f32 v194, v194\n\t" \
  "v_mfma_f32_32x32x16_bf16 v[96:111], v[240:243], v[228:231], v[96:111]\n\t" \
  "ds_read_b128 v[240:243], %[a1] offset:8448\n\t" \
  "v_cvt_pk_bf16_f32 v224, v192, v193\n\t" \
  "v_exp_f32 v195, v195\n\t" \
  "v_exp_f32 v196, v196\n\t" \
  "v_cvt_pk_bf16_f32 v225, v194, v195\n\t" \
  "v_exp_f32 v197, v197\n\t" \
  "v_mfma_f32_32x32x16_bf16 v[208:223], v[244:247], v[116:119], 0\n\t" \
  "ds_read_b128 v[244:247], %[ka] offset:9216\n\t" \
  "v_exp_f32 v198, v198\n\t" \
  "v_cvt_pk_bf16_f32 v226, v196, v197\n\t" \
  "v_exp_f32 v199, v199\n\t" \
  "v_exp_f32 v200, v200\n\t" \
  "v_cvt_pk_bf16_f32 v227, v198, v199\n\t" \
  "v_exp_f32 v201, v201\n\t" \
  "v_exp_f32 v202, v202\n\t" \
  "v_cvt_pk_bf16_f32 v228, v200, v201\n\t" \
  "v_exp_f32 v203, v203\n\t" \
  "v_mfma_f32_32x32x16_bf16 v[80:95], v[248:251], v[224:227], v[80:95]\n\t" \
  "ds_read_b128 v[236:239], %[a0] offset:8448\n\t" \
  "v_exp_f32 v204, v204\n\t" \
  "v_cvt_pk_bf16_f32 v229, v202, v203\n\t" \
  "v_exp_f32 v205, v205\n\t" \
  "v_exp_f32 v206, v206\n\t" \
  "v_cvt_pk_bf16_f32 v230, v204, v205\n\t" \
  "v_exp_f32 v207, v207\n\t" \
  "v_exp_f32 v208, v208\n\t" \
  "v_cvt_pk_bf16_f32 v231, v206, v207\n\t" \
  "v_exp_f32 v209, v209\n\t" \
  "v_exp_f32 v210, v210\n\t" \
  "s_add_u32 %[wa], %[wa], 4096\n\t" \
  "v_mfma_f32_32x32x16_bf16 v[80:95], v[252:255], v[228:231], v[80:95]\n\t" \
  "buffer_load_dwordx4 v[152:155], %[l16], %[rs], %[wa] offen sc1\n\t" \
  "v_cvt_pk_bf16_f32 v224, v208, v209\n\t" \
  "v_exp_f32 v211, v211\n\t" \
  "v_exp_f32 v212, v212\n\t" \
  "v_cvt_pk_bf16_f32 v225, v210, v211\n\t" \
  "v_exp_f32 v213, v213\n\t" \
  "s_add_u32 %[wb], %[wb], 4096\n\t" \
  "v_mfma_f32_32x32x16_bf16 v[192:207], v[232:235], v[112:115], 0\n\t" \
  "buffer_load_dwordx4 v[156:159], %[l16], %[rs], %[wb] offen sc1\n\t" \
  "v_exp_f32 v214, v214\n\t" \
  "v_cvt_pk_bf16_f32 v226, v212, v213\n\t" \
  "v_exp_f32 v215, v215\n\t" \
  "v_exp_f32 v216, v216\n\t" \
  "v_cvt_pk_bf16_f32 v227, v214, v215\n\t" \
  "v_exp_f32 v217, v217\n\t" \
  "v_exp_f32 v218, v218\n\t" \
  "v_cvt_pk_bf16_f32 v228, v216, v217\n\t" \
  "v_exp_f32 v219, v219\n\t" \
  "v_mfma_f32_32x32x16_bf16 v[96:111], v[248:251], v[224:227], v[96:111]\n\t" \
  "s_waitcnt lgkmcnt(0)\n\t" \
  "v_exp_f32 v220, v220\n\t" \
  "v_cvt_pk_bf16_f32 v229, v218, v219\n\t" \
  "v_exp_f32 v221, v221\n\t" \
  "v_exp_f32 v222, v222\n\t" \
  "v_cvt_pk_bf16_f32 v230, v220, v221\n\t" \
  "v_exp_f32 v223, v223\n\t" \
  "v_add_u32 %[ka], 8192, %[ka]\n\t" \
  "v_add_u32 %[a0], 8448, %[a0]\n\t" \
  "v_add_u32 %[a1], 8448, %[a1]\n\t" \
  "v_exp_f32 v192, v192\n\t" \
  "v_cvt_pk_bf16_f32 v231, v222, v223\n\t" \
  "v_exp_f32 v193, v193\n\t" \
  "v_exp_f32 v194, v194\n\t" \
  "v_mfma_f32_32x32x16_bf16 v[96:111], v[252:255], v[228:231], v[96:111]\n\t" \
  "ds_read_b128 v[252:255], %[a1] offset:1056\n\t" \
  "v_cvt_pk_bf16_f32 v224, v192, v193\n\t" \
  "v_exp_f32 v195, v195\n\t" \
  "v_exp_f32 v196, v196\n\t" \
  "v_cvt_pk_bf16_f32 v225, v194, v195\n\t" \
  "v_exp_f32 v197, v197\n\t" \
  "v_mfma_f32_32x32x16_bf16 v[208:223], v[232:235], v[116:119], 0\n\t" \
  "ds_read_b128 v[232:235], %[ka] offset:2048\n\t" \
  "v_exp_f32 v198, v198\n\t" \
  "v_cvt_pk_bf16_f32 v226, v196, v197\n\t" \
  "v_exp_f32 v199, v199\n\t" \
  "v_exp_f32 v200, v200\n\t" \
  "v_cvt_pk_bf16_f32 v227, v198, v199\n\t" \
  "v_exp_f32 v201, v201\n\t" \
  "v_exp_f32 v202, v202\n\t" \
  "v_cvt_pk_bf16_f32 v228, v200, v201\n\t" \
  "v_exp_f32 v203, v203\n\t" \
  "v_mfma_f32_32x32x16_bf16 v[80:95], v[236:239], v[224:227], v[80:95]\n\t" \
  "ds_read_b128 v[248:251], %[a0] offset:1056\n\t" \
  "v_exp_f32 v204, v204\n\t" \
  "v_cvt_pk_bf16_f32 v229, v202, v203\n\t" \
  "v_exp_f32 v205, v205\n\t" \
  "v_exp_f32 v206, v206\n\t" \
  "v_cvt_pk_bf16_f32 v230, v204, v205\n\t" \
  "v_exp_f32 v207, v207\n\t" \
  "v_exp_f32 v208, v208\n\t" \
  "v_cvt_pk_bf16_f32 v231, v206, v207\n\t" \
  "v_exp_f32 v209, v209\n\t" \
  "v_exp_f32 v210, v210\n\t" \
  "v_mfma_f32_32x32x16_bf16 v[80:95], v[240:243], v[228:231], v[80:95]\n\t" \
  "buffer_load_dwordx4 v[160:163], %[l16], %[rs], %[wa] offen offset:1024 sc1\n\t" \
  "v_cvt_pk_bf16_f32 v224, v208, v209\n\t" \
  "v_exp_f32 v211, v211\n\t" \
  "v_exp_f32 v212, v212\n\t" \
  "v_cvt_pk_bf16_f32 v225, v210, v211\n\t" \
  "v_exp_f32 v213, v213\n\t" \
  "v_mfma_f32_32x32x16_bf16 v[192:207], v[244:247], v[112:115], 0\n\t" \
  "buffer_load_dwordx4 v[164:167], %[l16], %[rs], %[wb] offen offset:1024 sc1\n\t" \
  "v_exp_f32 v214, v214\n\t" \
  "v_cvt_pk_bf16_f32 v226, v212, v213\n\t" \
  "v_exp_f32 v215, v215\n\t" \
  "v_exp_f32 v216, v216\n\t" \
  "v_cvt_pk_bf16_f32 v227, v214, v215\n\t" \
  "v_exp_f32 v217, v217\n\t" \
  "v_exp_f32 v218, v218\n\t" \
  "v_cvt_pk_bf16_f32 v228, v216, v217\n\t" \
  "v_exp_f32 v219, v219\n\t" \
  "v_mfma_f32_32x32x16_bf16 v[96:111], v[236:239], v[224:227], v[96:111]\n\t" \
  "s_waitcnt lgkmcnt(0)\n\t" \
  "v_exp_f32 v220, v220\n\t" \
  "v_cvt_pk_bf16_f32 v229, v218, v219\n\t" \
  "v_exp_f32 v221, v221\n\t" \
  "v_exp_f32 v222, v222\n\t" \
  "v_cvt_pk_bf16_f32 v230, v220, v221\n\t" \
  "v_exp_f32 v223, v223\n\t" \
  "v_exp_f32 v192, v192\n\t" \
  "v_cvt_pk_bf16_f32 v231, v222, v223\n\t" \
  "v_exp_f32 v193, v193\n\t" \
  "v_exp_f32 v194, v194\n\t" \
  "v_mfma_f32_32x32x16_bf16 v[96:111], v[240:243], v[228:231], v[96:111]\n\t" \
  "ds_read_b128 v[240:243], %[a1] offset:2112\n\t" \
  "v_cvt_pk_bf16_f32 v224, v192, v193\n\t" \
  "v_exp_f32 v195, v195\n\t" \
  "v_exp_f32 v196, v196\n\t" \
  "v_cvt_pk_bf16_f32 v225, v194, v195\n\t" \
  "v_exp_f32 v197, v197\n\t" \
  "v_mfma_f32_32x32x16_bf16 v[208:223], v[244:247], v[116:119], 0\n\t" \
  "ds_read_b128 v[244:247], %[ka] offset:3072\n\t" \
  "v_exp_f32 v198, v198\n\t" \
  "v_cvt_pk_bf16_f32 v226, v196, v197\n\t" \
  "v_exp_f32 v199, v199\n\t" \
  "v_exp_f32 v200, v200\n\t" \
  "v_cvt_pk_bf16_f32 v227, v198, v199\n\t" \
  "v_exp_f32 v201, v201\n\t" \
  "v_exp_f32 v202, v202\n\t" \
  "v_cvt_pk_bf16_f32 v228, v200, v201\n\t" \
  "v_exp_f32 v203, v203\n\t" \
  "v_mfma_f32_32x32x16_bf16 v[80:95], v[248:251], v[224:227], v[80:95]\n\t" \
  "ds_read_b128 v[236:239], %[a0] offset:2112\n\t" \
  "v_exp_f32 v204, v204\n\t" \
  "v_cvt_pk_bf16_f32 v229, v202, v203\n\t" \
  "v_exp_f32 v205, v205\n\t" \
  "v_exp_f32 v206, v206\n\t" \
  "v_cvt_pk_bf16_f32 v230, v204, v205\n\t" \
  "v_exp_f32 v207, v207\n\t" \
  "v_exp_f32 v208, v208\n\t" \
  "v_cvt_pk_bf16_f32 v231, v206, v207\n\t" \
  "v_exp_f32 v209, v209\n\t" \
  "v_exp_f32 v210, v210\n\t" \
  "v_mfma_f32_32x32x16_bf16 v[80:95], v[252:255], v[228:231], v[80:95]\n\t" \
  "buffer_load_dwordx4 v[168:171], %[l16], %[rs], %[wa] offen offset:2048 sc1\n\t" \
  "v_cvt_pk_bf16_f32 v224, v208, v209\n\t" \
  "v_exp_f32 v211, v211\n\t" \
  "v_exp_f32 v212, v212\n\t" \
  "v_cvt_pk_bf16_f32 v225, v210, v211\n\t" \
  "v_exp_f32 v213, v213\n\t" \
  "v_mfma_f32_32x32x16_bf16 v[192:207], v[232:235], v[112:115], 0\n\t" \
  "buffer_load_dwordx4 v[172:175], %[l16], %[rs], %[wb] offen offset:2048 sc1\n\t" \
  "v_exp_f32 v214, v214\n\t" \
  "v_cvt_pk_bf16_f32 v226, v212, v213\n\t" \
  "v_exp_f32 v215, v215\n\t" \
  "v_exp_f32 v216, v216\n\t" \
  "v_cvt_pk_bf16_f32 v227, v214, v215\n\t" \
  "v_exp_f32 v217, v217\n\t" \
  "v_exp_f32 v218, v218\n\t" \
  "v_cvt_pk_bf16_f32 v228, v216, v217\n\t" \
  "v_exp_f32 v219, v219\n\t" \
  "v_mfma_f32_32x32x16_bf16 v[96:111], v[248:251], v[224:227], v[96:111]\n\t" \
  "s_waitcnt lgkmcnt(0)\n\t" \
  "v_exp_f32 v220, v220\n\t" \
  "v_cvt_pk_bf16_f32 v229, v218, v219\n\t" \
  "v_exp_f32 v221, v221\n\t" \
  "v_exp_f32 v222, v222\n\t" \
  "v_cvt_pk_bf16_f32 v230, v220, v221\n\t" \
  "v_exp_f32 v223, v223\n\t" \
  "v_exp_f32 v192, v192\n\t" \
  "v_cvt_pk_bf16_f32 v231, v222, v223\n\t" \
  "v_exp_f32 v193, v193\n\t" \
  "v_exp_f32 v194, v194\n\t" \
  "v_mfma_f32_32x32x16_bf16 v[96:111], v[252:255], v[228:231], v[96:111]\n\t" \
  "ds_read_b128 v[252:255], %[a1] offset:3168\n\t" \
  "v_cvt_pk_bf16_f32 v224, v192, v193\n\t" \
  "v_exp_f32 v195, v195\n\t" \
  "v_exp_f32 v196, v196\n\t" \
  "v_cvt_pk_bf16_f32 v225, v194, v195\n\t" \
  "v_exp_f32 v197, v197\n\t" \
  "v_mfma_f32_32x32x16_bf16 v[208:223], v[232:235], v[116:119], 0\n\t" \
  "ds_read_b128 v[232:235], %[ka] offset:4096\n\t" \
  "v_exp_f32 v198, v198\n\t" \
  "v_cvt_pk_bf16_f32 v226, v196, v197\n\t" \
  "v_exp_f32 v199, v199\n\t" \
  "v_exp_f32 v200, v200\n\t" \
  "v_cvt_pk_bf16_f32 v227, v198, v199\n\t" \
  "v_exp_f32 v201, v201\n\t" \
  "v_exp_f32 v202, v202\n\t" \
  "v_cvt_pk_bf16_f32 v228, v200, v201\n\t" \
  "v_exp_f32 v203, v203\n\t" \
  "v_mfma_f32_32x32x16_bf16 v[80:95], v[236:239], v[224:227], v[80:95]\n\t" \
  "ds_read_b128 v[248:251], %[a0] offset:3168\n\t" \
  "v_exp_f32 v204, v204\n\t" \
  "v_cvt_pk_bf16_f32 v229, v202, v203\n\t" \
  "v_exp_f32 v205, v205\n\t" \
  "v_exp_f32 v206, v206\n\t" \
  "v_cvt_pk_bf16_f32 v230, v204, v205\n\t" \
  "v_exp_f32 v207, v207\n\t" \
  "v_exp_f32 v208, v208\n\t" \
  "v_cvt_pk_bf16_f32 v231, v206, v207\n\t" \
  "v_exp_f32 v209, v209\n\t" \
  "v_exp_f32 v210, v210\n\t" \
  "v_mfma_f32_32x32x16_bf16 v[80:95], v[240:243], v[228:231], v[80:95]\n\t" \
  "buffer_load_dwordx4 v[176:179], %[l16], %[rs], %[wa] offen offset:3072 sc1\n\t" \
  "v_cvt_pk_bf16_f32 v224, v208, v209\n\t" \
  "v_exp_f32 v211, v211\n\t" \
  "v_exp_f32 v212, v212\n\t" \
  "v_cvt_pk_bf16_f32 v225, v210, v211\n\t" \
  "v_exp_f32 v213, v213\n\t" \
  "v_mfma_f32_32x32x16_bf16 v[192:207], v[244:247], v[112:115], 0\n\t" \
  "buffer_load_dwordx4 v[180:183], %[l16], %[rs], %[wb] offen offset:3072 sc1\n\t" \
  "v_exp_f32 v214, v214\n\t" \
  "v_cvt_pk_bf16_f32 v226, v212, v213\n\t" \
  "v_exp_f32 v215, v215\n\t" \
  "v_exp_f32 v216, v216\n\t" \
  "v_cvt_pk_bf16_f32 v227, v214, v215\n\t" \
  "v_exp_f32 v217, v217\n\t" \
  "v_exp_f32 v218, v218\n\t" \
  "v_cvt_pk_bf16_f32 v228, v216, v217\n\t" \
  "v_exp_f32 v219, v219\n\t" \
  "v_mfma_f32_32x32x16_bf16 v[96:111], v[236:239], v[224:227], v[96:111]\n\t" \
  "s_waitcnt lgkmcnt(0)\n\t" \
  "v_exp_f32 v220, v220\n\t" \
  "v_cvt_pk_bf16_f32 v229, v218, v219\n\t" \
  "v_exp_f32 v221, v221\n\t" \
  "v_exp_f32 v222, v222\n\t" \
  "v_cvt_pk_bf16_f32 v230, v220, v221\n\t" \
  "v_exp_f32 v223, v223\n\t" \
  "v_exp_f32 v192, v192\n\t" \
  "v_cvt_pk_bf16_f32 v231, v222, v223\n\t" \
  "v_exp_f32 v193, v193\n\t" \
  "v_exp_f32 v194, v194\n\t" \
  "v_mfma_f32_32x32x16_bf16 v[96:111], v[240:243], v[228:231], v[96:111]\n\t" \
  "ds_read_b128 v[240:243], %[a1] offset:4224\n\t" \
  "v_cvt_pk_bf16_f32 v224, v192, v193\n\t" \
  "v_exp_f32 v195, v195\n\t" \
  "v_exp_f32 v196, v196\n\t" \
  "v_cvt_pk_bf16_f32 v225, v194, v195\n\t" \
  "v_exp_f32 v197, v197\n\t" \
  "v_mfma_f32_32x32x16_bf16 v[208:223], v[244:247], v[116:119], 0\n\t" \
  "ds_read_b128 v[244:247], %[ka] offset:5120\n\t" \
  "v_exp_f32 v198, v198\n\t" \
  "v_cvt_pk_bf16_f32 v226, v196, v197\n\t" \
  "v_exp_f32 v199, v199\n\t" \
  "v_exp_f32 v200, v200\n\t" \
  "v_cvt_pk_bf16_f32 v227, v198, v199\n\t" \
  "v_exp_f32 v201, v201\n\t" \
  "v_exp_f32 v202, v202\n\t" \
  "v_cvt_pk_bf16_f32 v228, v200, v201\n\t" \
  "v_exp_f32 v203, v203\n\t" \
  "v_mfma_f32_32x32x16_bf16 v[80:95], v[248:251], v[224:227], v[80:95]\n\t" \
  "ds_read_b128 v[236:239], %[a0] offset:4224\n\t" \
  "v_exp_f32 v204, v204\n\t" \
  "v_cvt_pk_bf16_f32 v229, v202, v203\n\t" \
  "v_exp_f32 v205, v205\n\t" \
  "v_exp_f32 v206, v206\n\t" \
  "v_cvt_pk_bf16_f32 v230, v204, v205\n\t" \
  "v_exp_f32 v207, v207\n\t" \
  "v_exp_f32 v208, v208\n\t" \
  "v_cvt_pk_bf16_f32 v231, v206, v207\n\t" \
  "v_exp_f32 v209, v209\n\t" \
  "v_exp_f32 v210, v210\n\t" \
  "s_add_u32 %[wa], %[wa], 4096\n\t" \
  "v_mfma_f32_32x32x16_bf16 v[80:95], v[252:255], v[228:231], v[80:95]\n\t" \
  "buffer_load_dwordx4 v[184:187], %[l16], %[rs], %[wa] offen sc1\n\t" \
  "v_cvt_pk_bf16_f32 v224, v208, v209\n\t" \
  "v_exp_f32 v211, v211\n\t" \
  "v_exp_f32 v212, v212\n\t" \
  "v_cvt_pk_bf16_f32 v225, v210, v211\n\t" \
  "v_exp_f32 v213, v213\n\t" \
  "s_add_u32 %[wb], %[wb], 4096\n\t" \
  "v_mfma_f32_32x32x16_bf16 v[192:207], v[232:235], v[112:115], 0\n\t" \
  "buffer_load_dwordx4 v[188:191], %[l16], %[rs], %[wb] offen sc1\n\t" \
  "v_exp_f32 v214, v214\n\t" \
  "v_cvt_pk_bf16_f32 v226, v212, v213\n\t" \
  "v_exp_f32 v215, v215\n\t" \
  "v_exp_f32 v216, v216\n\t" \
  "v_cvt_pk_bf16_f32 v227, v214, v215\n\t" \
  "v_exp_f32 v217, v217\n\t" \
  "v_exp_f32 v218, v218\n\t" \
  "v_cvt_pk_bf16_f32 v228, v216, v217\n\t" \
  "v_exp_f32 v219, v219\n\t" \
  "v_mfma_f32_32x32x16_bf16 v[96:111], v[248:251], v[224:227], v[96:111]\n\t" \
  "s_waitcnt lgkmcnt(0)\n\t" \
  "v_exp_f32 v220, v220\n\t" \
  "v_cvt_pk_bf16_f32 v229, v218, v219\n\t" \
  "v_exp_f32 v221, v221\n\t" \
  "v_exp_f32 v222, v222\n\t" \
  "v_cvt_pk_bf16_f32 v230, v220, v221\n\t" \
  "v_exp_f32 v223, v223\n\t" \
  "v_exp_f32 v192, v192\n\t" \
  "v_cvt_pk_bf16_f32 v231, v222, v223\n\t" \
  "v_exp_f32 v193, v193\n\t" \
  "v_exp_f32 v194, v194\n\t" \
  "v_mfma_f32_32x32x16_bf16 v[96:111], v[252:255], v[228:231], v[96:111]\n\t" \
  "ds_read_b128 v[252:255], %[a1] offset:5280\n\t" \
  "v_cvt_pk_bf16_f32 v224, v192, v193\n\t" \
  "v_exp_f32 v195, v195\n\t" \
  "v_exp_f32 v196, v196\n\t" \
  "v_cvt_pk_bf16_f32 v225, v194, v195\n\t" \
  "v_exp_f32 v197, v197\n\t" \
  "v_mfma_f32_32x32x16_bf16 v[208:223], v[232:235], v[116:119], 0\n\t" \
  "ds_read_b128 v[232:235], %[ka] offset:6144\n\t" \
  "v_exp_f32 v198, v198\n\t" \
  "v_cvt_pk_bf16_f32 v226, v196, v197\n\t" \
  "v_exp_f32 v199, v199\n\t" \
  "v_exp_f32 v200, v200\n\t" \
  "v_cvt_pk_bf16_f32 v227, v198, v199\n\t" \
  "v_exp_f32 v201, v201\n\t" \
  "v_exp_f32 v202, v202\n\t" \
  "v_cvt_pk_bf16_f32 v228, v200, v201\n\t" \
  "v_exp_f32 v203, v203\n\t" \
  "v_mfma_f32_32x32x16_bf16 v[80:95], v[236:239], v[224:227], v[80:95]\n\t" \
  "ds_read_b128 v[248:251], %[a0] offset:5280\n\t" \
  "v_exp_f32 v204, v204\n\t" \
  "v_cvt_pk_bf16_f32 v229, v202, v203\n\t" \
  "v_exp_f32 v205, v205\n\t" \
  "v_exp_f32 v206, v206\n\t" \
  "v_cvt_pk_bf16_f32 v230, v204, v205\n\t" \
  "v_exp_f32 v207, v207\n\t" \
  "v_exp_f32 v208, v208\n\t" \
  "v_cvt_pk_bf16_f32 v231, v206, v207\n\t" \
  "v_exp_f32 v209, v209\n\t" \
  "v_exp_f32 v210, v210\n\t" \
  "v_mfma_f32_32x32x16_bf16 v[80:95], v[240:243], v[228:231], v[80:95]\n\t" \
  "buffer_load_dwordx4 a[192:195], %[l16], %[rs], %[wa] offen offset:1024 sc1\n\t" \
  "v_cvt_pk_bf16_f32 v224, v208, v209\n\t" \
  "v_exp_f32 v211, v211\n\t" \
  "v_exp_f32 v212, v212\n\t" \
  "v_cvt_pk_bf16_f32 v225, v210, v211\n\t" \
  "v_exp_f32 v213, v213\n\t" \
  "v_mfma_f32_32x32x16_bf16 v[192:207], v[244:247], v[112:115], 0\n\t" \
  "buffer_load_dwordx4 a[196:199], %[l16], %[rs], %[wb] offen offset:1024 sc1\n\t" \
  "v_exp_f32 v214, v214\n\t" \
  "v_cvt_pk_bf16_f32 v226, v212, v213\n\t" \
  "v_exp_f32 v215, v215\n\t" \
  "v_exp_f32 v216, v216\n\t" \
  "v_cvt_pk_bf16_f32 v227, v214, v215\n\t" \
  "v_exp_f32 v217, v217\n\t" \
  "v_exp_f32 v218, v218\n\t" \
  "v_cvt_pk_bf16_f32 v228, v216, v217\n\t" \
  "v_exp_f32 v219, v219\n\t" \
  "v_mfma_f32_32x32x16_bf16 v[96:111], v[236:239], v[224:227], v[96:111]\n\t" \
  "s_waitcnt lgkmcnt(0)\n\t" \
  "v_exp_f32 v220, v220\n\t" \
  "v_cvt_pk_bf16_f32 v229, v218, v219\n\t" \
  "v_exp_f32 v221, v221\n\t" \
  "v_exp_f32 v222, v222\n\t" \
  "v_cvt_pk_bf16_f32 v230, v220, v221\n\t" \
  "v_exp_f32 v223, v223\n\t" \
  "v_exp_f32 v192, v192\n\t" \
  "v_cvt_pk_bf16_f32 v231, v222, v223\n\t" \
  "v_exp_f32 v193, v193\n\t" \
  "v_exp_f32 v194, v194\n\t" \
  "v_mfma_f32_32x32x16_bf16 v[96:111], v[240:243], v[228:231], v[96:111]\n\t" \
  "ds_read_b128 v[240:243], %[a1] offset:6336\n\t" \
  "v_cvt_pk_bf16_f32 v224, v192, v193\n\t" \
  "v_exp_f32 v195, v195\n\t" \
  "v_exp_f32 v196, v196\n\t" \
  "v_cvt_pk_bf16_f32 v225, v194, v195\n\t" \
  "v_exp_f32 v197, v197\n\t" \
  "v_mfma_f32_32x32x16_bf16 v[208:223], v[244:247], v[116:119], 0\n\t" \
  "ds_read_b128 v[244:247], %[ka] offset:7168\n\t" \
  "v_exp_f32 v198, v198\n\t" \
  "v_cvt_pk_bf16_f32 v226, v196, v197\n\t" \
  "v_exp_f32 v199, v199\n\t" \
  "v_exp_f32 v200, v200\n\t" \
  "v_cvt_pk_bf16_f32 v227, v198, v199\n\t" \
  "v_exp_f32 v201, v201\n\t" \
  "v_exp_f32 v202, v202\n\t" \
  "v_cvt_pk_bf16_f32 v228, v200, v201\n\t" \
  "v_exp_f32 v203, v203\n\t" \
  "v_mfma_f32_32x32x16_bf16 v[80:95], v[248:251], v[224:227], v[80:95]\n\t" \
  "ds_read_b128 v[236:239], %[a0] offset:6336\n\t" \
  "v_exp_f32 v204, v204\n\t" \
  "v_cvt_pk_bf16_f32 v229, v202, v203\n\t" \
  "v_exp_f32 v205, v205\n\t" \
  "v_exp_f32 v206, v206\n\t" \
  "v_cvt_pk_bf16_f32 v230, v204, v205\n\t" \
  "v_exp_f32 v207, v207\n\t" \
  "v_exp_f32 v208, v208\n\t" \
  "v_cvt_pk_bf16_f32 v231, v206, v207\n\t" \
  "v_exp_f32 v209, v209\n\t" \
  "v_exp_f32 v210, v210\n\t" \
  "v_mfma_f32_32x32x16_bf16 v[80:95], v[252:255], v[228:231], v[80:95]\n\t" \
  "buffer_load_dwordx4 a[200:203], %[l16], %[rs], %[wa] offen offset:2048 sc1\n\t" \
  "v_cvt_pk_bf16_f32 v224, v208, v209\n\t" \
  "v_exp_f32 v211, v211\n\t" \
  "v_exp_f32 v212, v212\n\t" \
  "v_cvt_pk_bf16_f32 v225, v210, v211\n\t" \
  "v_exp_f32 v213, v213\n\t" \
  "v_mfma_f32_32x32x16_bf16 v[192:207], v[232:235], v[112:115], 0\n\t" \
  "buffer_load_dwordx4 a[204:207], %[l16], %[rs], %[wb] offen offset:2048 sc1\n\t" \
  "v_exp_f32 v214, v214\n\t" \
  "v_cvt_pk_bf16_f32 v226, v212, v213\n\t" \
  "v_exp_f32 v215, v215\n\t" \
  "v_exp_f32 v216, v216\n\t" \
  "v_cvt_pk_bf16_f32 v227, v214, v215\n\t" \
  "v_exp_f32 v217, v217\n\t" \
  "v_exp_f32 v218, v218\n\t" \
  "v_cvt_pk_bf16_f32 v228, v216, v217\n\t" \
  "v_exp_f32 v219, v219\n\t" \
  "v_mfma_f32_32x32x16_bf16 v[96:111], v[248:251], v[224:227], v[96:111]\n\t" \
  "s_waitcnt lgkmcnt(0)\n\t" \
  "v_exp_f32 v220, v220\n\t" \
  "v_cvt_pk_bf16_f32 v229, v218, v219\n\t" \
  "v_exp_f32 v221, v221\n\t" \
  "v_exp_f32 v222, v222\n\t" \
  "v_cvt_pk_bf16_f32 v230, v220, v221\n\t" \
  "v_exp_f32 v223, v223\n\t" \
  "v_exp_f32 v192, v192\n\t" \
  "v_cvt_pk_bf16_f32 v231, v222, v223\n\t" \
  "v_exp_f32 v193, v193\n\t" \
  "v_exp_f32 v194, v194\n\t" \
  "v_mfma_f32_32x32x16_bf16 v[96:111], v[252:255], v[228:231], v[96:111]\n\t" \
  "ds_read_b128 v[252:255], %[a1] offset:7392\n\t" \
  "v_cvt_pk_bf16_f32 v224, v192, v193\n\t" \
  "v_exp_f32 v195, v195\n\t" \
  "v_exp_f32 v196, v196\n\t" \
  "v_cvt_pk_bf16_f32 v225, v194, v195\n\t" \
  "v_exp_f32 v197, v197\n\t" \
  "v_mfma_f32_32x32x16_bf16 v[208:223], v[232:235], v[116:119], 0\n\t" \
  "ds_read_b128 v[232:235], %[ka] offset:8192\n\t" \
  "v_exp_f32 v198, v198\n\t" \
  "v_cvt_pk_bf16_f32 v226, v196, v197\n\t" \
  "v_exp_f32 v199, v199\n\t" \
  "v_exp_f32 v200, v200\n\t" \
  "v_cvt_pk_bf16_f32 v227, v198, v199\n\t" \
  "v_exp_f32 v201, v201\n\t" \
  "v_exp_f32 v202, v202\n\t" \
  "v_cvt_pk_bf16_f32 v228, v200, v201\n\t" \
  "v_exp_f32 v203, v203\n\t" \
  "v_mfma_f32_32x32x16_bf16 v[80:95], v[236:239], v[224:227], v[80:95]\n\t" \
  "ds_read_b128 v[248:251], %[a0] offset:7392\n\t" \
  "v_exp_f32 v204, v204\n\t" \
  "v_cvt_pk_bf16_f32 v229, v202, v203\n\t" \
  "v_exp_f32 v205, v205\n\t" \
  "v_exp_f32 v206, v206\n\t" \
  "v_cvt_pk_bf16_f32 v230, v204, v205\n\t" \
  "v_exp_f32 v207, v207\n\t" \
  "v_exp_f32 v208, v208\n\t" \
  "v_cvt_pk_bf16_f32 v231, v206, v207\n\t" \
  "v_exp_f32 v209, v209\n\t" \
  "v_exp_f32 v210, v210\n\t" \
  "v_mfma_f32_32x32x16_bf16 v[80:95], v[240:243], v[228:231], v[80:95]\n\t" \
  "buffer_load_dwordx4 a[208:211], %[l16], %[rs], %[wa] offen offset:3072 sc1\n\t" \
  "v_cvt_pk_bf16_f32 v224, v208, v209\n\t" \
  "v_exp_f32 v211, v211\n\t" \
  "v_exp_f32 v212, v212\n\t" \
  "v_cvt_pk_bf16_f32 v225, v210, v211\n\t" \
  "v_exp_f32 v213, v213\n\t" \
  "v_mfma_f32_32x32x16_bf16 v[192:207], v[244:247], v[112:115], 0\n\t" \
  "buffer_load_dwordx4 a[212:215], %[l16], %[rs], %[wb] offen offset:3072 sc1\n\t" \
  "v_exp_f32 v214, v214\n\t" \
  "v_cvt_pk_bf16_f32 v226, v212, v213\n\t" \
  "v_exp_f32 v215, v215\n\t" \
  "v_exp_f32 v216, v216\n\t" \
  "v_cvt_pk_bf16_f32 v227, v214, v215\n\t" \
  "v_exp_f32 v217, v217\n\t" \
  "v_exp_f32 v218, v218\n\t" \
  "v_cvt_pk_bf16_f32 v228, v216, v217\n\t" \
  "v_exp_f32 v219, v219\n\t" \
  "v_mfma_f32_32x32x16_bf16 v[96:111], v[236:239], v[224:227], v[96:111]\n\t" \
  "s_waitcnt lgkmcnt(0)\n\t" \
  "v_exp_f32 v220, v220\n\t" \
  "v_cvt_pk_bf16_f32 v229, v218, v219\n\t" \
  "v_exp_f32 v221, v221\n\t" \
  "v_exp_f32 v222, v222\n\t" \
  "v_cvt_pk_bf16_f32 v230, v220, v221\n\t" \
  "v_exp_f32 v223, v223\n\t" \
  "v_exp_f32 v192, v192\n\t" \
  "v_cvt_pk_bf16_f32 v231, v222, v223\n\t" \
  "v_exp_f32 v193, v193\n\t" \
  "v_exp_f32 v194, v194\n\t" \
  "v_mfma_f32_32x32x16_bf16 v[96:111], v[240:243], v[228:231], v[96:111]\n\t" \
  "ds_read_b128 v[240:243], %[a1] offset:8448\n\t" \
  "v_cvt_pk_bf16_f32 v224, v192, v193\n\t" \
  "v_exp_f32 v195, v195\n\t" \
  "v_exp_f32 v196, v196\n\t" \
  "v_cvt_pk_bf16_f32 v225, v194, v195\n\t" \
  "v_exp_f32 v197, v197\n\t" \
  "v_mfma_f32_32x32x16_bf16 v[208:223], v[244:247], v[116:119], 0\n\t" \
  "ds_read_b128 v[244:247], %[ka] offset:9216\n\t" \
  "v_exp_f32 v198, v198\n\t" \
  "v_cvt_pk_bf16_f32 v226, v196, v197\n\t" \
  "v_exp_f32 v199, v199\n\t" \
  "v_exp_f32 v200, v200\n\t" \
  "v_cvt_pk_bf16_f32 v227, v198, v199\n\t" \
  "v_exp_f32 v201, v201\n\t" \
  "v_exp_f32 v202, v202\n\t" \
  "v_cvt_pk_bf16_f32 v228, v200, v201\n\t" \
  "v_exp_f32 v203, v203\n\t" \
  "v_mfma_f32_32x32x16_bf16 v[80:95], v[248:251], v[224:227], v[80:95]\n\t" \
  "ds_read_b128 v[236:239], %[a0] offset:8448\n\t" \
  "v_exp_f32 v204, v204\n\t" \
  "v_cvt_pk_bf16_f32 v229, v202, v203\n\t" \
  "v_exp_f32 v205, v205\n\t" \
  "v_exp_f32 v206, v206\n\t" \
  "v_cvt_pk_bf16_f32 v230, v204, v205\n\t" \
  "v_exp_f32 v207, v207\n\t" \
  "v_exp_f32 v208, v208\n\t" \
  "v_cvt_pk_bf16_f32 v231, v206, v207\n\t" \
  "v_exp_f32 v209, v209\n\t" \
  "v_exp_f32 v210, v210\n\t" \
  "s_add_u32 %[wa], %[wa], 4096\n\t" \
  "v_mfma_f32_32x32x16_bf16 v[80:95], v[252:255], v[228:231], v[80:95]\n\t" \
  "buffer_load_dwordx4 a[216:219], %[l16], %[rs], %[wa] offen sc1\n\t" \
  "v_cvt_pk_bf16_f32 v224, v208, v209\n\t" \
  "v_exp_f32 v211, v211\n\t" \
  "v_exp_f32 v212, v212\n\t" \
  "v_cvt_pk_bf16_f32 v225, v210, v211\n\t" \
  "v_exp_f32 v213, v213\n\t" \
  "s_add_u32 %[wb], %[wb], 4096\n\t" \
  "v_mfma_f32_32x32x16_bf16 v[192:207], v[232:235], v[112:115], 0\n\t" \
  "buffer_load_dwordx4 a[220:223], %[l16], %[rs], %[wb] offen sc1\n\t" \
  "v_exp_f32 v214, v214\n\t" \
  "v_cvt_pk_bf16_f32 v226, v212, v213\n\t" \
  "v_exp_f32 v215, v215\n\t" \
  "v_exp_f32 v216, v216\n\t" \
  "v_cvt_pk_bf16_f32 v227, v214, v215\n\t" \
  "v_exp_f32 v217, v217\n\t" \
  "v_exp_f32 v218, v218\n\t" \
  "v_cvt_pk_bf16_f32 v228, v216, v217\n\t" \
  "v_exp_f32 v219, v219\n\t" \
  "v_mfma_f32_32x32x16_bf16 v[96:111], v[248:251], v[224:227], v[96:111]\n\t" \
  "s_waitcnt lgkmcnt(0)\n\t" \
  "v_exp_f32 v220, v220\n\t" \
  "v_cvt_pk_bf16_f32 v229, v218, v219\n\t" \
  "v_exp_f32 v221, v221\n\t" \
  "v_exp_f32 v222, v222\n\t" \
  "v_cvt_pk_bf16_f32 v230, v220, v221\n\t" \
  "v_exp_f32 v223, v223\n\t" \
  "v_add_u32 %[ka], 8192, %[ka]\n\t" \
  "v_add_u32 %[a0], 8448, %[a0]\n\t" \
  "v_add_u32 %[a1], 8448, %[a1]\n\t" \
  "v_exp_f32 v192, v192\n\t" \
  "v_cvt_pk_bf16_f32 v231, v222, v223\n\t" \
  "v_exp_f32 v193, v193\n\t" \
  "v_exp_f32 v194, v194\n\t" \
  "v_mfma_f32_32x32x16_bf16 v[96:111], v[252:255], v[228:231], v[96:111]\n\t" \
  "ds_read_b128 v[252:255], %[a1] offset:1056\n\t" \
  "v_cvt_pk_bf16_f32 v224, v192, v193\n\t" \
  "v_exp_f32 v195, v195\n\t" \
  "v_exp_f32 v196, v196\n\t" \
  "v_cvt_pk_bf16_f32 v225, v194, v195\n\t" \
  "v_exp_f32 v197, v197\n\t" \
  "v_mfma_f32_32x32x16_bf16 v[208:223], v[232:235], v[116:119], 0\n\t" \
  "ds_read_b128 v[232:235], %[ka] offset:2048\n\t" \
  "v_exp_f32 v198, v198\n\t" \
  "v_cvt_pk_bf16_f32 v226, v196, v197\n\t" \
  "v_exp_f32 v199, v199\n\t" \
  "v_exp_f32 v200, v200\n\t" \
  "v_cvt_pk_bf16_f32 v227, v198, v199\n\t" \
  "v_exp_f32 v201, v201\n\t" \
  "v_exp_f32 v202, v202\n\t" \
  "v_cvt_pk_bf16_f32 v228, v200, v201\n\t" \
  "v_exp_f32 v203, v203\n\t" \
  "v_mfma_f32_32x32x16_bf16 v[80:95], v[236:239], v[224:227], v[80:95]\n\t" \
  "ds_read_b128 v[248:251], %[a0] offset:1056\n\t" \
  "v_exp_f32 v204, v204\n\t" \
  "v_cvt_pk_bf16_f32 v229, v202, v203\n\t" \
  "v_exp_f32 v205, v205\n\t" \
  "v_exp_f32 v206, v206\n\t" \
  "v_cvt_pk_bf16_f32 v230, v204, v205\n\t" \
  "v_exp_f32 v207, v207\n\t" \
  "v_exp_f32 v208, v208\n\t" \
  "v_cvt_pk_bf16_f32 v231, v206, v207\n\t" \
  "v_exp_f32 v209, v209\n\t" \
  "v_exp_f32 v210, v210\n\t" \
  "v_mfma_f32_32x32x16_bf16 v[80:95], v[240:243], v[228:231], v[80:95]\n\t" \
  "buffer_load_dwordx4 a[224:227], %[l16], %[rs], %[wa] offen offset:1024 sc1\n\t" \
  "v_cvt_pk_bf16_f32 v224, v208, v209\n\t" \
  "v_exp_f32 v211, v211\n\t" \
  "v_exp_f32 v212, v212\n\t" \
  "v_cvt_pk_bf16_f32 v225, v210, v211\n\t" \
  "v_exp_f32 v213, v213\n\t" \
  "v_mfma_f32_32x32x16_bf16 v[192:207], v[244:247], v[112:115], 0\n\t" \
  "buffer_load_dwordx4 a[228:231], %[l16], %[rs], %[wb] offen offset:1024 sc1\n\t" \
  "v_exp_f32 v214, v214\n\t" \
  "v_cvt_pk_bf16_f32 v226, v212, v213\n\t" \
  "v_exp_f32 v215, v215\n\t" \
  "v_exp_f32 v216, v216\n\t" \
  "v_cvt_pk_bf16_f32 v227, v214, v215\n\t" \
  "v_exp_f32 v217, v217\n\t" \
  "v_exp_f32 v218, v218\n\t" \
  "v_cvt_pk_bf16_f32 v228, v216, v217\n\t" \
  "v_exp_f32 v219, v219\n\t" \
  "v_mfma_f32_32x32x16_bf16 v[96:111], v[236:239], v[224:227], v[96:111]\n\t" \
  "s_waitcnt lgkmcnt(0)\n\t" \
  "v_exp_f32 v220, v220\n\t" \
  "v_cvt_pk_bf16_f32 v229, v218, v219\n\t" \
  "v_exp_f32 v221, v221\n\t" \
  "v_exp_f32 v222, v222\n\t" \
  "v_cvt_pk_bf16_f32 v230, v220, v221\n\t" \
  "v_exp_f32 v223, v223\n\t" \
  "v_exp_f32 v192, v192\n\t" \
  "v_cvt_pk_bf16_f32 v231, v222, v223\n\t" \
  "v_exp_f32 v193, v193\n\t" \
  "v_exp_f32 v194, v194\n\t" \
  "v_mfma_f32_32x32x16_bf16 v[96:111], v[240:243], v[228:231], v[96:111]\n\t" \
  "ds_read_b128 v[240:243], %[a1] offset:2112\n\t" \
  "v_cvt_pk_bf16_f32 v224, v192, v193\n\t" \
  "v_exp_f32 v195, v195\n\t" \
  "v_exp_f32 v196, v196\n\t" \
  "v_cvt_pk_bf16_f32 v225, v194, v195\n\t" \
  "v_exp_f32 v197, v197\n\t" \
  "v_mfma_f32_32x32x16_bf16 v[208:223], v[244:247], v[116:119], 0\n\t" \
  "ds_read_b128 v[244:247], %[ka] offset:3072\n\t" \
  "v_exp_f32 v198, v198\n\t" \
  "v_cvt_pk_bf16_f32 v226, v196, v197\n\t" \
  "v_exp_f32 v199, v199\n\t" \
  "v_exp_f32 v200, v200\n\t" \
  "v_cvt_pk_bf16_f32 v227, v198, v199\n\t" \
  "v_exp_f32 v201, v201\n\t" \
  "v_exp_f32 v202, v202\n\t" \
  "v_cvt_pk_bf16_f32 v228, v200, v201\n\t" \
  "v_exp_f32 v203, v203\n\t" \
  "v_mfma_f32_32x32x16_bf16 v[80:95], v[248:251], v[224:227], v[80:95]\n\t" \
  "ds_read_b128 v[236:239], %[a0] offset:2112\n\t" \
  "v_exp_f32 v204, v204\n\t" \
  "v_cvt_pk_bf16_f32 v229, v202, v203\n\t" \
  "v_exp_f32 v205, v205\n\t" \
  "v_exp_f32 v206, v206\n\t" \
  "v_cvt_pk_bf16_f32 v230, v204, v205\n\t" \
  "v_exp_f32 v207, v207\n\t" \
  "v_exp_f32 v208, v208\n\t" \
  "v_cvt_pk_bf16_f32 v231, v206, v207\n\t" \
  "v_exp_f32 v209, v209\n\t" \
  "v_exp_f32 v210, v210\n\t" \
  "v_mfma_f32_32x32x16_bf16 v[80:95], v[252:255], v[228:231], v[80:95]\n\t" \
  "buffer_load_dwordx4 a[232:235], %[l16], %[rs], %[wa] offen offset:2048 sc1\n\t" \
  "v_cvt_pk_bf16_f32 v224, v208, v209\n\t" \
  "v_exp_f32 v211, v211\n\t" \
  "v_exp_f32 v212, v212\n\t" \
  "v_cvt_pk_bf16_f32 v225, v210, v211\n\t" \
  "v_exp_f32 v213, v213\n\t" \
  "v_mfma_f32_32x32x16_bf16 v[192:207], v[232:235], v[112:115], 0\n\t" \
  "buffer_load_dwordx4 a[236:239], %[l16], %[rs], %[wb] offen offset:2048 sc1\n\t" \
  "v_exp_f32 v214, v214\n\t" \
  "v_cvt_pk_bf16_f32 v226, v212, v213\n\t" \
  "v_exp_f32 v215, v215\n\t" \
  "v_exp_f32 v216, v216\n\t" \
  "v_cvt_pk_bf16_f32 v227, v214, v215\n\t" \
  "v_exp_f32 v217, v217\n\t" \
  "v_exp_f32 v218, v218\n\t" \
  "v_cvt_pk_bf16_f32 v228, v216, v217\n\t" \
  "v_exp_f32 v219, v219\n\t" \
  "v_mfma_f32_32x32x16_bf16 v[96:111], v[248:251], v[224:227], v[96:111]\n\t" \
  "s_waitcnt lgkmcnt(0)\n\t" \
  "v_exp_f32 v220, v220\n\t" \
  "v_cvt_pk_bf16_f32 v229, v218, v219\n\t" \
  "v_exp_f32 v221, v221\n\t" \
  "v_exp_f32 v222, v222\n\t" \
  "v_cvt_pk_bf16_f32 v230, v220, v221\n\t" \
  "v_exp_f32 v223, v223\n\t" \
  "v_exp_f32 v192, v192\n\t" \
  "v_cvt_pk_bf16_f32 v231, v222, v223\n\t" \
  "v_exp_f32 v193, v193\n\t" \
  "v_exp_f32 v194, v194\n\t" \
  "v_mfma_f32_32x32x16_bf16 v[96:111], v[252:255], v[228:231], v[96:111]\n\t" \
  "ds_read_b128 v[252:255], %[a1] offset:3168\n\t" \
  "v_cvt_pk_bf16_f32 v224, v192, v193\n\t" \
  "v_exp_f32 v195, v195\n\t" \
  "v_exp_f32 v196, v196\n\t" \
  "v_cvt_pk_bf16_f32 v225, v194, v195\n\t" \
  "v_exp_f32 v197, v197\n\t" \
  "v_mfma_f32_32x32x16_bf16 v[208:223], v[232:235], v[116:119], 0\n\t" \
  "ds_read_b128 v[232:235], %[ka] offset:4096\n\t" \
  "v_exp_f32 v198, v198\n\t" \
  "v_cvt_pk_bf16_f32 v226, v196, v197\n\t" \
  "v_exp_f32 v199, v199\n\t" \
  "v_exp_f32 v200, v200\n\t" \
  "v_cvt_pk_bf16_f32 v227, v198, v199\n\t" \
  "v_exp_f32 v201, v201\n\t" \
  "v_exp_f32 v202, v202\n\t" \
  "v_cvt_pk_bf16_f32 v228, v200, v201\n\t" \
  "v_exp_f32 v203, v203\n\t" \
  "v_mfma_f32_32x32x16_bf16 v[80:95], v[236:239], v[224:227], v[80:95]\n\t" \
  "ds_read_b128 v[248:251], %[a0] offset:3168\n\t" \
  "v_exp_f32 v204, v204\n\t" \
  "v_cvt_pk_bf16_f32 v229, v202, v203\n\t" \
  "v_exp_f32 v205, v205\n\t" \
  "v_exp_f32 v206, v206\n\t" \
  "v_cvt_pk_bf16_f32 v230, v204, v205\n\t" \
  "v_exp_f32 v207, v207\n\t" \
  "v_exp_f32 v208, v208\n\t" \
  "v_cvt_pk_bf16_f32 v231, v206, v207\n\t" \
  "v_exp_f32 v209, v209\n\t" \
  "v_exp_f32 v210, v210\n\t" \
  "v_mfma_f32_32x32x16_bf16 v[80:95], v[240:243], v[228:231], v[80:95]\n\t" \
  "buffer_load_dwordx4 a[240:243], %[l16], %[rs], %[wa] offen offset:3072 sc1\n\t" \
  "v_cvt_pk_bf16_f32 v224, v208, v209\n\t" \
  "v_exp_f32 v211, v211\n\t" \
  "v_exp_f32 v212, v212\n\t" \
  "v_cvt_pk_bf16_f32 v225, v210, v211\n\t" \
  "v_exp_f32 v213, v213\n\t" \
  "v_mfma_f32_32x32x16_bf16 v[192:207], v[244:247], v[112:115], 0\n\t" \
  "buffer_load_dwordx4 a[244:247], %[l16], %[rs], %[wb] offen offset:3072 sc1\n\t" \
  "v_exp_f32 v214, v214\n\t" \
  "v_cvt_pk_bf16_f32 v226, v212, v213\n\t" \
  "v_exp_f32 v215, v215\n\t" \
  "v_exp_f32 v216, v216\n\t" \
  "v_cvt_pk_bf16_f32 v227, v214, v215\n\t" \
  "v_exp_f32 v217, v217\n\t" \
  "v_exp_f32 v218, v218\n\t" \
  "v_cvt_pk_bf16_f32 v228, v216, v217\n\t" \
  "v_exp_f32 v219, v219\n\t" \
  "v_mfma_f32_32x32x16_bf16 v[96:111], v[236:239], v[224:227], v[96:111]\n\t" \
  "s_waitcnt lgkmcnt(0)\n\t" \
  "v_exp_f32 v220, v220\n\t" \
  "v_cvt_pk_bf16_f32 v229, v218, v219\n\t" \
  "v_exp_f32 v221, v221\n\t" \
  "v_exp_f32 v222, v222\n\t" \
  "v_cvt_pk_bf16_f32 v230, v220, v221\n\t" \
  "v_exp_f32 v223, v223\n\t" \
  "v_exp_f32 v192, v192\n\t" \
  "v_cvt_pk_bf16_f32 v231, v222, v223\n\t" \
  "v_exp_f32 v193, v193\n\t" \
  "v_exp_f32 v194, v194\n\t" \
  "v_mfma_f32_32x32x16_bf16 v[96:111], v[240:243], v[228:231], v[96:111]\n\t" \
  "ds_read_b128 v[240:243], %[a1] offset:4224\n\t" \
  "v_cvt_pk_bf16_f32 v224, v192, v193\n\t" \
  "v_exp_f32 v195, v195\n\t" \
  "v_exp_f32 v196, v196\n\t" \
  "v_cvt_pk_bf16_f32 v225, v194, v195\n\t" \
  "v_exp_f32 v197, v197\n\t" \
  "v_mfma_f32_32x32x16_bf16 v[208:223], v[244:247], v[116:119], 0\n\t" \
  "ds_read_b128 v[244:247], %[ka] offset:5120\n\t" \
  "v_exp_f32 v198, v198\n\t" \
  "v_cvt_pk_bf16_f32 v226, v196, v197\n\t" \
  "v_exp_f32 v199, v199\n\t" \
  "v_exp_f32 v200, v200\n\t" \
  "v_cvt_pk_bf16_f32 v227, v198, v199\n\t" \
  "v_exp_f32 v201, v201\n\t" \
  "v_exp_f32 v202, v202\n\t" \
  "v_cvt_pk_bf16_f32 v228, v200, v201\n\t" \
  "v_exp_f32 v203, v203\n\t" \
  "v_mfma_f32_32x32x16_bf16 v[80:95], v[248:251], v[224:227], v[80:95]\n\t" \
  "ds_read_b128 v[236:239], %[a0] offset:4224\n\t" \
  "v_exp_f32 v204, v204\n\t" \
  "v_cvt_pk_bf16_f32 v229, v202, v203\n\t" \
  "v_exp_f32 v205, v205\n\t" \
  "v_exp_f32 v206, v206\n\t" \
  "v_cvt_pk_bf16_f32 v230, v204, v205\n\t" \
  "v_exp_f32 v207, v207\n\t" \
  "v_exp_f32 v208, v208\n\t" \
  "v_cvt_pk_bf16_f32 v231, v206, v207\n\t" \
  "v_exp_f32 v209, v209\n\t" \
  "v_exp_f32 v210, v210\n\t" \
  "s_add_u32 %[wa], %[wa], 4096\n\t" \
  "v_mfma_f32_32x32x16_bf16 v[80:95], v[252:255], v[228:231], v[80:95]\n\t" \
  "buffer_load_dwordx4 a[248:251], %[l16], %[rs], %[wa] offen sc1\n\t" \
  "v_cvt_pk_bf16_f32 v224, v208, v209\n\t" \
  "v_exp_f32 v211, v211\n\t" \
  "v_exp_f32 v212, v212\n\t" \
  "v_cvt_pk_bf16_f32 v225, v210, v211\n\t" \
  "v_exp_f32 v213, v213\n\t" \
  "s_add_u32 %[wb], %[wb], 4096\n\t" \
  "v_mfma_f32_32x32x16_bf16 v[192:207], v[232:235], v[112:115], 0\n\t" \
  "buffer_load_dwordx4 a[252:255], %[l16], %[rs], %[wb] offen sc1\n\t" \
  "v_exp_f32 v214, v214\n\t" \
  "v_cvt_pk_bf16_f32 v226, v212, v213\n\t" \
  "v_exp_f32 v215, v215\n\t" \
  "v_exp_f32 v216, v216\n\t" \
  "v_cvt_pk_bf16_f32 v227, v214, v215\n\t" \
  "v_exp_f32 v217, v217\n\t" \
  "v_exp_f32 v218, v218\n\t" \
  "v_cvt_pk_bf16_f32 v228, v216, v217\n\t" \
  "v_exp_f32 v219, v219\n\t" \
  "v_mfma_f32_32x32x16_bf16 v[96:111], v[248:251], v[224:227], v[96:111]\n\t" \
  "s_waitcnt lgkmcnt(0)\n\t" \
  "v_exp_f32 v220, v220\n\t" \
  "v_cvt_pk_bf16_f32 v229, v218, v219\n\t" \
  "v_exp_f32 v221, v221\n\t" \
  "v_exp_f32 v222, v222\n\t" \
  "v_cvt_pk_bf16_f32 v230, v220, v221\n\t" \
  "v_exp_f32 v223, v223\n\t" \
  "v_exp_f32 v192, v192\n\t" \
  "v_cvt_pk_bf16_f32 v231, v222, v223\n\t" \
  "v_exp_f32 v193, v193\n\t" \
  "v_exp_f32 v194, v194\n\t" \
  "v_mfma_f32_32x32x16_bf16 v[96:111], v[252:255], v[228:231], v[96:111]\n\t" \
  "ds_read_b128 v[252:255], %[a1] offset:5280\n\t" \
  "v_cvt_pk_bf16_f32 v224, v192, v193\n\t" \
  "v_exp_f32 v195, v195\n\t" \
  "v_exp_f32 v196, v196\n\t" \
  "v_cvt_pk_bf16_f32 v225, v194, v195\n\t" \
  "v_exp_f32 v197, v197\n\t" \
  "v_mfma_f32_32x32x16_bf16 v[208:223], v[232:235], v[116:119], 0\n\t" \
  "ds_read_b128 v[232:235], %[ka] offset:6144\n\t" \
  "v_exp_f32 v198, v198\n\t" \
  "v_cvt_pk_bf16_f32 v226, v196, v197\n\t" \
  "v_exp_f32 v199, v199\n\t" \
  "v_exp_f32 v200, v200\n\t" \
  "v_cvt_pk_bf16_f32 v227, v198, v199\n\t" \
  "v_exp_f32 v201, v201\n\t" \
  "v_exp_f32 v202, v202\n\t" \
  "v_cvt_pk_bf16_f32 v228, v200, v201\n\t" \
  "v_exp_f32 v203, v203\n\t" \
  "v_mfma_f32_32x32x16_bf16 v[80:95], v[236:239], v[224:227], v[80:95]\n\t" \
  "ds_read_b128 v[248:251], %[a0] offset:5280\n\t" \
  "v_exp_f32 v204, v204\n\t" \
  "v_cvt_pk_bf16_f32 v229, v202, v203\n\t" \
  "v_exp_f32 v205, v205\n\t" \
  "v_exp_f32 v206, v206\n\t" \
  "v_cvt_pk_bf16_f32 v230, v204, v205\n\t" \
  "v_exp_f32 v207, v207\n\t" \
  "v_exp_f32 v208, v208\n\t" \
  "v_cvt_pk_bf16_f32 v231, v206, v207\n\t" \
  "v_exp_f32 v209, v209\n\t" \
  "v_exp_f32 v210, v210\n\t" \
  "v_mfma_f32_32x32x16_bf16 v[80:95], v[240:243], v[228:231], v[80:95]\n\t" \
  "v_cvt_pk_bf16_f32 v224, v208, v209\n\t" \
  "v_exp_f32 v211, v211\n\t" \
  "v_exp_f32 v212, v212\n\t" \
  "v_cvt_pk_bf16_f32 v225, v210, v211\n\t" \
  "v_exp_f32 v213, v213\n\t" \
  "v_mfma_f32_32x32x16_bf16 v[192:207], v[244:247], v[112:115], 0\n\t" \
  "v_exp_f32 v214, v214\n\t" \
  "v_cvt_pk_bf16_f32 v226, v212, v213\n\t" \
  "v_exp_f32 v215, v215\n\t" \
  "v_exp_f32 v216, v216\n\t" \
  "v_cvt_pk_bf16_f32 v227, v214, v215\n\t" \
  "v_exp_f32 v217, v217\n\t" \
  "v_exp_f32 v218, v218\n\t" \
  "v_cvt_pk_bf16_f32 v228, v216, v217\n\t" \
  "v_exp_f32 v219, v219\n\t" \
  "v_mfma_f32_32x32x16_bf16 v[96:111], v[236:239], v[224:227], v[96:111]\n\t" \
  "s_waitcnt lgkmcnt(0)\n\t" \
  "v_exp_f32 v220, v220\n\t" \
  "v_cvt_pk_bf16_f32 v229, v218, v219\n\t" \
  "v_exp_f32 v221, v221\n\t" \
  "v_exp_f32 v222, v222\n\t" \
  "v_cvt_pk_bf16_f32 v230, v220, v221\n\t" \
  "v_exp_f32 v223, v223\n\t" \
  "v_exp_f32 v192, v192\n\t" \
  "v_cvt_pk_bf16_f32 v231, v222, v223\n\t" \
  "v_exp_f32 v193, v193\n\t" \
  "v_exp_f32 v194, v194\n\t" \
  "v_mfma_f32_32x32x16_bf16 v[96:111], v[240:243], v[228:231], v[96:111]\n\t" \
  "ds_read_b128 v[240:243], %[a1] offset:6336\n\t" \
  "v_cvt_pk_bf16_f32 v224, v192, v193\n\t" \
  "v_exp_f32 v195, v195\n\t" \
  "v_exp_f32 v196, v196\n\t" \
  "v_cvt_pk_bf16_f32 v225, v194, v195\n\t" \
  "v_exp_f32 v197, v197\n\t" \
  "v_mfma_f32_32x32x16_bf16 v[208:223], v[244:247], v[116:119], 0\n\t" \
  "ds_read_b128 v[244:247], %[ka] offset:7168\n\t" \
  "v_exp_f32 v198, v198\n\t" \
  "v_cvt_pk_bf16_f32 v226, v196, v197\n\t" \
  "v_exp_f32 v199, v199\n\t" \
  "v_exp_f32 v200, v200\n\t" \
  "v_cvt_pk_bf16_f32 v227, v198, v199\n\t" \
  "v_exp_f32 v201, v201\n\t" \
  "v_exp_f32 v202, v202\n\t" \
  "v_cvt_pk_bf16_f32 v228, v200, v201\n\t" \
  "v_exp_f32 v203, v203\n\t" \
  "v_mfma_f32_32x32x16_bf16 v[80:95], v[248:251], v[224:227], v[80:95]\n\t" \
  "ds_read_b128 v[236:239], %[a0] offset:6336\n\t" \
  "v_exp_f32 v204, v204\n\t" \
  "v_cvt_pk_bf16_f32 v229, v202, v203\n\t" \
  "v_exp_f32 v205, v205\n\t" \
  "v_exp_f32 v206, v206\n\t" \
  "v_cvt_pk_bf16_f32 v230, v204, v205\n\t" \
  "v_exp_f32 v207, v207\n\t" \
  "v_exp_f32 v208, v208\n\t" \
  "v_cvt_pk_bf16_f32 v231, v206, v207\n\t" \
  "v_exp_f32 v209, v209\n\t" \
  "v_exp_f32 v210, v210\n\t" \
  "v_mfma_f32_32x32x16_bf16 v[80:95], v[252:255], v[228:231], v[80:95]\n\t" \
  "v_cvt_pk_bf16_f32 v224, v208, v209\n\t" \
  "v_exp_f32 v211, v211\n\t" \
  "v_exp_f32 v212, v212\n\t" \
  "v_cvt_pk_bf16_f32 v225, v210, v211\n\t" \
  "v_exp_f32 v213, v213\n\t" \
  "v_mfma_f32_32x32x16_bf16 v[192:207], v[232:235], v[112:115], 0\n\t" \
  "v_exp_f32 v214, v214\n\t" \
  "v_cvt_pk_bf16_f32 v226, v212, v213\n\t" \
  "v_exp_f32 v215, v215\n\t" \
  "v_exp_f32 v216, v216\n\t" \
  "v_cvt_pk_bf16_f32 v227, v214, v215\n\t" \
  "v_exp_f32 v217, v217\n\t" \
  "v_exp_f32 v218, v218\n\t" \
  "v_cvt_pk_bf16_f32 v228, v216, v217\n\t" \
  "v_exp_f32 v219, v219\n\t" \
  "v_mfma_f32_32x32x16_bf16 v[96:111], v[248:251], v[224:227], v[96:111]\n\t" \
  "s_waitcnt lgkmcnt(0)\n\t" \
  "v_exp_f32 v220, v220\n\t" \
  "v_cvt_pk_bf16_f32 v229, v218, v219\n\t" \
  "v_exp_f32 v221, v221\n\t" \
  "v_exp_f32 v222, v222\n\t" \
  "v_cvt_pk_bf16_f32 v230, v220, v221\n\t" \
  "v_exp_f32 v223, v223\n\t" \
  "v_exp_f32 v192, v192\n\t" \
  "v_cvt_pk_bf16_f32 v231, v222, v223\n\t" \
  "v_exp_f32 v193, v193\n\t" \
  "v_exp_f32 v194, v194\n\t" \
  "v_mfma_f32_32x32x16_bf16 v[96:111], v[252:255], v[228:231], v[96:111]\n\t" \
  "ds_read_b128 v[252:255], %[a1] offset:7392\n\t" \
  "v_cvt_pk_bf16_f32 v224, v192, v193\n\t" \
  "v_exp_f32 v195, v195\n\t" \
  "v_exp_f32 v196, v196\n\t" \
  "v_cvt_pk_bf16_f32 v225, v194, v195\n\t" \
  "v_exp_f32 v197, v197\n\t" \
  "v_mfma_f32_32x32x16_bf16 v[208:223], v[232:235], v[116:119], 0\n\t" \
  "ds_read_b128 v[232:235], %[ka] offset:8192\n\t" \
  "v_exp_f32 v198, v198\n\t" \
  "v_cvt_pk_bf16_f32 v226, v196, v197\n\t" \
  "v_exp_f32 v199, v199\n\t" \
  "v_exp_f32 v200, v200\n\t" \
  "v_cvt_pk_bf16_f32 v227, v198, v199\n\t" \
  "v_exp_f32 v201, v201\n\t" \
  "v_exp_f32 v202, v202\n\t" \
  "v_cvt_pk_bf16_f32 v228, v200, v201\n\t" \
  "v_exp_f32 v203, v203\n\t" \
  "v_mfma_f32_32x32x16_bf16 v[80:95], v[236:239], v[224:227], v[80:95]\n\t" \
  "ds_read_b128 v[248:251], %[a0] offset:7392\n\t" \
  "v_exp_f32 v204, v204\n\t" \
  "v_cvt_pk_bf16_f32 v229, v202, v203\n\t" \
  "v_exp_f32 v205, v205\n\t" \
  "v_exp_f32 v206, v206\n\t" \
  "v_cvt_pk_bf16_f32 v230, v204, v205\n\t" \
  "v_exp_f32 v207, v207\n\t" \
  "v_exp_f32 v208, v208\n\t" \
  "v_cvt_pk_bf16_f32 v231, v206, v207\n\t" \
  "v_exp_f32 v209, v209\n\t" \
  "v_exp_f32 v210, v210\n\t" \
  "v_mfma_f32_32x32x16_bf16 v[80:95], v[240:243], v[228:231], v[80:95]\n\t" \
  "v_cvt_pk_bf16_f32 v224, v208, v209\n\t" \
  "v_exp_f32 v211, v211\n\t" \
  "v_exp_f32 v212, v212\n\t" \
  "v_cvt_pk_bf16_f32 v225, v210, v211\n\t" \
  "v_exp_f32 v213, v213\n\t" \
  "v_mfma_f32_32x32x16_bf16 v[192:207], v[244:247], v[112:115], 0\n\t" \
  "v_exp_f32 v214, v214\n\t" \
  "v_cvt_pk_bf16_f32 v226, v212, v213\n\t" \
  "v_exp_f32 v215, v215\n\t" \
  "v_exp_f32 v216, v216\n\t" \
  "v_cvt_pk_bf16_f32 v227, v214, v215\n\t" \
  "v_exp_f32 v217, v217\n\t" \
  "v_exp_f32 v218, v218\n\t" \
  "v_cvt_pk_bf16_f32 v228, v216, v217\n\t" \
  "v_exp_f32 v219, v219\n\t" \
  "v_mfma_f32_32x32x16_bf16 v[96:111], v[236:239], v[224:227], v[96:111]\n\t" \
  "s_waitcnt lgkmcnt(0)\n\t" \
  "v_exp_f32 v220, v220\n\t" \
  "v_cvt_pk_bf16_f32 v229, v218, v219\n\t" \
  "v_exp_f32 v221, v221\n\t" \
  "v_exp_f32 v222, v222\n\t" \
  "v_cvt_pk_bf16_f32 v230, v220, v221\n\t" \
  "v_exp_f32 v223, v223\n\t" \
  "v_exp_f32 v192, v192\n\t" \
  "v_cvt_pk_bf16_f32 v231, v222, v223\n\t" \
  "v_exp_f32 v193, v193\n\t" \
  "v_exp_f32 v194, v194\n\t" \
  "v_mfma_f32_32x32x16_bf16 v[96:111], v[240:243], v[228:231], v[96:111]\n\t" \
  "ds_read_b128 v[240:243], %[a1] offset:8448\n\t" \
  "v_cvt_pk_bf16_f32 v224, v192, v193\n\t" \
  "v_exp_f32 v195, v195\n\t" \
  "v_exp_f32 v196, v196\n\t" \
  "v_cvt_pk_bf16_f32 v225, v194, v195\n\t" \
  "v_exp_f32 v197, v197\n\t" \
  "v_mfma_f32_32x32x16_bf16 v[208:223], v[244:247], v[116:119], 0\n\t" \
  "ds_read_b128 v[244:247], %[ka] offset:9216\n\t" \
  "v_exp_f32 v198, v198\n\t" \
  "v_cvt_pk_bf16_f32 v226, v196, v197\n\t" \
  "v_exp_f32 v199, v199\n\t" \
  "v_exp_f32 v200, v200\n\t" \
  "v_cvt_pk_bf16_f32 v227, v198, v199\n\t" \
  "v_exp_f32 v201, v201\n\t" \
  "v_exp_f32 v202, v202\n\t" \
  "v_cvt_pk_bf16_f32 v228, v200, v201\n\t" \
  "v_exp_f32 v203, v203\n\t" \
  "v_mfma_f32_32x32x16_bf16 v[80:95], v[248:251], v[224:227], v[80:95]\n\t" \
  "ds_read_b128 v[236:239], %[a0] offset:8448\n\t" \
  "v_exp_f32 v204, v204\n\t" \
  "v_cvt_pk_bf16_f32 v229, v202, v203\n\t" \
  "v_exp_f32 v205, v205\n\t" \
  "v_exp_f32 v206, v206\n\t" \
  "v_cvt_pk_bf16_f32 v230, v204, v205\n\t" \
  "v_exp_f32 v207, v207\n\t" \
  "v_exp_f32 v208, v208\n\t" \
  "v_cvt_pk_bf16_f32 v231, v206, v207\n\t" \
  "v_exp_f32 v209, v209\n\t" \
  "v_exp_f32 v210, v210\n\t" \
  "v_mfma_f32_32x32x16_bf16 v[80:95], v[252:255], v[228:231], v[80:95]\n\t" \
  "v_cvt_pk_bf16_f32 v224, v208, v209\n\t" \
  "v_exp_f32 v211, v211\n\t" \
  "v_exp_f32 v212, v212\n\t" \
  "v_cvt_pk_bf16_f32 v225, v210, v211\n\t" \
  "v_exp_f32 v213, v213\n\t" \
  "v_mfma_f32_32x32x16_bf16 v[192:207], v[232:235], v[112:115], 0\n\t" \
  "v_exp_f32 v214, v214\n\t" \
  "v_cvt_pk_bf16_f32 v226, v212, v213\n\t" \
  "v_exp_f32 v215, v215\n\t" \
  "v_exp_f32 v216, v216\n\t" \
  "v_cvt_pk_bf16_f32 v227, v214, v215\n\t" \
  "v_exp_f32 v217, v217\n\t" \
  "v_exp_f32 v218, v218\n\t" \
  "v_cvt_pk_bf16_f32 v228, v216, v217\n\t" \
  "v_exp_f32 v219, v219\n\t" \
  "v_mfma_f32_32x32x16_bf16 v[96:111], v[248:251], v[224:227], v[96:111]\n\t" \
  "s_waitcnt lgkmcnt(0)\n\t" \
  "v_exp_f32 v220, v220\n\t" \
  "v_cvt_pk_bf16_f32 v229, v218, v219\n\t" \
  "v_exp_f32 v221, v221\n\t" \
  "v_exp_f32 v222, v222\n\t" \
  "v_cvt_pk_bf16_f32 v230, v220, v221\n\t" \
  "v_exp_f32 v223, v223\n\t" \
  "v_add_u32 %[ka], 8192, %[ka]\n\t" \
  "v_add_u32 %[a0], 8448, %[a0]\n\t" \
  "v_add_u32 %[a1], 8448, %[a1]\n\t" \
  "v_exp_f32 v192, v192\n\t" \
  "v_cvt_pk_bf16_f32 v231, v222, v223\n\t" \
  "v_exp_f32 v193, v193\n\t" \
  "v_exp_f32 v194, v194\n\t" \
  "v_mfma_f32_32x32x16_bf16 v[96:111], v[252:255], v[228:231], v[96:111]\n\t" \
  "ds_read_b128 v[252:255], %[a1] offset:1056\n\t" \
  "v_cvt_pk_bf16_f32 v224, v192, v193\n\t" \
  "v_exp_f32 v195, v195\n\t" \
  "v_exp_f32 v196, v196\n\t" \
  "v_cvt_pk_bf16_f32 v225, v194, v195\n\t" \
  "v_exp_f32 v197, v197\n\t" \
  "v_mfma_f32_32x32x16_bf16 v[208:223], v[232:235], v[116:119], 0\n\t" \
  "ds_read_b128 v[232:235], %[ka] offset:2048\n\t" \
  "v_exp_f32 v198, v198\n\t" \
  "v_cvt_pk_bf16_f32 v226, v196, v197\n\t" \
  "v_exp_f32 v199, v199\n\t" \
  "v_exp_f32 v200, v200\n\t" \
  "v_cvt_pk_bf16_f32 v227, v198, v199\n\t" \
  "v_exp_f32 v201, v201\n\t" \
  "v_exp_f32 v202, v202\n\t" \
  "v_cvt_pk_bf16_f32 v228, v200, v201\n\t" \
  "v_exp_f32 v203, v203\n\t" \
  "v_mfma_f32_32x32x16_bf16 v[80:95], v[236:239], v[224:227], v[80:95]\n\t" \
  "ds_read_b128 v[248:251], %[a0] offset:1056\n\t" \
  "v_exp_f32 v204, v204\n\t" \
  "v_cvt_pk_bf16_f32 v229, v202, v203\n\t" \
  "v_exp_f32 v205, v205\n\t" \
  "v_exp_f32 v206, v206\n\t" \
  "v_cvt_pk_bf16_f32 v230, v204, v205\n\t" \
  "v_exp_f32 v207, v207\n\t" \
  "v_exp_f32 v208, v208\n\t" \
  "v_cvt_pk_bf16_f32 v231, v206, v207\n\t" \
  "v_exp_f32 v209, v209\n\t" \
  "v_exp_f32 v210, v210\n\t" \
  "v_mfma_f32_32x32x16_bf16 v[80:95], v[240:243], v[228:231], v[80:95]\n\t" \
  "v_cvt_pk_bf16_f32 v224, v208, v209\n\t" \
  "v_exp_f32 v211, v211\n\t" \
  "v_exp_f32 v212, v212\n\t" \
  "v_cvt_pk_bf16_f32 v225, v210, v211\n\t" \
  "v_exp_f32 v213, v213\n\t" \
  "v_mfma_f32_32x32x16_bf16 v[192:207], v[244:247], v[112:115], 0\n\t" \
  "v_exp_f32 v214, v214\n\t" \
  "v_cvt_pk_bf16_f32 v226, v212, v213\n\t" \
  "v_exp_f32 v215, v215\n\t" \
  "v_exp_f32 v216, v216\n\t" \
  "v_cvt_pk_bf16_f32 v227, v214, v215\n\t" \
  "v_exp_f32 v217, v217\n\t" \
  "v_exp_f32 v218, v218\n\t" \
  "v_cvt_pk_bf16_f32 v228, v216, v217\n\t" \
  "v_exp_f32 v219, v219\n\t" \
  "v_mfma_f32_32x32x16_bf16 v[96:111], v[236:239], v[224:227], v[96:111]\n\t" \
  "s_waitcnt lgkmcnt(0)\n\t" \
  "v_exp_f32 v220, v220\n\t" \
  "v_cvt_pk_bf16_f32 v229, v218, v219\n\t" \
  "v_exp_f32 v221, v221\n\t" \
  "v_exp_f32 v222, v222\n\t" \
  "v_cvt_pk_bf16_f32 v230, v220, v221\n\t" \
  "v_exp_f32 v223, v223\n\t" \
  "v_exp_f32 v192, v192\n\t" \
  "v_cvt_pk_bf16_f32 v231, v222, v223\n\t" \
  "v_exp_f32 v193, v193\n\t" \
  "v_exp_f32 v194, v194\n\t" \
  "v_mfma_f32_32x32x16_bf16 v[96:111], v[240:243], v[228:231], v[96:111]\n\t" \
  "ds_read_b128 v[240:243], %[a1] offset:2112\n\t" \
  "v_cvt_pk_bf16_f32 v224, v192, v193\n\t" \
  "v_exp_f32 v195, v195\n\t" \
  "v_exp_f32 v196, v196\n\t" \
  "v_cvt_pk_bf16_f32 v225, v194, v195\n\t" \
  "v_exp_f32 v197, v197\n\t" \
  "v_mfma_f32_32x32x16_bf16 v[208:223], v[244:247], v[116:119], 0\n\t" \
  "ds_read_b128 v[244:247], %[ka] offset:3072\n\t" \
  "v_exp_f32 v198, v198\n\t" \
  "v_cvt_pk_bf16_f32 v226, v196, v197\n\t" \
  "v_exp_f32 v199, v199\n\t" \
  "v_exp_f32 v200, v200\n\t" \
  "v_cvt_pk_bf16_f32 v227, v198, v199\n\t" \
  "v_exp_f32 v201, v201\n\t" \
  "v_exp_f32 v202, v202\n\t" \
  "v_cvt_pk_bf16_f32 v228, v200, v201\n\t" \
  "v_exp_f32 v203, v203\n\t" \
  "v_mfma_f32_32x32x16_bf16 v[80:95], v[248:251], v[224:227], v[80:95]\n\t" \
  "ds_read_b128 v[236:239], %[a0] offset:2112\n\t" \
  "v_exp_f32 v204, v204\n\t" \
  "v_cvt_pk_bf16_f32 v229, v202, v203\n\t" \
  "v_exp_f32 v205, v205\n\t" \
  "v_exp_f32 v206, v206\n\t" \
  "v_cvt_pk_bf16_f32 v230, v204, v205\n\t" \
  "v_exp_f32 v207, v207\n\t" \
  "v_exp_f32 v208, v208\n\t" \
  "v_cvt_pk_bf16_f32 v231, v206, v207\n\t" \
  "v_exp_f32 v209, v209\n\t" \
  "v_exp_f32 v210, v210\n\t" \
  "v_mfma_f32_32x32x16_bf16 v[80:95], v[252:255], v[228:231], v[80:95]\n\t" \
  "v_cvt_pk_bf16_f32 v224, v208, v209\n\t" \
  "v_exp_f32 v211, v211\n\t" \
  "v_exp_f32 v212, v212\n\t" \
  "v_cvt_pk_bf16_f32 v225, v210, v211\n\t" \
  "v_exp_f32 v213, v213\n\t" \
  "v_mfma_f32_32x32x16_bf16 v[192:207], v[232:235], v[112:115], 0\n\t" \
  "v_exp_f32 v214, v214\n\t" \
  "v_cvt_pk_bf16_f32 v226, v212, v213\n\t" \
  "v_exp_f32 v215, v215\n\t" \
  "v_exp_f32 v216, v216\n\t" \
  "v_cvt_pk_bf16_f32 v227, v214, v215\n\t" \
  "v_exp_f32 v217, v217\n\t" \
  "v_exp_f32 v218, v218\n\t" \
  "v_cvt_pk_bf16_f32 v228, v216, v217\n\t" \
  "v_exp_f32 v219, v219\n\t" \
  "v_mfma_f32_32x32x16_bf16 v[96:111], v[248:251], v[224:227], v[96:111]\n\t" \
  "s_waitcnt lgkmcnt(0)\n\t" \
  "v_exp_f32 v220, v220\n\t" \
  "v_cvt_pk_bf16_f32 v229, v218, v219\n\t" \
  "v_exp_f32 v221, v221\n\t" \
  "v_exp_f32 v222, v222\n\t" \
  "v_cvt_pk_bf16_f32 v230, v220, v221\n\t" \
  "v_exp_f32 v223, v223\n\t" \
  "v_exp_f32 v192, v192\n\t" \
  "v_cvt_pk_bf16_f32 v231, v222, v223\n\t" \
  "v_exp_f32 v193, v193\n\t" \
  "v_exp_f32 v194, v194\n\t" \
  "v_mfma_f32_32x32x16_bf16 v[96:111], v[252:255], v[228:231], v[96:111]\n\t" \
  "ds_read_b128 v[252:255], %[a1] offset:3168\n\t" \
  "v_cvt_pk_bf16_f32 v224, v192, v193\n\t" \
  "v_exp_f32 v195, v195\n\t" \
  "v_exp_f32 v196, v196\n\t" \
  "v_cvt_pk_bf16_f32 v225, v194, v195\n\t" \
  "v_exp_f32 v197, v197\n\t" \
  "v_mfma_f32_32x32x16_bf16 v[208:223], v[232:235], v[116:119], 0\n\t" \
  "ds_read_b128 v[232:235], %[ka] offset:4096\n\t" \
  "v_exp_f32 v198, v198\n\t" \
  "v_cvt_pk_bf16_f32 v226, v196, v197\n\t" \
  "v_exp_f32 v199, v199\n\t" \
  "v_exp_f32 v200, v200\n\t" \
  "v_cvt_pk_bf16_f32 v227, v198, v199\n\t" \
  "v_exp_f32 v201, v201\n\t" \
  "v_exp_f32 v202, v202\n\t" \
  "v_cvt_pk_bf16_f32 v228, v200, v201\n\t" \
  "v_exp_f32 v203, v203\n\t" \
  "v_mfma_f32_32x32x16_bf16 v[80:95], v[236:239], v[224:227], v[80:95]\n\t" \
  "ds_read_b128 v[248:251], %[a0] offset:3168\n\t" \
  "v_exp_f32 v204, v204\n\t" \
  "v_cvt_pk_bf16_f32 v229, v202, v203\n\t" \
  "v_exp_f32 v205, v205\n\t" \
  "v_exp_f32 v206, v206\n\t" \
  "v_cvt_pk_bf16_f32 v230, v204, v205\n\t" \
  "v_exp_f32 v207, v207\n\t" \
  "v_exp_f32 v208, v208\n\t" \
  "v_cvt_pk_bf16_f32 v231, v206, v207\n\t" \
  "v_exp_f32 v209, v209\n\t" \
  "v_exp_f32 v210, v210\n\t" \
  "v_mfma_f32_32x32x16_bf16 v[80:95], v[240:243], v[228:231], v[80:95]\n\t" \
  "v_cvt_pk_bf16_f32 v224, v208, v209\n\t" \
  "v_exp_f32 v211, v211\n\t" \
  "v_exp_f32 v212, v212\n\t" \
  "v_cvt_pk_bf16_f32 v225, v210, v211\n\t" \
  "v_exp_f32 v213, v213\n\t" \
  "v_mfma_f32_32x32x16_bf16 v[192:207], v[244:247], v[112:115], 0\n\t" \
  "v_exp_f32 v214, v214\n\t" \
  "v_cvt_pk_bf16_f32 v226, v212, v213\n\t" \
  "v_exp_f32 v215, v215\n\t" \
  "v_exp_f32 v216, v216\n\t" \
  "v_cvt_pk_bf16_f32 v227, v214, v215\n\t" \
  "v_exp_f32 v217, v217\n\t" \
  "v_exp_f32 v218, v218\n\t" \
  "v_cvt_pk_bf16_f32 v228, v216, v217\n\t" \
  "v_exp_f32 v219, v219\n\t" \
  "v_mfma_f32_32x32x16_bf16 v[96:111], v[236:239], v[224:227], v[96:111]\n\t" \
  "s_waitcnt lgkmcnt(0)\n\t" \
  "v_exp_f32 v220, v220\n\t" \
  "v_cvt_pk_bf16_f32 v229, v218, v219\n\t" \
  "v_exp_f32 v221, v221\n\t" \
  "v_exp_f32 v222, v222\n\t" \
  "v_cvt_pk_bf16_f32 v230, v220, v221\n\t" \
  "v_exp_f32 v223, v223\n\t" \
  "v_exp_f32 v192, v192\n\t" \
  "v_cvt_pk_bf16_f32 v231, v222, v223\n\t" \
  "v_exp_f32 v193, v193\n\t" \
  "v_exp_f32 v194, v194\n\t" \
  "v_mfma_f32_32x32x16_bf16 v[96:111], v[240:243], v[228:231], v[96:111]\n\t" \
  "ds_read_b128 v[240:243], %[a1] offset:4224\n\t" \
  "v_cvt_pk_bf16_f32 v224, v192, v193\n\t" \
  "v_exp_f32 v195, v195\n\t" \
  "v_exp_f32 v196, v196\n\t" \
  "v_cvt_pk_bf16_f32 v225, v194, v195\n\t" \
  "v_exp_f32 v197, v197\n\t" \
  "v_mfma_f32_32x32x16_bf16 v[208:223], v[244:247], v[116:119], 0\n\t" \
  "ds_read_b128 v[244:247], %[ka] offset:5120\n\t" \
  "v_exp_f32 v198, v198\n\t" \
  "v_cvt_pk_bf16_f32 v226, v196, v197\n\t" \
  "v_exp_f32 v199, v199\n\t" \
  "v_exp_f32 v200, v200\n\t" \
  "v_cvt_pk_bf16_f32 v227, v198, v199\n\t" \
  "v_exp_f32 v201, v201\n\t" \
  "v_exp_f32 v202, v202\n\t" \
  "v_cvt_pk_bf16_f32 v228, v200, v201\n\t" \
  "v_exp_f32 v203, v203\n\t" \
  "v_mfma_f32_32x32x16_bf16 v[80:95], v[248:251], v[224:227], v[80:95]\n\t" \
  "ds_read_b128 v[236:239], %[a0] offset:4224\n\t" \
  "v_exp_f32 v204, v204\n\t" \
  "v_cvt_pk_bf16_f32 v229, v202, v203\n\t" \
  "v_exp_f32 v205, v205\n\t" \
  "v_exp_f32 v206, v206\n\t" \
  "v_cvt_pk_bf16_f32 v230, v204, v205\n\t" \
  "v_exp_f32 v207, v207\n\t" \
  "v_exp_f32 v208, v208\n\t" \
  "v_cvt_pk_bf16_f32 v231, v206, v207\n\t" \
  "v_exp_f32 v209, v209\n\t" \
  "v_exp_f32 v210, v210\n\t" \
  "v_mfma_f32_32x32x16_bf16 v[80:95], v[252:255], v[228:231], v[80:95]\n\t" \
  "v_cvt_pk_bf16_f32 v224, v208, v209\n\t" \
  "v_exp_f32 v211, v211\n\t" \
  "v_exp_f32 v212, v212\n\t" \
  "v_cvt_pk_bf16_f32 v225, v210, v211\n\t" \
  "v_exp_f32 v213, v213\n\t" \
  "v_mfma_f32_32x32x16_bf16 v[192:207], v[232:235], v[112:115], 0\n\t" \
  "v_exp_f32 v214, v214\n\t" \
  "v_cvt_pk_bf16_f32 v226, v212, v213\n\t" \
  "v_exp_f32 v215, v215\n\t" \
  "v_exp_f32 v216, v216\n\t" \
  "v_cvt_pk_bf16_f32 v227, v214, v215\n\t" \
  "v_exp_f32 v217, v217\n\t" \
  "v_exp_f32 v218, v218\n\t" \
  "v_cvt_pk_bf16_f32 v228, v216, v217\n\t" \
  "v_exp_f32 v219, v219\n\t" \
  "v_mfma_f32_32x32x16_bf16 v[96:111], v[248:251], v[224:227], v[96:111]\n\t" \
  "s_waitcnt lgkmcnt(0)\n\t" \
  "v_exp_f32 v220, v220\n\t" \
  "v_cvt_pk_bf16_f32 v229, v218, v219\n\t" \
  "v_exp_f32 v221, v221\n\t" \
  "v_exp_f32 v222, v222\n\t" \
  "v_cvt_pk_bf16_f32 v230, v220, v221\n\t" \
  "v_exp_f32 v223, v223\n\t" \
  "v_exp_f32 v192, v192\n\t" \
  "v_cvt_pk_bf16_f32 v231, v222, v223\n\t" \
  "v_exp_f32 v193, v193\n\t" \
  "v_exp_f32 v194, v194\n\t" \
  "v_mfma_f32_32x32x16_bf16 v[96:111], v[252:255], v[228:231], v[96:111]\n\t" \
  "ds_read_b128 v[252:255], %[a1] offset:5280\n\t" \
  "v_cvt_pk_bf16_f32 v224, v192, v193\n\t" \
  "v_exp_f32 v195, v195\n\t" \
  "v_exp_f32 v196, v196\n\t" \
  "v_cvt_pk_bf16_f32 v225, v194, v195\n\t" \
  "v_exp_f32 v197, v197\n\t" \
  "v_mfma_f32_32x32x16_bf16 v[208:223], v[232:235], v[116:119], 0\n\t" \
  "ds_read_b128 v[232:235], %[ka] offset:6144\n\t" \
  "v_exp_f32 v198, v198\n\t" \
  "v_cvt_pk_bf16_f32 v226, v196, v197\n\t" \
  "v_exp_f32 v199, v199\n\t" \
  "v_exp_f32 v200, v200\n\t" \
  "v_cvt_pk_bf16_f32 v227, v198, v199\n\t" \
  "v_exp_f32 v201, v201\n\t" \
  "v_exp_f32 v202, v202\n\t" \
  "v_cvt_pk_bf16_f32 v228, v200, v201\n\t" \
  "v_exp_f32 v203, v203\n\t" \
  "v_mfma_f32_32x32x16_bf16 v[80:95], v[236:239], v[224:227], v[80:95]\n\t" \
  "ds_read_b128 v[248:251], %[a0] offset:5280\n\t" \
  "v_exp_f32 v204, v204\n\t" \
  "v_cvt_pk_bf16_f32 v229, v202, v203\n\t" \
  "v_exp_f32 v205, v205\n\t" \
  "v_exp_f32 v206, v206\n\t" \
  "v_cvt_pk_bf16_f32 v230, v204, v205\n\t" \
  "v_exp_f32 v207, v207\n\t" \
  "v_exp_f32 v208, v208\n\t" \
  "v_cvt_pk_bf16_f32 v231, v206, v207\n\t" \
  "v_exp_f32 v209, v209\n\t" \
  "v_exp_f32 v210, v210\n\t" \
  "v_mfma_f32_32x32x16_bf16 v[80:95], v[240:243], v[228:231], v[80:95]\n\t" \
  "v_cvt_pk_bf16_f32 v224, v208, v209\n\t" \
  "v_exp_f32 v211, v211\n\t" \
  "v_exp_f32 v212, v212\n\t" \
  "v_cvt_pk_bf16_f32 v225, v210, v211\n\t" \
  "v_exp_f32 v213, v213\n\t" \
  "v_mfma_f32_32x32x16_bf16 v[192:207], v[244:247], v[112:115], 0\n\t" \
  "v_exp_f32 v214, v214\n\t" \
  "v_cvt_pk_bf16_f32 v226, v212, v213\n\t" \
  "v_exp_f32 v215, v215\n\t" \
  "v_exp_f32 v216, v216\n\t" \
  "v_cvt_pk_bf16_f32 v227, v214, v215\n\t" \
  "v_exp_f32 v217, v217\n\t" \
  "v_exp_f32 v218, v218\n\t" \
  "v_cvt_pk_bf16_f32 v228, v216, v217\n\t" \
  "v_exp_f32 v219, v219\n\t" \
  "v_mfma_f32_32x32x16_bf16 v[96:111], v[236:239], v[224:227], v[96:111]\n\t" \
  "s_waitcnt lgkmcnt(0)\n\t" \
  "v_exp_f32 v220, v220\n\t" \
  "v_cvt_pk_bf16_f32 v229, v218, v219\n\t" \
  "v_exp_f32 v221, v221\n\t" \
  "v_exp_f32 v222, v222\n\t" \
  "v_cvt_pk_bf16_f32 v230, v220, v221\n\t" \
  "v_exp_f32 v223, v223\n\t" \
  "v_exp_f32 v192, v192\n\t" \
  "v_cvt_pk_bf16_f32 v231, v222, v223\n\t" \
  "v_exp_f32 v193, v193\n\t" \
  "v_exp_f32 v194, v194\n\t" \
  "v_mfma_f32_32x32x16_bf16 v[96:111], v[240:243], v[228:231], v[96:111]\n\t" \
  "ds_read_b128 v[240:243], %[a1] offset:6336\n\t" \
  "v_cvt_pk_bf16_f32 v224, v192, v193\n\t" \
  "v_exp_f32 v195, v195\n\t" \
  "v_exp_f32 v196, v196\n\t" \
  "v_cvt_pk_bf16_f32 v225, v194, v195\n\t" \
  "v_exp_f32 v197, v197\n\t" \
  "v_mfma_f32_32x32x16_bf16 v[208:223], v[244:247], v[116:119], 0\n\t" \
  "ds_read_b128 v[244:247], %[ka] offset:7168\n\t" \
  "v_exp_f32 v198, v198\n\t" \
  "v_cvt_pk_bf16_f32 v226, v196, v197\n\t" \
  "v_exp_f32 v199, v199\n\t" \
  "v_exp_f32 v200, v200\n\t" \
  "v_cvt_pk_bf16_f32 v227, v198, v199\n\t" \
  "v_exp_f32 v201, v201\n\t" \
  "v_exp_f32 v202, v202\n\t" \
  "v_cvt_pk_bf16_f32 v228, v200, v201\n\t" \
  "v_exp_f32 v203, v203\n\t" \
  "v_mfma_f32_32x32x16_bf16 v[80:95], v[248:251], v[224:227], v[80:95]\n\t" \
  "ds_read_b128 v[236:239], %[a0] offset:6336\n\t" \
  "v_exp_f32 v204, v204\n\t" \
  "v_cvt_pk_bf16_f32 v229, v202, v203\n\t" \
  "v_exp_f32 v205, v205\n\t" \
  "v_exp_f32 v206, v206\n\t" \
  "v_cvt_pk_bf16_f32 v230, v204, v205\n\t" \
  "v_exp_f32 v207, v207\n\t" \
  "v_exp_f32 v208, v208\n\t" \
  "v_cvt_pk_bf16_f32 v231, v206, v207\n\t" \
  "v_exp_f32 v209, v209\n\t" \
  "v_exp_f32 v210, v210\n\t" \
  "v_mfma_f32_32x32x16_bf16 v[80:95], v[252:255], v[228:231], v[80:95]\n\t" \
  "v_cvt_pk_bf16_f32 v224, v208, v209\n\t" \
  "v_exp_f32 v211, v211\n\t" \
  "v_exp_f32 v212, v212\n\t" \
  "v_cvt_pk_bf16_f32 v225, v210, v211\n\t" \
  "v_exp_f32 v213, v213\n\t" \
  "v_mfma_f32_32x32x16_bf16 v[192:207], v[232:235], v[112:115], 0\n\t" \
  "v_exp_f32 v214, v214\n\t" \
  "v_cvt_pk_bf16_f32 v226, v212, v213\n\t" \
  "v_exp_f32 v215, v215\n\t" \
  "v_exp_f32 v216, v216\n\t" \
  "v_cvt_pk_bf16_f32 v227, v214, v215\n\t" \
  "v_exp_f32 v217, v217\n\t" \
  "v_exp_f32 v218, v218\n\t" \
  "v_cvt_pk_bf16_f32 v228, v216, v217\n\t" \
  "v_exp_f32 v219, v219\n\t" \
  "v_mfma_f32_32x32x16_bf16 v[96:111], v[248:251], v[224:227], v[96:111]\n\t" \
  "s_waitcnt lgkmcnt(0)\n\t" \
  "v_exp_f32 v220, v220\n\t" \
  "v_cvt_pk_bf16_f32 v229, v218, v219\n\t" \
  "v_exp_f32 v221, v221\n\t" \
  "v_exp_f32 v222, v222\n\t" \
  "v_cvt_pk_bf16_f32 v230, v220, v221\n\t" \
  "v_exp_f32 v223, v223\n\t" \
  "v_exp_f32 v192, v192\n\t" \
  "v_cvt_pk_bf16_f32 v231, v222, v223\n\t" \
  "v_exp_f32 v193, v193\n\t" \
  "v_exp_f32 v194, v194\n\t" \
  "v_mfma_f32_32x32x16_bf16 v[96:111], v[252:255], v[228:231], v[96:111]\n\t" \
  "ds_read_b128 v[252:255], %[a1] offset:7392\n\t" \
  "v_cvt_pk_bf16_f32 v224, v192, v193\n\t" \
  "v_exp_f32 v195, v195\n\t" \
  "v_exp_f32 v196, v196\n\t" \
  "v_cvt_pk_bf16_f32 v225, v194, v195\n\t" \
  "v_exp_f32 v197, v197\n\t" \
  "v_mfma_f32_32x32x16_bf16 v[208:223], v[232:235], v[116:119], 0\n\t" \
  "ds_read_b128 v[232:235], %[ka] offset:8192\n\t" \
  "v_exp_f32 v198, v198\n\t" \
  "v_cvt_pk_bf16_f32 v226, v196, v197\n\t" \
  "v_exp_f32 v199, v199\n\t" \
  "v_exp_f32 v200, v200\n\t" \
  "v_cvt_pk_bf16_f32 v227, v198, v199\n\t" \
  "v_exp_f32 v201, v201\n\t" \
  "v_exp_f32 v202, v202\n\t" \
  "v_cvt_pk_bf16_f32 v228, v200, v201\n\t" \
  "v_exp_f32 v203, v203\n\t" \
  "v_mfma_f32_32x32x16_bf16 v[80:95], v[236:239], v[224:227], v[80:95]\n\t" \
  "ds_read_b128 v[248:251], %[a0] offset:7392\n\t" \
  "v_exp_f32 v204, v204\n\t" \
  "v_cvt_pk_bf16_f32 v229, v202, v203\n\t" \
  "v_exp_f32 v205, v205\n\t" \
  "v_exp_f32 v206, v206\n\t" \
  "v_cvt_pk_bf16_f32 v230, v204, v205\n\t" \
  "v_exp_f32 v207, v207\n\t" \
  "v_exp_f32 v208, v208\n\t" \
  "v_cvt_pk_bf16_f32 v231, v206, v207\n\t" \
  "v_exp_f32 v209, v209\n\t" \
  "v_exp_f32 v210, v210\n\t" \
  "v_mfma_f32_32x32x16_bf16 v[80:95], v[240:243], v[228:231], v[80:95]\n\t" \
  "v_cvt_pk_bf16_f32 v224, v208, v209\n\t" \
  "v_exp_f32 v211, v211\n\t" \
  "v_exp_f32 v212, v212\n\t" \
  "v_cvt_pk_bf16_f32 v225, v210, v211\n\t" \
  "v_exp_f32 v213, v213\n\t" \
  "v_mfma_f32_32x32x16_bf16 v[192:207], v[244:247], v[112:115], 0\n\t" \
  "v_exp_f32 v214, v214\n\t" \
  "v_cvt_pk_bf16_f32 v226, v212, v213\n\t" \
  "v_exp_f32 v215, v215\n\t" \
  "v_exp_f32 v216, v216\n\t" \
  "v_cvt_pk_bf16_f32 v227, v214, v215\n\t" \
  "v_exp_f32 v217, v217\n\t" \
  "v_exp_f32 v218, v218\n\t" \
  "v_cvt_pk_bf16_f32 v228, v216, v217\n\t" \
  "v_exp_f32 v219, v219\n\t" \
  "v_mfma_f32_32x32x16_bf16 v[96:111], v[236:239], v[224:227], v[96:111]\n\t" \
  "s_waitcnt lgkmcnt(0)\n\t" \
  "v_exp_f32 v220, v220\n\t" \
  "v_cvt_pk_bf16_f32 v229, v218, v219\n\t" \
  "v_exp_f32 v221, v221\n\t" \
  "v_exp_f32 v222, v222\n\t" \
  "v_cvt_pk_bf16_f32 v230, v220, v221\n\t" \
  "v_exp_f32 v223, v223\n\t" \
  "v_exp_f32 v192, v192\n\t" \
  "v_cvt_pk_bf16_f32 v231, v222, v223\n\t" \
  "v_exp_f32 v193, v193\n\t" \
  "v_exp_f32 v194, v194\n\t" \
  "v_mfma_f32_32x32x16_bf16 v[96:111], v[240:243], v[228:231], v[96:111]\n\t" \
  "ds_read_b128 v[240:243], %[a1] offset:8448\n\t" \
  "v_cvt_pk_bf16_f32 v224, v192, v193\n\t" \
  "v_exp_f32 v195, v195\n\t" \
  "v_exp_f32 v196, v196\n\t" \
  "v_cvt_pk_bf16_f32 v225, v194, v195\n\t" \
  "v_exp_f32 v197, v197\n\t" \
  "v_mfma_f32_32x32x16_bf16 v[208:223], v[244:247], v[116:119], 0\n\t" \
  "ds_read_b128 v[244:247], %[ka] offset:9216\n\t" \
  "v_exp_f32 v198, v198\n\t" \
  "v_cvt_pk_bf16_f32 v226, v196, v197\n\t" \
  "v_exp_f32 v199, v199\n\t" \
  "v_exp_f32 v200, v200\n\t" \
  "v_cvt_pk_bf16_f32 v227, v198, v199\n\t" \
  "v_exp_f32 v201, v201\n\t" \
  "v_exp_f32 v202, v202\n\t" \
  "v_cvt_pk_bf16_f32 v228, v200, v201\n\t" \
  "v_exp_f32 v203, v203\n\t" \
  "v_mfma_f32_32x32x16_bf16 v[80:95], v[248:251], v[224:227], v[80:95]\n\t" \
  "ds_read_b128 v[236:239], %[a0] offset:8448\n\t" \
  "v_exp_f32 v204, v204\n\t" \
  "v_cvt_pk_bf16_f32 v229, v202, v203\n\t" \
  "v_exp_f32 v205, v205\n\t" \
  "v_exp_f32 v206, v206\n\t" \
  "v_cvt_pk_bf16_f32 v230, v204, v205\n\t" \
  "v_exp_f32 v207, v207\n\t" \
  "v_exp_f32 v208, v208\n\t" \
  "v_cvt_pk_bf16_f32 v231, v206, v207\n\t" \
  "v_exp_f32 v209, v209\n\t" \
  "v_exp_f32 v210, v210\n\t" \
  "v_mfma_f32_32x32x16_bf16 v[80:95], v[252:255], v[228:231], v[80:95]\n\t" \
  "v_cvt_pk_bf16_f32 v224, v208, v209\n\t" \
  "v_exp_f32 v211, v211\n\t" \
  "v_exp_f32 v212, v212\n\t" \
  "v_cvt_pk_bf16_f32 v225, v210, v211\n\t" \
  "v_exp_f32 v213, v213\n\t" \
  "v_mfma_f32_32x32x16_bf16 v[192:207], v[232:235], v[112:115], 0\n\t" \
  "v_exp_f32 v214, v214\n\t" \
  "v_cvt_pk_bf16_f32 v226, v212, v213\n\t" \
  "v_exp_f32 v215, v215\n\t" \
  "v_exp_f32 v216, v216\n\t" \
  "v_cvt_pk_bf16_f32 v227, v214, v215\n\t" \
  "v_exp_f32 v217, v217\n\t" \
  "v_exp_f32 v218, v218\n\t" \
  "v_cvt_pk_bf16_f32 v228, v216, v217\n\t" \
  "v_exp_f32 v219, v219\n\t" \
  "v_mfma_f32_32x32x16_bf16 v[96:111], v[248:251], v[224:227], v[96:111]\n\t" \
  "s_waitcnt lgkmcnt(0)\n\t" \
  "v_exp_f32 v220, v220\n\t" \
  "v_cvt_pk_bf16_f32 v229, v218, v219\n\t" \
  "v_exp_f32 v221, v221\n\t" \
  "v_exp_f32 v222, v222\n\t" \
  "v_cvt_pk_bf16_f32 v230, v220, v221\n\t" \
  "v_exp_f32 v223, v223\n\t" \
  "v_add_u32 %[ka], 8192, %[ka]\n\t" \
  "v_add_u32 %[a0], 8448, %[a0]\n\t" \
  "v_add_u32 %[a1], 8448, %[a1]\n\t" \
  "s_nop 0\n\t" \
  "v_cvt_pk_bf16_f32 v231, v222, v223\n\t" \
  "s_nop 1\n\t" \
  "s_waitcnt lgkmcnt(0)\n\t" \
  "v_mfma_f32_32x32x16_bf16 v[96:111], v[252:255], v[228:231], v[96:111]\n\t" \
  "s_nop 7\n\t" \
  "s_nop 7\n\t"
#define ATTN_TRIPS 0
#define ATT_INIT_ASM \
  "v_mov_b32 v80, 0\n\t" \
  "v_mov_b32 v81, 0\n\t" \
  "v_mov_b32 v82, 0\n\t" \
  "v_mov_b32 v83, 0\n\t" \
  "v_mov_b32 v84, 0\n\t" \
  "v_mov_b32 v85, 0\n\t" \
  "v_mov_b32 v86, 0\n\t" \
  "v_mov_b32 v87, 0\n\t" \
  "v_mov_b32 v88, 0\n\t" \
  "v_mov_b32 v89, 0\n\t" \
  "v_mov_b32 v90, 0\n\t" \
  "v_mov_b32 v91, 0\n\t" \
  "v_mov_b32 v92, 0\n\t" \
  "v_mov_b32 v93, 0\n\t" \
  "v_mov_b32 v94, 0\n\t" \
  "v_mov_b32 v95, 0\n\t" \
  "v_mov_b32 v96, 0\n\t" \
  "v_mov_b32 v97, 0\n\t" \
  "v_mov_b32 v98, 0\n\t" \
  "v_mov_b32 v99, 0\n\t" \
  "v_mov_b32 v100, 0\n\t" \
  "v_mov_b32 v101, 0\n\t" \
  "v_mov_b32 v102, 0\n\t" \
  "v_mov_b32 v103, 0\n\t" \
  "v_mov_b32 v104, 0\n\t" \
  "v_mov_b32 v105, 0\n\t" \
  "v_mov_b32 v106, 0\n\t" \
  "v_mov_b32 v107, 0\n\t" \
  "v_mov_b32 v108, 0\n\t" \
  "v_mov_b32 v109, 0\n\t" \
  "v_mov_b32 v110, 0\n\t" \
  "v_mov_b32 v111, 0\n\t" \
  "v_mov_b32 v252, 0\n\t" \
  "v_mov_b32 v253, 0\n\t" \
  "v_mov_b32 v254, 0\n\t" \
  "v_mov_b32 v255, 0\n\t" \
  "v_mov_b32 v208, 0\n\t" \
  "v_mov_b32 v209, 0\n\t" \
  "v_mov_b32 v210, 0\n\t" \
  "v_mov_b32 v211, 0\n\t" \
  "v_mov_b32 v212, 0\n\t" \
  "v_mov_b32 v213, 0\n\t" \
  "v_mov_b32 v214, 0\n\t" \
  "v_mov_b32 v215, 0\n\t" \
  "v_mov_b32 v216, 0\n\t" \
  "v_mov_b32 v217, 0\n\t" \
  "v_mov_b32 v218, 0\n\t" \
  "v_mov_b32 v219, 0\n\t" \
  "v_mov_b32 v220, 0\n\t" \
  "v_mov_b32 v221, 0\n\t" \
  "v_mov_b32 v222, 0\n\t" \
  "v_mov_b32 v223, 0\n\t" \
  "v_mov_b32 v228, 0\n\t" \
  "v_mov_b32 v229, 0\n\t" \
  "v_mov_b32 v230, 0\n\t" \
  "v_mov_b32 v231, 0\n\t"
#define ATT_Q_ASM \
  "s_nop 4\n\t" \
  "buffer_load_dwordx4 v[112:115], %[l16], %[rs], %[qo] offen sc1\n\t" \
  "buffer_load_dwordx4 v[116:119], %[l16], %[rs], %[qo] offen offset:1024 sc1\n\t" \
  "buffer_load_dword v244, %[l4], %[rs], %[ko] offen sc1\n\t"
#define ATT_PRE_ASM \
  "s_waitcnt vmcnt(10)\n\t" \
  "v_lshlrev_b32 v247, 16, v112\n\t" \
  "v_and_b32 v248, 0xffff0000, v112\n\t" \
  "v_fma_f32 v245, v247, v247, 0\n\t" \
  "v_fmac_f32 v245, v248, v248\n\t" \
  "v_lshlrev_b32 v247, 16, v113\n\t" \
  "v_and_b32 v248, 0xffff0000, v113\n\t" \
  "v_fma_f32 v245, v247, v247, v245\n\t" \
  "v_fmac_f32 v245, v248, v248\n\t" \
  "v_lshlrev_b32 v247, 16, v114\n\t" \
  "v_and_b32 v248, 0xffff0000, v114\n\t" \
  "v_fma_f32 v245, v247, v247, v245\n\t" \
  "v_fmac_f32 v245, v248, v248\n\t" \
  "v_lshlrev_b32 v247, 16, v115\n\t" \
  "v_and_b32 v248, 0xffff0000, v115\n\t" \
  "v_fma_f32 v245, v247, v247, v245\n\t" \
  "v_fmac_f32 v245, v248, v248\n\t" \
  "v_lshlrev_b32 v247, 16, v116\n\t" \
  "v_and_b32 v248, 0xffff0000, v116\n\t" \
  "v_fma_f32 v246, v247, v247, 0\n\t" \
  "v_fmac_f32 v246, v248, v248\n\t" \
  "v_lshlrev_b32 v247, 16, v117\n\t" \
  "v_and_b32 v248, 0xffff0000, v117\n\t" \
  "v_fma_f32 v246, v247, v247, v246\n\t" \
  "v_fmac_f32 v246, v248, v248\n\t" \
  "v_lshlrev_b32 v247, 16, v118\n\t" \
  "v_and_b32 v248, 0xffff0000, v118\n\t" \
  "v_fma_f32 v246, v247, v247, v246\n\t" \
  "v_fmac_f32 v246, v248, v248\n\t" \
  "v_lshlrev_b32 v247, 16, v119\n\t" \
  "v_and_b32 v248, 0xffff0000, v119\n\t" \
  "v_fma_f32 v246, v247, v247, v246\n\t" \
  "v_fmac_f32 v246, v248, v248\n\t" \
  "v_mov_b32 v247, v245\n\t" \
  "s_nop 1\n\t" \
  "v_permlane32_swap_b32 v245, v247\n\t" \
  "v_add_f32 v245, v245, v247\n\t" \
  "v_mov_b32 v247, v246\n\t" \
  "s_nop 1\n\t" \
  "v_permlane32_swap_b32 v246, v247\n\t" \
  "v_add_f32 v246, v246, v247\n\t" \
  "v_max_f32 v245, v245, v246\n\t" \
  "v_max_f32_dpp v244, v244, v244 row_shr:1 row_mask:0xf bank_mask:0xf\n\t" \
  "s_nop 1\n\t" \
  "v_max_f32_dpp v244, v244, v244 row_shr:2 row_mask:0xf bank_mask:0xf\n\t" \
  "s_nop 1\n\t" \
  "v_max_f32_dpp v244, v244, v244 row_shr:4 row_mask:0xf bank_mask:0xf\n\t" \
  "s_nop 1\n\t" \
  "v_max_f32_dpp v244, v244, v244 row_shr:8 row_mask:0xf bank_mask:0xf\n\t" \
  "s_nop 1\n\t" \
  "v_max_f32_dpp v244, v244, v244 row_bcast:15 row_mask:0xa bank_mask:0xf\n\t" \
  "s_nop 1\n\t" \
  "v_readlane_b32 %[t0], v244, 31\n\t" \
  "v_readlane_b32 %[t1], v244, 63\n\t" \
  "s_nop 3\n\t" \
  "s_max_u32 %[t0], %[t0], %[t1]\n\t" \
  "s_nop 1\n\t" \
  "v_mul_f32 v245, %[t0], v245\n\t" \
  "v_mov_b32 v247, %[bnd]\n\t" \
  "s_nop 0\n\t" \
  "v_cmp_le_f32 vcc, v245, v247\n\t" \
  "s_nop 4\n\t" \
  "s_cmp_eq_u64 vcc, exec\n\t" \
  "s_cselect_b32 %[fast], 1, 0\n\t" \
  "s_waitcnt vmcnt(0) lgkmcnt(0)\n\t" \
  "s_barrier\n\t"
#define ATT_SLOW_ASM \
  "v_mov_b32 v80, 0\n\t" \
  "v_mov_b32 v81, 0\n\t" \
  "v_mov_b32 v82, 0\n\t" \
  "v_mov_b32 v83, 0\n\t" \
  "v_mov_b32 v84, 0\n\t" \
  "v_mov_b32 v85, 0\n\t" \
  "v_mov_b32 v86, 0\n\t" \
  "v_mov_b32 v87, 0\n\t" \
  "v_mov_b32 v88, 0\n\t" \
  "v_mov_b32 v89, 0\n\t" \
  "v_mov_b32 v90, 0\n\t" \
  "v_mov_b32 v91, 0\n\t" \
  "v_mov_b32 v92, 0\n\t" \
  "v_mov_b32 v93, 0\n\t" \
  "v_mov_b32 v94, 0\n\t" \
  "v_mov_b32 v95, 0\n\t" \
  "v_mov_b32 v96, 0\n\t" \
  "v_mov_b32 v97, 0\n\t" \
  "v_mov_b32 v98, 0\n\t" \
  "v_mov_b32 v99, 0\n\t" \
  "v_mov_b32 v100, 0\n\t" \
  "v_mov_b32 v101, 0\n\t" \
  "v_mov_b32 v102, 0\n\t" \
  "v_mov_b32 v103, 0\n\t" \
  "v_mov_b32 v104, 0\n\t" \
  "v_mov_b32 v105, 0\n\t" \
  "v_mov_b32 v106, 0\n\t" \
  "v_mov_b32 v107, 0\n\t" \
  "v_mov_b32 v108, 0\n\t" \
  "v_mov_b32 v109, 0\n\t" \
  "v_mov_b32 v110, 0\n\t" \
  "v_mov_b32 v111, 0\n\t" \
  "v_mov_b32 v244, 0xff7fffff\n\t" \
  "v_mov_b32 v245, 0xff7fffff\n\t" \
  "s_mov_b32 %[cnt], 64\n\t" \
  "1:\n\t" \
  "ds_read_b128 v[232:235], %[ka]\n\t" \
  "ds_read_b128 v[236:239], %[a0]\n\t" \
  "ds_read_b128 v[240:243], %[a1]\n\t" \
  "s_waitcnt lgkmcnt(0)\n\t" \
  "v_mfma_f32_32x32x16_bf16 v[192:207], v[232:235], v[112:115], 0\n\t" \
  "s_nop 7\n\t" \
  "s_nop 3\n\t" \
  "v_max3_f32 v246, v192, v193, v194\n\t" \
  "v_max3_f32 v246, v246, v195, v196\n\t" \
  "v_max3_f32 v246, v246, v197, v198\n\t" \
  "v_max3_f32 v246, v246, v199, v200\n\t" \
  "v_max3_f32 v246, v246, v201, v202\n\t" \
  "v_max3_f32 v246, v246, v203, v204\n\t" \
  "v_max3_f32 v246, v246, v205, v206\n\t" \
  "v_max_f32 v246, v246, v207\n\t" \
  "v_mov_b32 v247, v246\n\t" \
  "s_nop 1\n\t" \
  "v_permlane32_swap_b32 v246, v247\n\t" \
  "v_max3_f32 v246, v246, v247, v244\n\t" \
  "v_sub_f32 v247, v244, v246\n\t" \
  "v_exp_f32 v247, v247\n\t" \
  "v_mov_b32 v244, v246\n\t" \
  "v_sub_f32 v192, v192, v246\n\t" \
  "v_sub_f32 v193, v193, v246\n\t" \
  "v_sub_f32 v194, v194, v246\n\t" \
  "v_sub_f32 v195, v195, v246\n\t" \
  "v_sub_f32 v196, v196, v246\n\t" \
  "v_sub_f32 v197, v197, v246\n\t" \
  "v_sub_f32 v198, v198, v246\n\t" \
  "v_sub_f32 v199, v199, v246\n\t" \
  "v_sub_f32 v200, v200, v246\n\t" \
  "v_sub_f32 v201, v201, v246\n\t" \
  "v_sub_f32 v202, v202, v246\n\t" \
  "v_sub_f32 v203, v203, v246\n\t" \
  "v_sub_f32 v204, v204, v246\n\t" \
  "v_sub_f32 v205, v205, v246\n\t" \
  "v_sub_f32 v206, v206, v246\n\t" \
  "v_sub_f32 v207, v207, v246\n\t" \
  "v_mul_f32 v80, v247, v80\n\t" \
  "v_mul_f32 v81, v247, v81\n\t" \
  "v_mul_f32 v82, v247, v82\n\t" \
  "v_mul_f32 v83, v247, v83\n\t" \
  "v_mul_f32 v84, v247, v84\n\t" \
  "v_mul_f32 v85, v247, v85\n\t" \
  "v_mul_f32 v86, v247, v86\n\t" \
  "v_mul_f32 v87, v247, v87\n\t" \
  "v_mul_f32 v88, v247, v88\n\t" \
  "v_mul_f32 v89, v247, v89\n\t" \
  "v_mul_f32 v90, v247, v90\n\t" \
  "v_mul_f32 v91, v247, v91\n\t" \
  "v_mul_f32 v92, v247, v92\n\t" \
  "v_mul_f32 v93, v247, v93\n\t" \
  "v_mul_f32 v94, v247, v94\n\t" \
  "v_mul_f32 v95, v247, v95\n\t" \
  "v_exp_f32 v192, v192\n\t" \
  "v_exp_f32 v193, v193\n\t" \
  "v_exp_f32 v194, v194\n\t" \
  "v_exp_f32 v195, v195\n\t" \
  "v_exp_f32 v196, v196\n\t" \
  "v_exp_f32 v197, v197\n\t" \
  "v_exp_f32 v198, v198\n\t" \
  "v_exp_f32 v199, v199\n\t" \
  "v_exp_f32 v200, v200\n\t" \
  "v_exp_f32 v201, v201\n\t" \
  "v_exp_f32 v202, v202\n\t" \
  "v_exp_f32 v203, v203\n\t" \
  "v_exp_f32 v204, v204\n\t" \
  "v_exp_f32 v205, v205\n\t" \
  "v_exp_f32 v206, v206\n\t" \
  "v_exp_f32 v207, v207\n\t" \
  "v_cvt_pk_bf16_f32 v224, v192, v193\n\t" \
  "v_cvt_pk_bf16_f32 v225, v194, v195\n\t" \
  "v_cvt_pk_bf16_f32 v226, v196, v197\n\t" \
  "v_cvt_pk_bf16_f32 v227, v198, v199\n\t" \
  "v_cvt_pk_bf16_f32 v228, v200, v201\n\t" \
  "v_cvt_pk_bf16_f32 v229, v202, v203\n\t" \
  "v_cvt_pk_bf16_f32 v230, v204, v205\n\t" \
  "v_cvt_pk_bf16_f32 v231, v206, v207\n\t" \
  "v_mfma_f32_32x32x16_bf16 v[80:95], v[236:239], v[224:227], v[80:95]\n\t" \
  "s_nop 0\n\t" \
  "v_mfma_f32_32x32x16_bf16 v[80:95], v[240:243], v[228:231], v[80:95]\n\t" \
  "v_mfma_f32_32x32x16_bf16 v[192:207], v[232:235], v[116:119], 0\n\t" \
  "s_nop 7\n\t" \
  "s_nop 3\n\t" \
  "v_max3_f32 v246, v192, v193, v194\n\t" \
  "v_max3_f32 v246, v246, v195, v196\n\t" \
  "v_max3_f32 v246, v246, v197, v198\n\t" \
  "v_max3_f32 v246, v246, v199, v200\n\t" \
  "v_max3_f32 v246, v246, v201, v202\n\t" \
  "v_max3_f32 v246, v246, v203, v204\n\t" \
  "v_max3_f32 v246, v246, v205, v206\n\t" \
  "v_max_f32 v246, v246, v207\n\t" \
  "v_mov_b32 v247, v246\n\t" \
  "s_nop 1\n\t" \
  "v_permlane32_swap_b32 v246, v247\n\t" \
  "v_max3_f32 v246, v246, v247, v245\n\t" \
  "v_sub_f32 v247, v245, v246\n\t" \
  "v_exp_f32 v247, v247\n\t" \
  "v_mov_b32 v245, v246\n\t" \
  "v_sub_f32 v192, v192, v246\n\t" \
  "v_sub_f32 v193, v193, v246\n\t" \
  "v_sub_f32 v194, v194, v246\n\t" \
  "v_sub_f32 v195, v195, v246\n\t" \
  "v_sub_f32 v196, v196, v246\n\t" \
  "v_sub_f32 v197, v197, v246\n\t" \
  "v_sub_f32 v198, v198, v246\n\t" \
  "v_sub_f32 v199, v199, v246\n\t" \
  "v_sub_f32 v200, v200, v246\n\t" \
  "v_sub_f32 v201, v201, v246\n\t" \
  "v_sub_f32 v202, v202, v246\n\t" \
  "v_sub_f32 v203, v203, v246\n\t" \
  "v_sub_f32 v204, v204, v246\n\t" \
  "v_sub_f32 v205, v205, v246\n\t" \
  "v_sub_f32 v206, v206, v246\n\t" \
  "v_sub_f32 v207, v207, v246\n\t" \
  "v_mul_f32 v96, v247, v96\n\t" \
  "v_mul_f32 v97, v247, v97\n\t" \
  "v_mul_f32 v98, v247, v98\n\t" \
  "v_mul_f32 v99, v247, v99\n\t" \
  "v_mul_f32 v100, v247, v100\n\t" \
  "v_mul_f32 v101, v247, v101\n\t" \
  "v_mul_f32 v102, v247, v102\n\t" \
  "v_mul_f32 v103, v247, v103\n\t" \
  "v_mul_f32 v104, v247, v104\n\t" \
  "v_mul_f32 v105, v247, v105\n\t" \
  "v_mul_f32 v106, v247, v106\n\t" \
  "v_mul_f32 v107, v247, v107\n\t" \
  "v_mul_f32 v108, v247, v108\n\t" \
  "v_mul_f32 v109, v247, v109\n\t" \
  "v_mul_f32 v110, v247, v110\n\t" \
  "v_mul_f32 v111, v247, v111\n\t" \
  "v_exp_f32 v192, v192\n\t" \
  "v_exp_f32 v193, v193\n\t" \
  "v_exp_f32 v194, v194\n\t" \
  "v_exp_f32 v195, v195\n\t" \
  "v_exp_f32 v196, v196\n\t" \
  "v_exp_f32 v197, v197\n\t" \
  "v_exp_f32 v198, v198\n\t" \
  "v_exp_f32 v199, v199\n\t" \
  "v_exp_f32 v200, v200\n\t" \
  "v_exp_f32 v201, v201\n\t" \
  "v_exp_f32 v202, v202\n\t" \
  "v_exp_f32 v203, v203\n\t" \
  "v_exp_f32 v204, v204\n\t" \
  "v_exp_f32 v205, v205\n\t" \
  "v_exp_f32 v206, v206\n\t" \
  "v_exp_f32 v207, v207\n\t" \
  "v_cvt_pk_bf16_f32 v224, v192, v193\n\t" \
  "v_cvt_pk_bf16_f32 v225, v194, v195\n\t" \
  "v_cvt_pk_bf16_f32 v226, v196, v197\n\t" \
  "v_cvt_pk_bf16_f32 v227, v198, v199\n\t" \
  "v_cvt_pk_bf16_f32 v228, v200, v201\n\t" \
  "v_cvt_pk_bf16_f32 v229, v202, v203\n\t" \
  "v_cvt_pk_bf16_f32 v230, v204, v205\n\t" \
  "v_cvt_pk_bf16_f32 v231, v206, v207\n\t" \
  "v_mfma_f32_32x32x16_bf16 v[96:111], v[236:239], v[224:227], v[96:111]\n\t" \
  "s_nop 0\n\t" \
  "v_mfma_f32_32x32x16_bf16 v[96:111], v[240:243], v[228:231], v[96:111]\n\t" \
  "v_add_u32 %[ka], 1024, %[ka]\n\t" \
  "v_add_u32 %[a0], 1056, %[a0]\n\t" \
  "v_add_u32 %[a1], 1056, %[a1]\n\t" \
  "s_nop 7\n\t" \
  "s_nop 7\n\t" \
  "s_sub_u32 %[cnt], %[cnt], 1\n\t" \
  "s_cmp_lg_u32 %[cnt], 0\n\t" \
  "s_cbranch_scc1 1b\n\t" \
  "s_nop 7\n\t" \
  "s_nop 7\n\t"
#define ATT_SLOWLD_ASM \
  "s_nop 4\n\t" \
  "s_mov_b32 m0, %[mk]\n\t" \
  "s_nop 0\n\t" \
  "buffer_load_dwordx4 %[l16], %[rs], %[gk] offen sc1 lds\n\t" \
  "s_add_u32 m0, m0, 4096\n\t" \
  "s_add_u32 %[gk], %[gk], 4096\n\t" \
  "buffer_load_dwordx4 %[l16], %[rs], %[gk] offen sc1 lds\n\t" \
  "s_add_u32 m0, m0, 4096\n\t" \
  "s_add_u32 %[gk], %[gk], 4096\n\t" \
  "buffer_load_dwordx4 %[l16], %[rs], %[gk] offen sc1 lds\n\t" \
  "s_add_u32 m0, m0, 4096\n\t" \
  "s_add_u32 %[gk], %[gk], 4096\n\t" \
  "buffer_load_dwordx4 %[l16], %[rs], %[gk] offen sc1 lds\n\t" \
  "s_add_u32 m0, m0, 4096\n\t" \
  "s_add_u32 %[gk], %[gk], 4096\n\t" \
  "buffer_load_dwordx4 %[l16], %[rs], %[gk] offen sc1 lds\n\t" \
  "s_add_u32 m0, m0, 4096\n\t" \
  "s_add_u32 %[gk], %[gk], 4096\n\t" \
  "buffer_load_dwordx4 %[l16], %[rs], %[gk] offen sc1 lds\n\t" \
  "s_add_u32 m0, m0, 4096\n\t" \
  "s_add_u32 %[gk], %[gk], 4096\n\t" \
  "buffer_load_dwordx4 %[l16], %[rs], %[gk] offen sc1 lds\n\t" \
  "s_add_u32 m0, m0, 4096\n\t" \
  "s_add_u32 %[gk], %[gk], 4096\n\t" \
  "buffer_load_dwordx4 %[l16], %[rs], %[gk] offen sc1 lds\n\t" \
  "s_add_u32 m0, m0, 4096\n\t" \
  "s_add_u32 %[gk], %[gk], 4096\n\t" \
  "buffer_load_dwordx4 %[l16], %[rs], %[gk] offen sc1 lds\n\t" \
  "s_add_u32 m0, m0, 4096\n\t" \
  "s_add_u32 %[gk], %[gk], 4096\n\t" \
  "buffer_load_dwordx4 %[l16], %[rs], %[gk] offen sc1 lds\n\t" \
  "s_add_u32 m0, m0, 4096\n\t" \
  "s_add_u32 %[gk], %[gk], 4096\n\t" \
  "buffer_load_dwordx4 %[l16], %[rs], %[gk] offen sc1 lds\n\t" \
  "s_mov_b32 m0, %[mv]\n\t" \
  "s_nop 0\n\t" \
  "buffer_load_dwordx4 %[l16], %[rs], %[gv] offen sc1 lds\n\t" \
  "s_add_u32 m0, m0, 4224\n\t" \
  "s_add_u32 %[gv], %[gv], 4096\n\t" \
  "buffer_load_dwordx4 %[l16], %[rs], %[gv] offen sc1 lds\n\t" \
  "s_add_u32 m0, m0, 4224\n\t" \
  "s_add_u32 %[gv], %[gv], 4096\n\t" \
  "buffer_load_dwordx4 %[l16], %[rs], %[gv] offen sc1 lds\n\t" \
  "s_add_u32 m0, m0, 4224\n\t" \
  "s_add_u32 %[gv], %[gv], 4096\n\t" \
  "buffer_load_dwordx4 %[l16], %[rs], %[gv] offen sc1 lds\n\t" \
  "s_add_u32 m0, m0, 4224\n\t" \
  "s_add_u32 %[gv], %[gv], 4096\n\t" \
  "buffer_load_dwordx4 %[l16], %[rs], %[gv] offen sc1 lds\n\t" \
  "s_add_u32 m0, m0, 4224\n\t" \
  "s_add_u32 %[gv], %[gv], 4096\n\t" \
  "buffer_load_dwordx4 %[l16], %[rs], %[gv] offen sc1 lds\n\t" \
  "s_add_u32 m0, m0, 4224\n\t" \
  "s_add_u32 %[gv], %[gv], 4096\n\t" \
  "buffer_load_dwordx4 %[l16], %[rs], %[gv] offen sc1 lds\n\t" \
  "s_add_u32 m0, m0, 4224\n\t" \
  "s_add_u32 %[gv], %[gv], 4096\n\t" \
  "buffer_load_dwordx4 %[l16], %[rs], %[gv] offen sc1 lds\n\t" \
  "s_add_u32 m0, m0, 4224\n\t" \
  "s_add_u32 %[gv], %[gv], 4096\n\t" \
  "buffer_load_dwordx4 %[l16], %[rs], %[gv] offen sc1 lds\n\t" \
  "s_add_u32 m0, m0, 4224\n\t" \
  "s_add_u32 %[gv], %[gv], 4096\n\t" \
  "buffer_load_dwordx4 %[l16], %[rs], %[gv] offen sc1 lds\n\t" \
  "s_add_u32 m0, m0, 4224\n\t" \
  "s_add_u32 %[gv], %[gv], 4096\n\t" \
  "buffer_load_dwordx4 %[l16], %[rs], %[gv] offen sc1 lds\n\t" \
  "buffer_load_dwordx4 a[128:131], %[l16], %[rs], %[gwoa] offen sc1\n\t" \
  "buffer_load_dwordx4 a[132:135], %[l16], %[rs], %[gwob] offen sc1\n\t" \
  "buffer_load_dwordx4 a[136:139], %[l16], %[rs], %[gwoa] offen offset:1024 sc1\n\t" \
  "buffer_load_dwordx4 a[140:143], %[l16], %[rs], %[gwob] offen offset:1024 sc1\n\t" \
  "buffer_load_dwordx4 a[144:147], %[l16], %[rs], %[gwoa] offen offset:2048 sc1\n\t" \
  "buffer_load_dwordx4 a[148:151], %[l16], %[rs], %[gwob] offen offset:2048 sc1\n\t" \
  "buffer_load_dwordx4 a[152:155], %[l16], %[rs], %[gwoa] offen offset:3072 sc1\n\t" \
  "buffer_load_dwordx4 a[156:159], %[l16], %[rs], %[gwob] offen offset:3072 sc1\n\t" \
  "s_add_u32 %[gwoa], %[gwoa], 4096\n\t" \
  "buffer_load_dwordx4 a[160:163], %[l16], %[rs], %[gwoa] offen sc1\n\t" \
  "s_add_u32 %[gwob], %[gwob], 4096\n\t" \
  "buffer_load_dwordx4 a[164:167], %[l16], %[rs], %[gwob] offen sc1\n\t" \
  "buffer_load_dwordx4 a[168:171], %[l16], %[rs], %[gwoa] offen offset:1024 sc1\n\t" \
  "buffer_load_dwordx4 a[172:175], %[l16], %[rs], %[gwob] offen offset:1024 sc1\n\t" \
  "buffer_load_dwordx4 a[176:179], %[l16], %[rs], %[gwoa] offen offset:2048 sc1\n\t" \
  "buffer_load_dwordx4 a[180:183], %[l16], %[rs], %[gwob] offen offset:2048 sc1\n\t" \
  "buffer_load_dwordx4 a[184:187], %[l16], %[rs], %[gwoa] offen offset:3072 sc1\n\t" \
  "buffer_load_dwordx4 a[188:191], %[l16], %[rs], %[gwob] offen offset:3072 sc1\n\t" \
  "buffer_load_dwordx4 a[0:3], %[l16], %[rs], %[gw1] offen sc1\n\t" \
  "buffer_load_dwordx4 a[4:7], %[l16], %[rs], %[gw1] offen offset:1024 sc1\n\t" \
  "buffer_load_dwordx4 a[8:11], %[l16], %[rs], %[gw1] offen offset:2048 sc1\n\t" \
  "buffer_load_dwordx4 a[12:15], %[l16], %[rs], %[gw1] offen offset:3072 sc1\n\t" \
  "s_add_u32 %[gw1], %[gw1], 4096\n\t" \
  "buffer_load_dwordx4 a[16:19], %[l16], %[rs], %[gw1] offen sc1\n\t" \
  "buffer_load_dwordx4 a[20:23], %[l16], %[rs], %[gw1] offen offset:1024 sc1\n\t" \
  "buffer_load_dwordx4 a[24:27], %[l16], %[rs], %[gw1] offen offset:2048 sc1\n\t" \
  "buffer_load_dwordx4 a[28:31], %[l16], %[rs], %[gw1] offen offset:3072 sc1\n\t" \
  "s_add_u32 %[gw1], %[gw1], 4096\n\t" \
  "buffer_load_dwordx4 a[32:35], %[l16], %[rs], %[gw1] offen sc1\n\t" \
  "buffer_load_dwordx4 a[36:39], %[l16], %[rs], %[gw1] offen offset:1024 sc1\n\t" \
  "buffer_load_dwordx4 a[40:43], %[l16], %[rs], %[gw1] offen offset:2048 sc1\n\t" \
  "buffer_load_dwordx4 a[44:47], %[l16], %[rs], %[gw1] offen offset:3072 sc1\n\t" \
  "s_add_u32 %[gw1], %[gw1], 4096\n\t" \
  "buffer_load_dwordx4 a[48:51], %[l16], %[rs], %[gw1] offen sc1\n\t" \
  "buffer_load_dwordx4 a[52:55], %[l16], %[rs], %[gw1] offen offset:1024 sc1\n\t" \
  "buffer_load_dwordx4 a[56:59], %[l16], %[rs], %[gw1] offen offset:2048 sc1\n\t" \
  "buffer_load_dwordx4 a[60:63], %[l16], %[rs], %[gw1] offen offset:3072 sc1\n\t" \
  "s_add_u32 %[gw1], %[gw1], 4096\n\t" \
  "buffer_load_dwordx4 a[64:67], %[l16], %[rs], %[gw1] offen sc1\n\t" \
  "buffer_load_dwordx4 a[68:71], %[l16], %[rs], %[gw1] offen offset:1024 sc1\n\t" \
  "buffer_load_dwordx4 a[72:75], %[l16], %[rs], %[gw1] offen offset:2048 sc1\n\t" \
  "buffer_load_dwordx4 a[76:79], %[l16], %[rs], %[gw1] offen offset:3072 sc1\n\t" \
  "s_add_u32 %[gw1], %[gw1], 4096\n\t" \
  "buffer_load_dwordx4 a[80:83], %[l16], %[rs], %[gw1] offen sc1\n\t" \
  "buffer_load_dwordx4 a[84:87], %[l16], %[rs], %[gw1] offen offset:1024 sc1\n\t" \
  "buffer_load_dwordx4 a[88:91], %[l16], %[rs], %[gw1] offen offset:2048 sc1\n\t" \
  "buffer_load_dwordx4 a[92:95], %[l16], %[rs], %[gw1] offen offset:3072 sc1\n\t" \
  "s_add_u32 %[gw1], %[gw1], 4096\n\t" \
  "buffer_load_dwordx4 a[96:99], %[l16], %[rs], %[gw1] offen sc1\n\t" \
  "buffer_load_dwordx4 a[100:103], %[l16], %[rs], %[gw1] offen offset:1024 sc1\n\t" \
  "buffer_load_dwordx4 a[104:107], %[l16], %[rs], %[gw1] offen offset:2048 sc1\n\t" \
  "buffer_load_dwordx4 a[108:111], %[l16], %[rs], %[gw1] offen offset:3072 sc1\n\t" \
  "s_add_u32 %[gw1], %[gw1], 4096\n\t" \
  "buffer_load_dwordx4 a[112:115], %[l16], %[rs], %[gw1] offen sc1\n\t" \
  "buffer_load_dwordx4 a[116:119], %[l16], %[rs], %[gw1] offen offset:1024 sc1\n\t" \
  "buffer_load_dwordx4 a[120:123], %[l16], %[rs], %[gw1] offen offset:2048 sc1\n\t" \
  "buffer_load_dwordx4 a[124:127], %[l16], %[rs], %[gw1] offen offset:3072 sc1\n\t" \
  "buffer_load_dwordx4 v[120:123], %[l16], %[rs], %[wa] offen sc1\n\t" \
  "buffer_load_dwordx4 v[124:127], %[l16], %[rs], %[wb] offen sc1\n\t" \
  "buffer_load_dwordx4 v[128:131], %[l16], %[rs], %[wa] offen offset:1024 sc1\n\t" \
  "buffer_load_dwordx4 v[132:135], %[l16], %[rs], %[wb] offen offset:1024 sc1\n\t" \
  "buffer_load_dwordx4 v[136:139], %[l16], %[rs], %[wa] offen offset:2048 sc1\n\t" \
  "buffer_load_dwordx4 v[140:143], %[l16], %[rs], %[wb] offen offset:2048 sc1\n\t" \
  "buffer_load_dwordx4 v[144:147], %[l16], %[rs], %[wa] offen offset:3072 sc1\n\t" \
  "buffer_load_dwordx4 v[148:151], %[l16], %[rs], %[wb] offen offset:3072 sc1\n\t" \
  "s_add_u32 %[wa], %[wa], 4096\n\t" \
  "buffer_load_dwordx4 v[152:155], %[l16], %[rs], %[wa] offen sc1\n\t" \
  "s_add_u32 %[wb], %[wb], 4096\n\t" \
  "buffer_load_dwordx4 v[156:159], %[l16], %[rs], %[wb] offen sc1\n\t" \
  "buffer_load_dwordx4 v[160:163], %[l16], %[rs], %[wa] offen offset:1024 sc1\n\t" \
  "buffer_load_dwordx4 v[164:167], %[l16], %[rs], %[wb] offen offset:1024 sc1\n\t" \
  "buffer_load_dwordx4 v[168:171], %[l16], %[rs], %[wa] offen offset:2048 sc1\n\t" \
  "buffer_load_dwordx4 v[172:175], %[l16], %[rs], %[wb] offen offset:2048 sc1\n\t" \
  "buffer_load_dwordx4 v[176:179], %[l16], %[rs], %[wa] offen offset:3072 sc1\n\t" \
  "buffer_load_dwordx4 v[180:183], %[l16], %[rs], %[wb] offen offset:3072 sc1\n\t" \
  "s_add_u32 %[wa], %[wa], 4096\n\t" \
  "buffer_load_dwordx4 v[184:187], %[l16], %[rs], %[wa] offen sc1\n\t" \
  "s_add_u32 %[wb], %[wb], 4096\n\t" \
  "buffer_load_dwordx4 v[188:191], %[l16], %[rs], %[wb] offen sc1\n\t" \
  "buffer_load_dwordx4 a[192:195], %[l16], %[rs], %[wa] offen offset:1024 sc1\n\t" \
  "buffer_load_dwordx4 a[196:199], %[l16], %[rs], %[wb] offen offset:1024 sc1\n\t" \
  "buffer_load_dwordx4 a[200:203], %[l16], %[rs], %[wa] offen offset:2048 sc1\n\t" \
  "buffer_load_dwordx4 a[204:207], %[l16], %[rs], %[wb] offen offset:2048 sc1\n\t" \
  "buffer_load_dwordx4 a[208:211], %[l16], %[rs], %[wa] offen offset:3072 sc1\n\t" \
  "buffer_load_dwordx4 a[212:215], %[l16], %[rs], %[wb] offen offset:3072 sc1\n\t" \
  "s_add_u32 %[wa], %[wa], 4096\n\t" \
  "buffer_load_dwordx4 a[216:219], %[l16], %[rs], %[wa] offen sc1\n\t" \
  "s_add_u32 %[wb], %[wb], 4096\n\t" \
  "buffer_load_dwordx4 a[220:223], %[l16], %[rs], %[wb] offen sc1\n\t" \
  "buffer_load_dwordx4 a[224:227], %[l16], %[rs], %[wa] offen offset:1024 sc1\n\t" \
  "buffer_load_dwordx4 a[228:231], %[l16], %[rs], %[wb] offen offset:1024 sc1\n\t" \
  "buffer_load_dwordx4 a[232:235], %[l16], %[rs], %[wa] offen offset:2048 sc1\n\t" \
  "buffer_load_dwordx4 a[236:239], %[l16], %[rs], %[wb] offen offset:2048 sc1\n\t" \
  "buffer_load_dwordx4 a[240:243], %[l16], %[rs], %[wa] offen offset:3072 sc1\n\t" \
  "buffer_load_dwordx4 a[244:247], %[l16], %[rs], %[wb] offen offset:3072 sc1\n\t" \
  "s_add_u32 %[wa], %[wa], 4096\n\t" \
  "buffer_load_dwordx4 a[248:251], %[l16], %[rs], %[wa] offen sc1\n\t" \
  "s_add_u32 %[wb], %[wb], 4096\n\t" \
  "buffer_load_dwordx4 a[252:255], %[l16], %[rs], %[wb] offen sc1\n\t"
#define ATT_EPI_ASM \
  "v_rcp_f32 v196, v88\n\t" \
  "s_nop 0\n\t" \
  "v_mul_f32 v80, v196, v80\n\t" \
  "v_mul_f32 v81, v196, v81\n\t" \
  "v_mul_f32 v82, v196, v82\n\t" \
  "v_mul_f32 v83, v196, v83\n\t" \
  "v_mul_f32 v84, v196, v84\n\t" \
  "v_mul_f32 v85, v196, v85\n\t" \
  "v_mul_f32 v86, v196, v86\n\t" \
  "v_mul_f32 v87, v196, v87\n\t" \
  "v_cvt_pk_bf16_f32 v192, v80, v81\n\t" \
  "v_cvt_pk_bf16_f32 v193, v82, v83\n\t" \
  "v_cvt_pk_bf16_f32 v194, v84, v85\n\t" \
  "v_cvt_pk_bf16_f32 v195, v86, v87\n\t" \
  "buffer_store_dwordx4 v[192:195], %[l16], %[rs], %[so0] offen sc1\n\t" \
  "v_rcp_f32 v204, v104\n\t" \
  "s_nop 0\n\t" \
  "v_mul_f32 v96, v204, v96\n\t" \
  "v_mul_f32 v97, v204, v97\n\t" \
  "v_mul_f32 v98, v204, v98\n\t" \
  "v_mul_f32 v99, v204, v99\n\t" \
  "v_mul_f32 v100, v204, v100\n\t" \
  "v_mul_f32 v101, v204, v101\n\t" \
  "v_mul_f32 v102, v204, v102\n\t" \
  "v_mul_f32 v103, v204, v103\n\t" \
  "v_cvt_pk_bf16_f32 v200, v96, v97\n\t" \
  "v_cvt_pk_bf16_f32 v201, v98, v99\n\t" \
  "v_cvt_pk_bf16_f32 v202, v100, v101\n\t" \
  "v_cvt_pk_bf16_f32 v203, v102, v103\n\t" \
  "buffer_store_dwordx4 v[200:203], %[l16], %[rs], %[so1] offen sc1\n\t"
#define Q0_ASM \
  "s_nop 4\n\t" \
  "s_add_u32 %[sq1], %[sq], 65536\n\t" \
  "s_add_u32 %[sk1], %[sk], 65536\n\t" \
  "buffer_load_dwordx2 v[120:121], %[sv], %[rs], %[sst] offen sc1\n\t" \
  "buffer_load_dwordx2 v[122:123], %[sv], %[rs], %[sst] offen offset:256 sc1\n\t" \
  "buffer_load_dwordx2 v[124:125], %[sv], %[rs], %[sst] offen offset:512 sc1\n\t" \
  "buffer_load_dwordx2 v[126:127], %[sv], %[rs], %[sst] offen offset:768 sc1\n\t" \
  "buffer_load_dwordx2 v[128:129], %[sv], %[rs], %[sst] offen offset:1024 sc1\n\t" \
  "buffer_load_dwordx2 v[130:131], %[sv], %[rs], %[sst] offen offset:1280 sc1\n\t" \
  "buffer_load_dwordx2 v[132:133], %[sv], %[rs], %[sst] offen offset:1536 sc1\n\t" \
  "buffer_load_dwordx2 v[134:135], %[sv], %[rs], %[sst] offen offset:1792 sc1\n\t" \
  "buffer_load_dword v240, %[l4], %[rs], %[scq] offen offset:0 sc1\n\t" \
  "buffer_load_dword v241, %[l4], %[rs], %[scq] offen offset:128 sc1\n\t" \
  "buffer_load_dword v242, %[l4], %[rs], %[scq] offen offset:256 sc1\n\t" \
  "buffer_load_dword v243, %[l4], %[rs], %[scq] offen offset:384 sc1\n\t" \
  "buffer_load_dword v244, %[l4], %[rs], %[scq] offen offset:512 sc1\n\t" \
  "buffer_load_dword v245, %[l4], %[rs], %[scq] offen offset:640 sc1\n\t" \
  "s_mov_b32 %[t0], %[sact]\n\t" \
  "buffer_load_dwordx4 a[0:3], %[l16], %[rs], %[t0] offen sc1\n\t" \
  "buffer_load_dwordx4 a[4:7], %[l16], %[rs], %[t0] offen offset:1024 sc1\n\t" \
  "buffer_load_dwordx4 a[8:11], %[l16], %[rs], %[t0] offen offset:2048 sc1\n\t" \
  "buffer_load_dwordx4 a[12:15], %[l16], %[rs], %[t0] offen offset:3072 sc1\n\t" \
  "s_add_u32 %[t0], %[t0], 4096\n\t" \
  "buffer_load_dwordx4 a[16:19], %[l16], %[rs], %[t0] offen sc1\n\t" \
  "buffer_load_dwordx4 a[20:23], %[l16], %[rs], %[t0] offen offset:1024 sc1\n\t" \
  "buffer_load_dwordx4 a[24:27], %[l16], %[rs], %[t0] offen offset:2048 sc1\n\t" \
  "buffer_load_dwordx4 a[28:31], %[l16], %[rs], %[t0] offen offset:3072 sc1\n\t" \
  "s_add_u32 %[t0], %[t0], 4096\n\t" \
  "buffer_load_dwordx4 a[32:35], %[l16], %[rs], %[t0] offen sc1\n\t" \
  "buffer_load_dwordx4 a[36:39], %[l16], %[rs], %[t0] offen offset:1024 sc1\n\t" \
  "s_mov_b32 m0, %[lA]\n\t" \
  "s_mov_b32 %[t1], %[gA]\n\t" \
  "buffer_load_dwordx4 %[l16], %[rs], %[t1] offen sc1 lds\n\t" \
  "s_add_u32 m0, m0, 2048\n\t" \
  "s_add_u32 %[t1], %[t1], 2048\n\t" \
  "buffer_load_dwordx4 %[l16], %[rs], %[t1] offen sc1 lds\n\t" \
  "s_add_u32 m0, m0, 2048\n\t" \
  "s_add_u32 %[t1], %[t1], 2048\n\t" \
  "buffer_load_dwordx4 %[l16], %[rs], %[t1] offen sc1 lds\n\t" \
  "s_add_u32 m0, m0, 2048\n\t" \
  "s_add_u32 %[t1], %[t1], 2048\n\t" \
  "buffer_load_dwordx4 %[l16], %[rs], %[t1] offen sc1 lds\n\t" \
  "s_add_u32 m0, m0, 2048\n\t" \
  "s_add_u32 %[t1], %[t1], 2048\n\t" \
  "buffer_load_dwordx4 %[l16], %[rs], %[t1] offen sc1 lds\n\t" \
  "s_add_u32 m0, m0, 2048\n\t" \
  "s_add_u32 %[t1], %[t1], 2048\n\t" \
  "buffer_load_dwordx4 %[l16], %[rs], %[t1] offen sc1 lds\n\t" \
  "s_add_u32 m0, m0, 2048\n\t" \
  "s_add_u32 %[t1], %[t1], 2048\n\t" \
  "buffer_load_dwordx4 %[l16], %[rs], %[t1] offen sc1 lds\n\t" \
  "s_add_u32 m0, m0, 2048\n\t" \
  "s_add_u32 %[t1], %[t1], 2048\n\t" \
  "buffer_load_dwordx4 %[l16], %[rs], %[t1] offen sc1 lds\n\t" \
  "s_waitcnt vmcnt(0)\n\t" \
  "s_barrier\n\t" \
  "ds_read_b128 v[208:211], %[lw0]\n\t" \
  "ds_read_b128 v[212:215], %[lw0] offset:32768\n\t" \
  "ds_read_b128 v[216:219], %[lw0] offset:1024\n\t" \
  "ds_read_b128 v[220:223], %[lw0] offset:33792\n\t" \
  "ds_read_b128 v[224:227], %[lw0] offset:2048\n\t" \
  "ds_read_b128 v[228:231], %[lw0] offset:34816\n\t" \
  "s_waitcnt lgkmcnt(5)\n\t" \
  "v_mfma_f32_32x32x16_bf16 v[80:95], v[208:211], a[0:3], 0\n\t" \
  "s_add_u32 m0, m0, 2048\n\t" \
  "s_add_u32 %[t1], %[t1], 2048\n\t" \
  "buffer_load_dwordx4 %[l16], %[rs], %[t1] offen sc1 lds\n\t" \
  "ds_read_b128 v[232:235], %[lw0] offset:3072\n\t" \
  "buffer_load_dwordx4 a[40:43], %[l16], %[rs], %[t0] offen offset:2048 sc1\n\t" \
  "s_waitcnt lgkmcnt(5)\n\t" \
  "v_mfma_f32_32x32x16_bf16 v[96:111], v[212:215], a[0:3], 0\n\t" \
  "s_add_u32 m0, m0, 2048\n\t" \
  "s_add_u32 %[t1], %[t1], 2048\n\t" \
  "buffer_load_dwordx4 %[l16], %[rs], %[t1] offen sc1 lds\n\t" \
  "ds_read_b128 v[236:239], %[lw0] offset:35840\n\t" \
  "s_waitcnt lgkmcnt(5)\n\t" \
  "v_mfma_f32_32x32x16_bf16 v[80:95], v[216:219], a[4:7], v[80:95]\n\t" \
  "s_add_u32 m0, m0, 2048\n\t" \
  "s_add_u32 %[t1], %[t1], 2048\n\t" \
  "buffer_load_dwordx4 %[l16], %[rs], %[t1] offen sc1 lds\n\t" \
  "ds_read_b128 v[208:211], %[lw0] offset:4096\n\t" \
  "buffer_load_dwordx4 a[44:47], %[l16], %[rs], %[t0] offen offset:3072 sc1\n\t" \
  "s_waitcnt lgkmcnt(5)\n\t" \
  "v_mfma_f32_32x32x16_bf16 v[96:111], v[220:223], a[4:7], v[96:111]\n\t" \
  "s_add_u32 m0, m0, 2048\n\t" \
  "s_add_u32 %[t1], %[t1], 2048\n\t" \
  "buffer_load_dwordx4 %[l16], %[rs], %[t1] offen sc1 lds\n\t" \
  "ds_read_b128 v[212:215], %[lw0] offset:36864\n\t" \
  "s_waitcnt lgkmcnt(5)\n\t" \
  "v_mfma_f32_32x32x16_bf16 v[80:95], v[224:227], a[8:11], v[80:95]\n\t" \
  "s_add_u32 m0, m0, 2048\n\t" \
  "s_add_u32 %[t1], %[t1], 2048\n\t" \
  "buffer_load_dwordx4 %[l16], %[rs], %[t1] offen sc1 lds\n\t" \
  "ds_read_b128 v[216:219], %[lw0] offset:5120\n\t" \
  "s_add_u32 %[t0], %[t0], 4096\n\t" \
  "buffer_load_dwordx4 a[48:51], %[l16], %[rs], %[t0] offen sc1\n\t" \
  "v_add_f32 v118, v120, v122\n\t" \
  "s_waitcnt lgkmcnt(5)\n\t" \
  "v_mfma_f32_32x32x16_bf16 v[96:111], v[228:231], a[8:11], v[96:111]\n\t" \
  "s_add_u32 m0, m0, 2048\n\t" \
  "s_add_u32 %[t1], %[t1], 2048\n\t" \
  "buffer_load_dwordx4 %[l16], %[rs], %[t1] offen sc1 lds\n\t" \
  "ds_read_b128 v[220:223], %[lw0] offset:37888\n\t" \
  "v_add_f32 v118, v118, v124\n\t" \
  "v_add_f32 v118, v118, v126\n\t" \
  "s_waitcnt lgkmcnt(5)\n\t" \
  "v_mfma_f32_32x32x16_bf16 v[80:95], v[232:235], a[12:15], v[80:95]\n\t" \
  "s_add_u32 m0, m0, 2048\n\t" \
  "s_add_u32 %[t1], %[t1], 2048\n\t" \
  "buffer_load_dwordx4 %[l16], %[rs], %[t1] offen sc1 lds\n\t" \
  "ds_read_b128 v[224:227], %[lw0] offset:6144\n\t" \
  "buffer_load_dwordx4 a[52:55], %[l16], %[rs], %[t0] offen offset:1024 sc1\n\t" \
  "v_add_f32 v118, v118, v128\n\t" \
  "v_add_f32 v118, v118, v130\n\t" \
  "s_waitcnt lgkmcnt(5)\n\t" \
  "v_mfma_f32_32x32x16_bf16 v[96:111], v[236:239], a[12:15], v[96:111]\n\t" \
  "s_add_u32 m0, m0, 2048\n\t" \
  "s_add_u32 %[t1], %[t1], 2048\n\t" \
  "buffer_load_dwordx4 %[l16], %[rs], %[t1] offen sc1 lds\n\t" \
  "ds_read_b128 v[228:231], %[lw0] offset:38912\n\t" \
  "v_add_f32 v118, v118, v132\n\t" \
  "v_add_f32 v118, v118, v134\n\t" \
  "s_waitcnt lgkmcnt(5)\n\t" \
  "v_mfma_f32_32x32x16_bf16 v[80:95], v[208:211], a[16:19], v[80:95]\n\t" \
  "s_mov_b32 m0, %[lB]\n\t" \
  "s_mov_b32 %[t1], %[gB]\n\t" \
  "buffer_load_dwordx4 %[l16], %[rs], %[t1] offen sc1 lds\n\t" \
  "ds_read_b128 v[232:235], %[lw0] offset:7168\n\t" \
  "buffer_load_dwordx4 a[56:59], %[l16], %[rs], %[t0] offen offset:2048 sc1\n\t" \
  "v_mov_b32 v116, v118\n\t" \
  "s_nop 1\n\t" \
  "v_permlane32_swap_b32 v118, v116\n\t" \
  "s_waitcnt lgkmcnt(5)\n\t" \
  "v_mfma_f32_32x32x16_bf16 v[96:111], v[212:215], a[16:19], v[96:111]\n\t" \
  "s_add_u32 m0, m0, 4096\n\t" \
  "s_add_u32 %[t1], %[t1], 4096\n\t" \
  "buffer_load_dwordx4 %[l16], %[rs], %[t1] offen sc1 lds\n\t" \
  "ds_read_b128 v[236:239], %[lw0] offset:39936\n\t" \
  "v_add_f32 v118, v118, v116\n\t" \
  "v_mul_f32 v118, 0x3d800000, v118\n\t" \
  "s_waitcnt lgkmcnt(5)\n\t" \
  "v_mfma_f32_32x32x16_bf16 v[80:95], v[216:219], a[20:23], v[80:95]\n\t" \
  "s_add_u32 m0, m0, 4096\n\t" \
  "s_add_u32 %[t1], %[t1], 4096\n\t" \
  "buffer_load_dwordx4 %[l16], %[rs], %[t1] offen sc1 lds\n\t" \
  "ds_read_b128 v[208:211], %[lw0] offset:8192\n\t" \
  "buffer_load_dwordx4 a[60:63], %[l16], %[rs], %[t0] offen offset:3072 sc1\n\t" \
  "v_add_f32 v240, v240, v241\n\t" \
  "v_mul_f32 v240, v240, -%[hm]\n\t" \
  "s_waitcnt lgkmcnt(5)\n\t" \
  "v_mfma_f32_32x32x16_bf16 v[96:111], v[220:223], a[20:23], v[96:111]\n\t" \
  "s_add_u32 m0, m0, 4096\n\t" \
  "s_add_u32 %[t1], %[t1], 4096\n\t" \
  "buffer_load_dwordx4 %[l16], %[rs], %[t1] offen sc1 lds\n\t" \
  "ds_read_b128 v[212:215], %[lw0] offset:40960\n\t" \
  "v_cvt_pk_bf16_f32 v176, v240, v240\n\t" \
  "v_mov_b32 v178, 0\n\t" \
  "s_waitcnt lgkmcnt(5)\n\t" \
  "v_mfma_f32_32x32x16_bf16 v[80:95], v[224:227], a[24:27], v[80:95]\n\t" \
  "s_add_u32 m0, m0, 4096\n\t" \
  "s_add_u32 %[t1], %[t1], 4096\n\t" \
  "buffer_load_dwordx4 %[l16], %[rs], %[t1] offen sc1 lds\n\t" \
  "ds_read_b128 v[216:219], %[lw0] offset:9216\n\t" \
  "s_add_u32 %[t0], %[t0], 4096\n\t" \
  "buffer_load_dwordx4 a[64:67], %[l16], %[rs], %[t0] offen sc1\n\t" \
  "v_lshlrev_b32 v177, 16, v176\n\t" \
  "v_mov_b32 v179, 0\n\t" \
  "s_waitcnt lgkmcnt(5)\n\t" \
  "v_mfma_f32_32x32x16_bf16 v[96:111], v[228:231], a[24:27], v[96:111]\n\t" \
  "s_add_u32 m0, m0, 4096\n\t" \
  "s_add_u32 %[t1], %[t1], 4096\n\t" \
  "buffer_load_dwordx4 %[l16], %[rs], %[t1] offen sc1 lds\n\t" \
  "ds_read_b128 v[220:223], %[lw0] offset:41984\n\t" \
  "v_sub_f32 v177, v240, v177\n\t" \
  "s_nop 0\n\t" \
  "s_waitcnt lgkmcnt(5)\n\t" \
  "v_mfma_f32_32x32x16_bf16 v[80:95], v[232:235], a[28:31], v[80:95]\n\t" \
  "s_add_u32 m0, m0, 4096\n\t" \
  "s_add_u32 %[t1], %[t1], 4096\n\t" \
  "buffer_load_dwordx4 %[l16], %[rs], %[t1] offen sc1 lds\n\t" \
  "ds_read_b128 v[224:227], %[lw0] offset:10240\n\t" \
  "buffer_load_dwordx4 a[68:71], %[l16], %[rs], %[t0] offen offset:1024 sc1\n\t" \
  "v_cvt_pk_bf16_f32 v177, v177, v178\n\t" \
  "v_add_f32 v242, v242, v243\n\t" \
  "s_waitcnt lgkmcnt(5)\n\t" \
  "v_mfma_f32_32x32x16_bf16 v[96:111], v[236:239], a[28:31], v[96:111]\n\t" \
  "s_add_u32 m0, m0, 4096\n\t" \
  "s_add_u32 %[t1], %[t1], 4096\n\t" \
  "buffer_load_dwordx4 %[l16], %[rs], %[t1] offen sc1 lds\n\t" \
  "ds_read_b128 v[228:231], %[lw0] offset:43008\n\t" \
  "v_mul_f32 v242, v242, -%[hm]\n\t" \
  "v_cvt_pk_bf16_f32 v180, v242, v242\n\t" \
  "s_waitcnt lgkmcnt(5)\n\t" \
  "v_mfma_f32_32x32x16_bf16 v[80:95], v[208:211], a[32:35], v[80:95]\n\t" \
  "ds_read_b128 v[232:235], %[lw0] offset:11264\n\t" \
  "buffer_load_dwordx4 a[72:75], %[l16], %[rs], %[t0] offen offset:2048 sc1\n\t" \
  "v_mov_b32 v182, 0\n\t" \
  "v_lshlrev_b32 v181, 16, v180\n\t" \
  "s_waitcnt lgkmcnt(5)\n\t" \
  "v_mfma_f32_32x32x16_bf16 v[96:111], v[212:215], a[32:35], v[96:111]\n\t" \
  "ds_read_b128 v[236:239], %[lw0] offset:44032\n\t" \
  "v_mov_b32 v183, 0\n\t" \
  "v_sub_f32 v181, v242, v181\n\t" \
  "s_waitcnt lgkmcnt(5)\n\t" \
  "v_mfma_f32_32x32x16_bf16 v[80:95], v[216:219], a[36:39], v[80:95]\n\t" \
  "ds_read_b128 v[208:211], %[lw0] offset:12288\n\t" \
  "buffer_load_dwordx4 a[76:79], %[l16], %[rs], %[t0] offen offset:3072 sc1\n\t" \
  "s_nop 0\n\t" \
  "v_cvt_pk_bf16_f32 v181, v181, v182\n\t" \
  "s_waitcnt lgkmcnt(5)\n\t" \
  "v_mfma_f32_32x32x16_bf16 v[96:111], v[220:223], a[36:39], v[96:111]\n\t" \
  "ds_read_b128 v[212:215], %[lw0] offset:45056\n\t" \
  "v_add_f32 v244, v244, v245\n\t" \
  "v_mul_f32 v244, v244, -%[hm]\n\t" \
  "s_waitcnt vmcnt(24) lgkmcnt(5)\n\t" \
  "v_mfma_f32_32x32x16_bf16 v[80:95], v[224:227], a[40:43], v[80:95]\n\t" \
  "ds_read_b128 v[216:219], %[lw0] offset:13312\n\t" \
  "s_add_u32 %[t0], %[t0], 4096\n\t" \
  "buffer_load_dwordx4 a[80:83], %[l16], %[rs], %[t0] offen sc1\n\t" \
  "v_cvt_pk_bf16_f32 v184, v244, v244\n\t" \
  "v_mov_b32 v186, 0\n\t" \
  "s_waitcnt lgkmcnt(5)\n\t" \
  "v_mfma_f32_32x32x16_bf16 v[96:111], v[228:231], a[40:43], v[96:111]\n\t" \
  "ds_read_b128 v[220:223], %[lw0] offset:46080\n\t" \
  "v_lshlrev_b32 v185, 16, v184\n\t" \
  "v_mov_b32 v187, 0\n\t" \
  "s_waitcnt vmcnt(22) lgkmcnt(5)\n\t" \
  "v_mfma_f32_32x32x16_bf16 v[80:95], v[232:235], a[44:47], v[80:95]\n\t" \
  "ds_read_b128 v[224:227], %[lw0] offset:14336\n\t" \
  "buffer_load_dwordx4 a[84:87], %[l16], %[rs], %[t0] offen offset:1024 sc1\n\t" \
  "v_sub_f32 v185, v244, v185\n\t" \
  "s_nop 0\n\t" \
  "s_waitcnt lgkmcnt(5)\n\t" \
  "v_mfma_f32_32x32x16_bf16 v[96:111], v[236:239], a[44:47], v[96:111]\n\t" \
  "ds_read_b128 v[228:231], %[lw0] offset:47104\n\t" \
  "v_cvt_pk_bf16_f32 v185, v185, v186\n\t" \
  "v_mul_f32 v116, v118, %[hm]\n\t" \
  "s_waitcnt vmcnt(20) lgkmcnt(5)\n\t" \
  "v_mfma_f32_32x32x16_bf16 v[80:95], v[208:211], a[48:51], v[80:95]\n\t" \
  "ds_read_b128 v[232:235], %[lw0] offset:15360\n\t" \
  "buffer_load_dwordx4 a[88:91], %[l16], %[rs], %[t0] offen offset:2048 sc1\n\t" \
  "v_cvt_pk_bf16_f32 v117, v116, v116\n\t" \
  "v_mov_b32 v114, 0\n\t" \
  "s_waitcnt lgkmcnt(5)\n\t" \
  "v_mfma_f32_32x32x16_bf16 v[96:111], v[212:215], a[48:51], v[96:111]\n\t" \
  "ds_read_b128 v[236:239], %[lw0] offset:48128\n\t" \
  "v_lshlrev_b32 v117, 16, v117\n\t" \
  "v_mov_b32 v115, 0\n\t" \
  "s_waitcnt vmcnt(18) lgkmcnt(5)\n\t" \
  "v_mfma_f32_32x32x16_bf16 v[80:95], v[216:219], a[52:55], v[80:95]\n\t" \
  "s_waitcnt vmcnt(17)\n\t" \
  "s_barrier\n\t" \
  "ds_read_b128 v[208:211], %[lw0] offset:16384\n\t" \
  "buffer_load_dwordx4 a[92:95], %[l16], %[rs], %[t0] offen offset:3072 sc1\n\t" \
  "v_sub_f32 v117, v116, v117\n\t" \
  "v_cvt_pk_bf16_f32 v113, v116, v114\n\t" \
  "s_waitcnt lgkmcnt(5)\n\t" \
  "v_mfma_f32_32x32x16_bf16 v[96:111], v[220:223], a[52:55], v[96:111]\n\t" \
  "ds_read_b128 v[212:215], %[lw0] offset:49152\n\t" \
  "v_cvt_pk_bf16_f32 v112, v116, v117\n\t" \
  "v_add_f32 v138, v121, v123\n\t" \
  "s_waitcnt vmcnt(16) lgkmcnt(5)\n\t" \
  "v_mfma_f32_32x32x16_bf16 v[80:95], v[224:227], a[56:59], v[80:95]\n\t" \
  "ds_read_b128 v[216:219], %[lw0] offset:17408\n\t" \
  "s_add_u32 %[t0], %[t0], 4096\n\t" \
  "buffer_load_dwordx4 a[96:99], %[l16], %[rs], %[t0] offen sc1\n\t" \
  "v_add_f32 v138, v138, v125\n\t" \
  "v_add_f32 v138, v138, v127\n\t" \
  "s_waitcnt lgkmcnt(5)\n\t" \
  "v_mfma_f32_32x32x16_bf16 v[96:111], v[228:231], a[56:59], v[96:111]\n\t" \
  "ds_read_b128 v[220:223], %[lw0] offset:50176\n\t" \
  "v_add_f32 v138, v138, v129\n\t" \
  "v_add_f32 v138, v138, v131\n\t" \
  "s_waitcnt vmcnt(14) lgkmcnt(5)\n\t" \
  "v_mfma_f32_32x32x16_bf16 v[80:95], v[232:235], a[60:63], v[80:95]\n\t" \
  "ds_read_b128 v[224:227], %[lw0] offset:18432\n\t" \
  "buffer_load_dwordx4 a[100:103], %[l16], %[rs], %[t0] offen offset:1024 sc1\n\t" \
  "v_add_f32 v138, v138, v133\n\t" \
  "v_add_f32 v138, v138, v135\n\t" \
  "s_waitcnt lgkmcnt(5)\n\t" \
  "v_mfma_f32_32x32x16_bf16 v[96:111], v[236:239], a[60:63], v[96:111]\n\t" \
  "ds_read_b128 v[228:231], %[lw0] offset:51200\n\t" \
  "v_sub_f32 v116, v120, v118\n\t" \
  "v_fma_f32 v137, v116, v116, 0\n\t" \
  "s_waitcnt vmcnt(12) lgkmcnt(5)\n\t" \
  "v_mfma_f32_32x32x16_bf16 v[80:95], v[208:211], a[64:67], v[80:95]\n\t" \
  "ds_read_b128 v[232:235], %[lw0] offset:19456\n\t" \
  "buffer_load_dwordx4 a[104:107], %[l16], %[rs], %[t0] offen offset:2048 sc1\n\t" \
  "v_sub_f32 v116, v122, v118\n\t" \
  "v_fma_f32 v137, v116, v116, v137\n\t" \
  "s_waitcnt lgkmcnt(5)\n\t" \
  "v_mfma_f32_32x32x16_bf16 v[96:111], v[212:215], a[64:67], v[96:111]\n\t" \
  "ds_read_b128 v[236:239], %[lw0] offset:52224\n\t" \
  "v_sub_f32 v116, v124, v118\n\t" \
  "v_fma_f32 v137, v116, v116, v137\n\t" \
  "s_waitcnt vmcnt(10) lgkmcnt(5)\n\t" \
  "v_mfma_f32_32x32x16_bf16 v[80:95], v[216:219], a[68:71], v[80:95]\n\t" \
  "ds_read_b128 v[208:211], %[lw0] offset:20480\n\t" \
  "buffer_load_dwordx4 a[108:111], %[l16], %[rs], %[t0] offen offset:3072 sc1\n\t" \
  "v_sub_f32 v116, v126, v118\n\t" \
  "v_fma_f32 v137, v116, v116, v137\n\t" \
  "s_waitcnt lgkmcnt(5)\n\t" \
  "v_mfma_f32_32x32x16_bf16 v[96:111], v[220:223], a[68:71], v[96:111]\n\t" \
  "ds_read_b128 v[212:215], %[lw0] offset:53248\n\t" \
  "v_sub_f32 v116, v128, v118\n\t" \
  "v_fma_f32 v137, v116, v116, v137\n\t" \
  "s_waitcnt vmcnt(9) lgkmcnt(5)\n\t" \
  "v_mfma_f32_32x32x16_bf16 v[80:95], v[224:227], a[72:75], v[80:95]\n\t" \
  "ds_read_b128 v[216:219], %[lw0] offset:21504\n\t" \
  "s_add_u32 %[t0], %[t0], 4096\n\t" \
  "buffer_load_dwordx4 a[112:115], %[l16], %[rs], %[t0] offen sc1\n\t" \
  "v_sub_f32 v116, v130, v118\n\t" \
  "v_fma_f32 v137, v116, v116, v137\n\t" \
  "s_waitcnt lgkmcnt(5)\n\t" \
  "v_mfma_f32_32x32x16_bf16 v[96:111], v[228:231], a[72:75], v[96:111]\n\t" \
  "ds_read_b128 v[220:223], %[lw0] offset:54272\n\t" \
  "v_sub_f32 v116, v132, v118\n\t" \
  "v_fma_f32 v137, v116, v116, v137\n\t" \
  "s_waitcnt vmcnt(9) lgkmcnt(5)\n\t" \
  "v_mfma_f32_32x32x16_bf16 v[80:95], v[232:235], a[76:79], v[80:95]\n\t" \
  "ds_read_b128 v[224:227], %[lw0] offset:22528\n\t" \
  "buffer_load_dwordx4 a[116:119], %[l16], %[rs], %[t0] offen offset:1024 sc1\n\t" \
  "v_sub_f32 v116, v134, v118\n\t" \
  "v_fma_f32 v137, v116, v116, v137\n\t" \
  "s_waitcnt lgkmcnt(5)\n\t" \
  "v_mfma_f32_32x32x16_bf16 v[96:111], v[236:239], a[76:79], v[96:111]\n\t" \
  "ds_read_b128 v[228:231], %[lw0] offset:55296\n\t" \
  "v_fmac_f32 v138, 0x42000000, v137\n\t" \
  "v_mov_b32 v116, v138\n\t" \
  "s_waitcnt vmcnt(9) lgkmcnt(5)\n\t" \
  "v_mfma_f32_32x32x16_bf16 v[80:95], v[208:211], a[80:83], v[80:95]\n\t" \
  "ds_read_b128 v[232:235], %[lw0] offset:23552\n\t" \
  "buffer_load_dwordx4 a[120:123], %[l16], %[rs], %[t0] offen offset:2048 sc1\n\t" \
  "v_permlane32_swap_b32 v138, v116\n\t" \
  "v_add_f32 v138, v138, v116\n\t" \
  "s_waitcnt lgkmcnt(5)\n\t" \
  "v_mfma_f32_32x32x16_bf16 v[96:111], v[212:215], a[80:83], v[96:111]\n\t" \
  "ds_read_b128 v[236:239], %[lw0] offset:56320\n\t" \
  "v_mov_b32 v116, 0x3727c5ac\n\t" \
  "v_fmac_f32 v116, 0x3b000000, v138\n\t" \
  "s_waitcnt vmcnt(9) lgkmcnt(5)\n\t" \
  "v_mfma_f32_32x32x16_bf16 v[80:95], v[216:219], a[84:87], v[80:95]\n\t" \
  "ds_read_b128 v[208:211], %[lw0] offset:24576\n\t" \
  "buffer_load_dwordx4 a[124:127], %[l16], %[rs], %[t0] offen offset:3072 sc1\n\t" \
  "v_rsq_f32 v119, v116\n\t" \
  "s_nop 0\n\t" \
  "s_waitcnt lgkmcnt(5)\n\t" \
  "v_mfma_f32_32x32x16_bf16 v[96:111], v[220:223], a[84:87], v[96:111]\n\t" \
  "ds_read_b128 v[212:215], %[lw0] offset:57344\n\t" \
  "v_mul_f32 v136, 0x3eb8aa3b, v119\n\t" \
  "ds_write_b32 %[ltw], v119\n\t" \
  "s_waitcnt vmcnt(9) lgkmcnt(6)\n\t" \
  "v_mfma_f32_32x32x16_bf16 v[80:95], v[224:227], a[88:91], v[80:95]\n\t" \
  "ds_read_b128 v[216:219], %[lw0] offset:25600\n\t" \
  "s_waitcnt lgkmcnt(1)\n\t" \
  "ds_read_b128 v[156:159], %[ltr]\n\t" \
  "v_mfma_f32_32x32x16_bf16 v[96:111], v[228:231], a[88:91], v[96:111]\n\t" \
  "ds_read_b128 v[220:223], %[lw0] offset:58368\n\t" \
  "ds_read_b128 v[160:163], %[ltr] offset:32\n\t" \
  "ds_read_b128 v[164:167], %[ltr] offset:64\n\t" \
  "s_waitcnt vmcnt(8)\n\t" \
  "v_mfma_f32_32x32x16_bf16 v[80:95], v[232:235], a[92:95], v[80:95]\n\t" \
  "ds_read_b128 v[224:227], %[lw0] offset:26624\n\t" \
  "ds_read_b128 v[168:171], %[ltr] offset:96\n\t" \
  "v_mfma_f32_32x32x16_bf16 v[96:111], v[236:239], a[92:95], v[96:111]\n\t" \
  "ds_read_b128 v[228:231], %[lw0] offset:59392\n\t" \
  "s_waitcnt vmcnt(7)\n\t" \
  "v_mfma_f32_32x32x16_bf16 v[80:95], v[208:211], a[96:99], v[80:95]\n\t" \
  "ds_read_b128 v[232:235], %[lw0] offset:27648\n\t" \
  "v_mfma_f32_32x32x16_bf16 v[96:111], v[212:215], a[96:99], v[96:111]\n\t" \
  "ds_read_b128 v[236:239], %[lw0] offset:60416\n\t" \
  "s_waitcnt vmcnt(6) lgkmcnt(9)\n\t" \
  "v_mfma_f32_32x32x16_bf16 v[80:95], v[216:219], a[100:103], v[80:95]\n\t" \
  "ds_read_b128 v[208:211], %[lw0] offset:28672\n\t" \
  "s_waitcnt lgkmcnt(8)\n\t" \
  "v_mfma_f32_32x32x16_bf16 v[96:111], v[220:223], a[100:103], v[96:111]\n\t" \
  "ds_read_b128 v[212:215], %[lw0] offset:61440\n\t" \
  "s_waitcnt vmcnt(5) lgkmcnt(6)\n\t" \
  "v_mfma_f32_32x32x16_bf16 v[80:95], v[224:227], a[104:107], v[80:95]\n\t" \
  "ds_read_b128 v[216:219], %[lw0] offset:29696\n\t" \
  "s_waitcnt lgkmcnt(5)\n\t" \
  "v_mfma_f32_32x32x16_bf16 v[96:111], v[228:231], a[104:107], v[96:111]\n\t" \
  "ds_read_b128 v[220:223], %[lw0] offset:62464\n\t" \
  "s_waitcnt vmcnt(4) lgkmcnt(5)\n\t" \
  "v_mfma_f32_32x32x16_bf16 v[80:95], v[232:235], a[108:111], v[80:95]\n\t" \
  "ds_read_b128 v[224:227], %[lw0] offset:30720\n\t" \
  "s_waitcnt lgkmcnt(5)\n\t" \
  "v_mfma_f32_32x32x16_bf16 v[96:111], v[236:239], a[108:111], v[96:111]\n\t" \
  "ds_read_b128 v[228:231], %[lw0] offset:63488\n\t" \
  "s_waitcnt vmcnt(3) lgkmcnt(5)\n\t" \
  "v_mfma_f32_32x32x16_bf16 v[80:95], v[208:211], a[112:115], v[80:95]\n\t" \
  "ds_read_b128 v[232:235], %[lw0] offset:31744\n\t" \
  "s_waitcnt lgkmcnt(5)\n\t" \
  "v_mfma_f32_32x32x16_bf16 v[96:111], v[212:215], a[112:115], v[96:111]\n\t" \
  "ds_read_b128 v[236:239], %[lw0] offset:64512\n\t" \
  "s_waitcnt vmcnt(2) lgkmcnt(5)\n\t" \
  "v_mfma_f32_32x32x16_bf16 v[80:95], v[216:219], a[116:119], v[80:95]\n\t" \
  "s_waitcnt lgkmcnt(4)\n\t" \
  "v_mfma_f32_32x32x16_bf16 v[96:111], v[220:223], a[116:119], v[96:111]\n\t" \
  "s_waitcnt vmcnt(1) lgkmcnt(3)\n\t" \
  "v_mfma_f32_32x32x16_bf16 v[80:95], v[224:227], a[120:123], v[80:95]\n\t" \
  "s_barrier\n\t" \
  "ds_read_b128 v[216:219], %[lw1]\n\t" \
  "s_waitcnt lgkmcnt(3)\n\t" \
  "v_mfma_f32_32x32x16_bf16 v[96:111], v[228:231], a[120:123], v[96:111]\n\t" \
  "ds_read_b128 v[220:223], %[lw1] offset:1024\n\t" \
  "s_waitcnt vmcnt(0) lgkmcnt(3)\n\t" \
  "v_mfma_f32_32x32x16_bf16 v[80:95], v[232:235], a[124:127], v[80:95]\n\t" \
  "ds_read_b128 v[224:227], %[lw1] offset:2048\n\t" \
  "s_waitcnt lgkmcnt(3)\n\t" \
  "v_mfma_f32_32x32x16_bf16 v[96:111], v[236:239], a[124:127], v[96:111]\n\t" \
  "ds_read_b128 v[228:231], %[lw1] offset:3072\n\t" \
  "v_mfma_f32_32x32x16_bf16 v[80:95], v[176:179], v[112:115], v[80:95]\n\t" \
  "ds_read_b128 v[232:235], %[lw1] offset:4096\n\t" \
  "v_mfma_f32_32x32x16_bf16 v[96:111], v[180:183], v[112:115], v[96:111]\n\t" \
  "ds_read_b128 v[236:239], %[lw1] offset:5120\n\t" \
  "s_waitcnt lgkmcnt(5)\n\t" \
  "v_mfma_f32_32x32x16_bf16 v[192:207], a[0:3], v[216:219], 0\n\t" \
  "ds_read_b128 v[208:211], %[lw1] offset:6144\n\t" \
  "s_waitcnt lgkmcnt(5)\n\t" \
  "v_mfma_f32_32x32x16_bf16 v[192:207], a[4:7], v[220:223], v[192:207]\n\t" \
  "ds_read_b128 v[212:215], %[lw1] offset:7168\n\t" \
  "s_waitcnt lgkmcnt(5)\n\t" \
  "v_mfma_f32_32x32x16_bf16 v[192:207], a[8:11], v[224:227], v[192:207]\n\t" \
  "ds_read_b128 v[216:219], %[lw1] offset:8192\n\t" \
  "v_mul_f32 v80, v136, v80\n\t" \
  "v_mul_f32 v81, v136, v81\n\t" \
  "v_mul_f32 v82, v136, v82\n\t" \
  "v_mul_f32 v83, v136, v83\n\t" \
  "s_waitcnt lgkmcnt(5)\n\t" \
  "v_mfma_f32_32x32x16_bf16 v[192:207], a[12:15], v[228:231], v[192:207]\n\t" \
  "ds_read_b128 v[220:223], %[lw1] offset:9216\n\t" \
  "v_mul_f32 v84, v136, v84\n\t" \
  "v_mul_f32 v85, v136, v85\n\t" \
  "v_mul_f32 v86, v136, v86\n\t" \
  "v_mul_f32 v87, v136, v87\n\t" \
  "s_waitcnt lgkmcnt(5)\n\t" \
  "v_mfma_f32_32x32x16_bf16 v[192:207], a[16:19], v[232:235], v[192:207]\n\t" \
  "ds_read_b128 v[224:227], %[lw1] offset:10240\n\t" \
  "v_cvt_pk_bf16_f32 v140, v80, v81\n\t" \
  "v_cvt_pk_bf16_f32 v141, v82, v83\n\t" \
  "v_cvt_pk_bf16_f32 v142, v84, v85\n\t" \
  "v_cvt_pk_bf16_f32 v143, v86, v87\n\t" \
  "s_waitcnt lgkmcnt(5)\n\t" \
  "v_mfma_f32_32x32x16_bf16 v[192:207], a[20:23], v[236:239], v[192:207]\n\t" \
  "ds_read_b128 v[228:231], %[lw1] offset:11264\n\t" \
  "buffer_store_dwordx4 v[140:143], %[l16], %[rs], %[sq] offen sc1\n\t" \
  "v_mul_f32 v88, v136, v88\n\t" \
  "v_mul_f32 v89, v136, v89\n\t" \
  "v_mul_f32 v90, v136, v90\n\t" \
  "s_waitcnt lgkmcnt(5)\n\t" \
  "v_mfma_f32_32x32x16_bf16 v[192:207], a[24:27], v[208:211], v[192:207]\n\t" \
  "ds_read_b128 v[232:235], %[lw1] offset:12288\n\t" \
  "v_mul_f32 v91, v136, v91\n\t" \
  "v_mul_f32 v92, v136, v92\n\t" \
  "v_mul_f32 v93, v136, v93\n\t" \
  "v_mul_f32 v94, v136, v94\n\t" \
  "s_waitcnt lgkmcnt(5)\n\t" \
  "v_mfma_f32_32x32x16_bf16 v[192:207], a[28:31], v[212:215], v[192:207]\n\t" \
  "ds_read_b128 v[236:239], %[lw1] offset:13312\n\t" \
  "v_mul_f32 v95, v136, v95\n\t" \
  "v_cvt_pk_bf16_f32 v144, v88, v89\n\t" \
  "v_cvt_pk_bf16_f32 v145, v90, v91\n\t" \
  "v_cvt_pk_bf16_f32 v146, v92, v93\n\t" \
  "s_waitcnt lgkmcnt(5)\n\t" \
  "v_mfma_f32_32x32x16_bf16 v[192:207], a[32:35], v[216:219], v[192:207]\n\t" \
  "ds_read_b128 v[208:211], %[lw1] offset:14336\n\t" \
  "v_cvt_pk_bf16_f32 v147, v94, v95\n\t" \
  "buffer_store_dwordx4 v[144:147], %[l16], %[rs], %[sq1] offen sc1\n\t" \
  "v_mul_f32 v96, v119, v96\n\t" \
  "v_mul_f32 v97, v119, v97\n\t" \
  "s_waitcnt lgkmcnt(5)\n\t" \
  "v_mfma_f32_32x32x16_bf16 v[192:207], a[36:39], v[220:223], v[192:207]\n\t" \
  "ds_read_b128 v[212:215], %[lw1] offset:15360\n\t" \
  "v_mul_f32 v98, v119, v98\n\t" \
  "v_mul_f32 v99, v119, v99\n\t" \
  "v_mul_f32 v100, v119, v100\n\t" \
  "v_mul_f32 v101, v119, v101\n\t" \
  "s_waitcnt lgkmcnt(5)\n\t" \
  "v_mfma_f32_32x32x16_bf16 v[192:207], a[40:43], v[224:227], v[192:207]\n\t" \
  "ds_read_b128 v[216:219], %[lw1] offset:16384\n\t" \
  "v_mul_f32 v102, v119, v102\n\t" \
  "v_mul_f32 v103, v119, v103\n\t" \
  "v_cvt_pk_bf16_f32 v148, v96, v97\n\t" \
  "v_cvt_pk_bf16_f32 v149, v98, v99\n\t" \
  "s_waitcnt lgkmcnt(5)\n\t" \
  "v_mfma_f32_32x32x16_bf16 v[192:207], a[44:47], v[228:231], v[192:207]\n\t" \
  "ds_read_b128 v[220:223], %[lw1] offset:17408\n\t" \
  "v_cvt_pk_bf16_f32 v150, v100, v101\n\t" \
  "v_cvt_pk_bf16_f32 v151, v102, v103\n\t" \
  "buffer_store_dwordx4 v[148:151], %[l16], %[rs], %[sk] offen sc1\n\t" \
  "v_mul_f32 v172, v96, v96\n\t" \
  "s_waitcnt lgkmcnt(5)\n\t" \
  "v_mfma_f32_32x32x16_bf16 v[192:207], a[48:51], v[232:235], v[192:207]\n\t" \
  "ds_read_b128 v[224:227], %[lw1] offset:18432\n\t" \
  "v_fmac_f32 v172, v97, v97\n\t" \
  "v_fmac_f32 v172, v98, v98\n\t" \
  "v_fmac_f32 v172, v99, v99\n\t" \
  "v_fmac_f32 v172, v100, v100\n\t" \
  "s_waitcnt lgkmcnt(5)\n\t" \
  "v_mfma_f32_32x32x16_bf16 v[192:207], a[52:55], v[236:239], v[192:207]\n\t" \
  "ds_read_b128 v[228:231], %[lw1] offset:19456\n\t" \
  "v_fmac_f32 v172, v101, v101\n\t" \
  "v_fmac_f32 v172, v102, v102\n\t" \
  "v_fmac_f32 v172, v103, v103\n\t" \
  "v_mov_b32 v173, v172\n\t" \
  "s_waitcnt lgkmcnt(5)\n\t" \
  "v_mfma_f32_32x32x16_bf16 v[192:207], a[56:59], v[208:211], v[192:207]\n\t" \
  "ds_read_b128 v[232:235], %[lw1] offset:20480\n\t" \
  "v_permlane32_swap_b32 v172, v173\n\t" \
  "v_add_f32 v172, v172, v173\n\t" \
  "s_nop 1\n\t" \
  "v_max_f32_dpp v172, v172, v172 row_shr:1 row_mask:0xf bank_mask:0xf\n\t" \
  "s_nop 1\n\t" \
  "v_max_f32_dpp v172, v172, v172 row_shr:2 row_mask:0xf bank_mask:0xf\n\t" \
  "s_waitcnt lgkmcnt(5)\n\t" \
  "v_mfma_f32_32x32x16_bf16 v[192:207], a[60:63], v[212:215], v[192:207]\n\t" \
  "ds_read_b128 v[236:239], %[lw1] offset:21504\n\t" \
  "v_max_f32_dpp v172, v172, v172 row_shr:4 row_mask:0xf bank_mask:0xf\n\t" \
  "s_nop 1\n\t" \
  "v_max_f32_dpp v172, v172, v172 row_shr:8 row_mask:0xf bank_mask:0xf\n\t" \
  "s_nop 1\n\t" \
  "v_max_f32_dpp v172, v172, v172 row_bcast:15 row_mask:0xa bank_mask:0xf\n\t" \
  "buffer_store_dword v172, %[vkm], %[rs], %[skm] offen sc1\n\t" \
  "s_waitcnt lgkmcnt(5)\n\t" \
  "v_mfma_f32_32x32x16_bf16 v[192:207], a[64:67], v[216:219], v[192:207]\n\t" \
  "ds_read_b128 v[208:211], %[lw1] offset:22528\n\t" \
  "v_mul_f32 v104, v119, v104\n\t" \
  "v_mul_f32 v105, v119, v105\n\t" \
  "v_mul_f32 v106, v119, v106\n\t" \
  "v_mul_f32 v107, v119, v107\n\t" \
  "s_waitcnt lgkmcnt(5)\n\t" \
  "v_mfma_f32_32x32x16_bf16 v[192:207], a[68:71], v[220:223], v[192:207]\n\t" \
  "ds_read_b128 v[212:215], %[lw1] offset:23552\n\t" \
  "v_mul_f32 v108, v119, v108\n\t" \
  "v_mul_f32 v109, v119, v109\n\t" \
  "v_mul_f32 v110, v119, v110\n\t" \
  "v_mul_f32 v111, v119, v111\n\t" \
  "s_waitcnt lgkmcnt(5)\n\t" \
  "v_mfma_f32_32x32x16_bf16 v[192:207], a[72:75], v[224:227], v[192:207]\n\t" \
  "ds_read_b128 v[216:219], %[lw1] offset:24576\n\t" \
  "v_cvt_pk_bf16_f32 v152, v104, v105\n\t" \
  "v_cvt_pk_bf16_f32 v153, v106, v107\n\t" \
  "v_cvt_pk_bf16_f32 v154, v108, v109\n\t" \
  "v_cvt_pk_bf16_f32 v155, v110, v111\n\t" \
  "s_waitcnt lgkmcnt(5)\n\t" \
  "v_mfma_f32_32x32x16_bf16 v[192:207], a[76:79], v[228:231], v[192:207]\n\t" \
  "ds_read_b128 v[220:223], %[lw1] offset:25600\n\t" \
  "buffer_store_dwordx4 v[152:155], %[l16], %[rs], %[sk1] offen sc1\n\t" \
  "v_mul_f32 v174, v104, v104\n\t" \
  "v_fmac_f32 v174, v105, v105\n\t" \
  "v_fmac_f32 v174, v106, v106\n\t" \
  "s_waitcnt lgkmcnt(5)\n\t" \
  "v_mfma_f32_32x32x16_bf16 v[192:207], a[80:83], v[232:235], v[192:207]\n\t" \
  "ds_read_b128 v[224:227], %[lw1] offset:26624\n\t" \
  "v_fmac_f32 v174, v107, v107\n\t" \
  "v_fmac_f32 v174, v108, v108\n\t" \
  "v_fmac_f32 v174, v109, v109\n\t" \
  "v_fmac_f32 v174, v110, v110\n\t" \
  "s_waitcnt lgkmcnt(5)\n\t" \
  "v_mfma_f32_32x32x16_bf16 v[192:207], a[84:87], v[236:239], v[192:207]\n\t" \
  "ds_read_b128 v[228:231], %[lw1] offset:27648\n\t" \
  "v_fmac_f32 v174, v111, v111\n\t" \
  "v_mov_b32 v175, v174\n\t" \
  "s_nop 1\n\t" \
  "v_permlane32_swap_b32 v174, v175\n\t" \
  "v_add_f32 v174, v174, v175\n\t" \
  "s_waitcnt lgkmcnt(5)\n\t" \
  "v_mfma_f32_32x32x16_bf16 v[192:207], a[88:91], v[208:211], v[192:207]\n\t" \
  "ds_read_b128 v[232:235], %[lw1] offset:28672\n\t" \
  "v_max_f32_dpp v174, v174, v174 row_shr:1 row_mask:0xf bank_mask:0xf\n\t" \
  "s_nop 1\n\t" \
  "v_max_f32_dpp v174, v174, v174 row_shr:2 row_mask:0xf bank_mask:0xf\n\t" \
  "s_nop 1\n\t" \
  "v_max_f32_dpp v174, v174, v174 row_shr:4 row_mask:0xf bank_mask:0xf\n\t" \
  "s_nop 1\n\t" \
  "v_max_f32_dpp v174, v174, v174 row_shr:8 row_mask:0xf bank_mask:0xf\n\t" \
  "s_waitcnt lgkmcnt(5)\n\t" \
  "v_mfma_f32_32x32x16_bf16 v[192:207], a[92:95], v[212:215], v[192:207]\n\t" \
  "ds_read_b128 v[236:239], %[lw1] offset:29696\n\t" \
  "v_max_f32_dpp v174, v174, v174 row_bcast:15 row_mask:0xa bank_mask:0xf\n\t" \
  "buffer_store_dword v174, %[vkm], %[rs], %[skm] offen offset:256 sc1\n\t" \
  "s_waitcnt lgkmcnt(5)\n\t" \
  "v_mfma_f32_32x32x16_bf16 v[192:207], a[96:99], v[216:219], v[192:207]\n\t" \
  "ds_read_b128 v[208:211], %[lw1] offset:30720\n\t" \
  "s_waitcnt lgkmcnt(5)\n\t" \
  "v_mfma_f32_32x32x16_bf16 v[192:207], a[100:103], v[220:223], v[192:207]\n\t" \
  "ds_read_b128 v[212:215], %[lw1] offset:31744\n\t" \
  "s_waitcnt lgkmcnt(5)\n\t" \
  "v_mfma_f32_32x32x16_bf16 v[192:207], a[104:107], v[224:227], v[192:207]\n\t" \
  "s_waitcnt lgkmcnt(4)\n\t" \
  "v_mfma_f32_32x32x16_bf16 v[192:207], a[108:111], v[228:231], v[192:207]\n\t" \
  "s_waitcnt lgkmcnt(3)\n\t" \
  "v_mfma_f32_32x32x16_bf16 v[192:207], a[112:115], v[232:235], v[192:207]\n\t" \
  "s_waitcnt lgkmcnt(2)\n\t" \
  "v_mfma_f32_32x32x16_bf16 v[192:207], a[116:119], v[236:239], v[192:207]\n\t" \
  "s_waitcnt lgkmcnt(1)\n\t" \
  "v_mfma_f32_32x32x16_bf16 v[192:207], a[120:123], v[208:211], v[192:207]\n\t" \
  "s_waitcnt lgkmcnt(0)\n\t" \
  "v_mfma_f32_32x32x16_bf16 v[192:207], a[124:127], v[212:215], v[192:207]\n\t" \
  "v_mfma_f32_32x32x16_bf16 v[192:207], v[112:115], v[184:187], v[192:207]\n\t" \
  "s_nop 7\n\t" \
  "s_nop 3\n\t" \
  "v_mul_f32 v192, v156, v192\n\t" \
  "v_mul_f32 v193, v157, v193\n\t" \
  "v_mul_f32 v194, v158, v194\n\t" \
  "v_mul_f32 v195, v159, v195\n\t" \
  "v_mul_f32 v196, v160, v196\n\t" \
  "v_mul_f32 v197, v161, v197\n\t" \
  "v_mul_f32 v198, v162, v198\n\t" \
  "v_mul_f32 v199, v163, v199\n\t" \
  "v_cvt_pk_bf16_f32 v140, v192, v193\n\t" \
  "v_cvt_pk_bf16_f32 v141, v194, v195\n\t" \
  "v_cvt_pk_bf16_f32 v142, v196, v197\n\t" \
  "v_cvt_pk_bf16_f32 v143, v198, v199\n\t" \
  "buffer_store_dwordx4 v[140:143], %[vv], %[rs], %[svv] offen sc1\n\t" \
  "v_mul_f32 v200, v164, v200\n\t" \
  "v_mul_f32 v201, v165, v201\n\t" \
  "v_mul_f32 v202, v166, v202\n\t" \
  "v_mul_f32 v203, v167, v203\n\t" \
  "v_mul_f32 v204, v168, v204\n\t" \
  "v_mul_f32 v205, v169, v205\n\t" \
  "v_mul_f32 v206, v170, v206\n\t" \
  "v_mul_f32 v207, v171, v207\n\t" \
  "v_cvt_pk_bf16_f32 v144, v200, v201\n\t" \
  "v_cvt_pk_bf16_f32 v145, v202, v203\n\t" \
  "v_cvt_pk_bf16_f32 v146, v204, v205\n\t" \
  "v_cvt_pk_bf16_f32 v147, v206, v207\n\t" \
  "buffer_store_dwordx4 v[144:147], %[vv], %[rs], %[svv] offen offset:512 sc1\n\t"
#define Q1_ASM \
  "s_nop 4\n\t" \
  "s_add_u32 %[sq1], %[sq], 65536\n\t" \
  "s_add_u32 %[sk1], %[sk], 65536\n\t" \
  "buffer_load_dwordx2 v[120:121], %[sv], %[rs], %[sst] offen sc1\n\t" \
  "buffer_load_dwordx2 v[122:123], %[sv], %[rs], %[sst] offen offset:256 sc1\n\t" \
  "buffer_load_dwordx2 v[124:125], %[sv], %[rs], %[sst] offen offset:512 sc1\n\t" \
  "buffer_load_dwordx2 v[126:127], %[sv], %[rs], %[sst] offen offset:768 sc1\n\t" \
  "buffer_load_dwordx2 v[128:129], %[sv], %[rs], %[sst] offen offset:1024 sc1\n\t" \
  "buffer_load_dwordx2 v[130:131], %[sv], %[rs], %[sst] offen offset:1280 sc1\n\t" \
  "buffer_load_dwordx2 v[132:133], %[sv], %[rs], %[sst] offen offset:1536 sc1\n\t" \
  "buffer_load_dwordx2 v[134:135], %[sv], %[rs], %[sst] offen offset:1792 sc1\n\t" \
  "s_mov_b32 %[t0], %[sact]\n\t" \
  "buffer_load_dwordx4 a[0:3], %[l16], %[rs], %[t0] offen sc1\n\t" \
  "buffer_load_dwordx4 a[4:7], %[l16], %[rs], %[t0] offen offset:1024 sc1\n\t" \
  "buffer_load_dwordx4 a[8:11], %[l16], %[rs], %[t0] offen offset:2048 sc1\n\t" \
  "buffer_load_dwordx4 a[12:15], %[l16], %[rs], %[t0] offen offset:3072 sc1\n\t" \
  "s_add_u32 %[t0], %[t0], 4096\n\t" \
  "buffer_load_dwordx4 a[16:19], %[l16], %[rs], %[t0] offen sc1\n\t" \
  "buffer_load_dwordx4 a[20:23], %[l16], %[rs], %[t0] offen offset:1024 sc1\n\t" \
  "buffer_load_dwordx4 a[24:27], %[l16], %[rs], %[t0] offen offset:2048 sc1\n\t" \
  "buffer_load_dwordx4 a[28:31], %[l16], %[rs], %[t0] offen offset:3072 sc1\n\t" \
  "s_add_u32 %[t0], %[t0], 4096\n\t" \
  "buffer_load_dwordx4 a[32:35], %[l16], %[rs], %[t0] offen sc1\n\t" \
  "buffer_load_dwordx4 a[36:39], %[l16], %[rs], %[t0] offen offset:1024 sc1\n\t" \
  "s_waitcnt vmcnt(34)\n\t" \
  "s_barrier\n\t" \
  "ds_read_b128 v[208:211], %[lw0]\n\t" \
  "ds_read_b128 v[212:215], %[lw0] offset:32768\n\t" \
  "ds_read_b128 v[216:219], %[lw0] offset:1024\n\t" \
  "ds_read_b128 v[220:223], %[lw0] offset:33792\n\t" \
  "ds_read_b128 v[224:227], %[lw0] offset:2048\n\t" \
  "ds_read_b128 v[228:231], %[lw0] offset:34816\n\t" \
  "s_waitcnt vmcnt(9) lgkmcnt(5)\n\t" \
  "v_mfma_f32_32x32x16_bf16 v[80:95], v[208:211], a[0:3], 0\n\t" \
  "ds_read_b128 v[232:235], %[lw0] offset:3072\n\t" \
  "buffer_load_dwordx4 a[40:43], %[l16], %[rs], %[t0] offen offset:2048 sc1\n\t" \
  "s_waitcnt lgkmcnt(5)\n\t" \
  "v_mfma_f32_32x32x16_bf16 v[96:111], v[212:215], a[0:3], 0\n\t" \
  "ds_read_b128 v[236:239], %[lw0] offset:35840\n\t" \
  "s_waitcnt vmcnt(9) lgkmcnt(5)\n\t" \
  "v_mfma_f32_32x32x16_bf16 v[80:95], v[216:219], a[4:7], v[80:95]\n\t" \
  "ds_read_b128 v[208:211], %[lw0] offset:4096\n\t" \
  "buffer_load_dwordx4 a[44:47], %[l16], %[rs], %[t0] offen offset:3072 sc1\n\t" \
  "s_waitcnt lgkmcnt(5)\n\t" \
  "v_mfma_f32_32x32x16_bf16 v[96:111], v[220:223], a[4:7], v[96:111]\n\t" \
  "ds_read_b128 v[212:215], %[lw0] offset:36864\n\t" \
  "s_waitcnt vmcnt(9) lgkmcnt(5)\n\t" \
  "v_mfma_f32_32x32x16_bf16 v[80:95], v[224:227], a[8:11], v[80:95]\n\t" \
  "ds_read_b128 v[216:219], %[lw0] offset:5120\n\t" \
  "s_add_u32 %[t0], %[t0], 4096\n\t" \
  "buffer_load_dwordx4 a[48:51], %[l16], %[rs], %[t0] offen sc1\n\t" \
  "v_add_f32 v118, v120, v122\n\t" \
  "s_waitcnt lgkmcnt(5)\n\t" \
  "v_mfma_f32_32x32x16_bf16 v[96:111], v[228:231], a[8:11], v[96:111]\n\t" \
  "ds_read_b128 v[220:223], %[lw0] offset:37888\n\t" \
  "v_add_f32 v118, v118, v124\n\t" \
  "v_add_f32 v118, v118, v126\n\t" \
  "s_waitcnt vmcnt(9) lgkmcnt(5)\n\t" \
  "v_mfma_f32_32x32x16_bf16 v[80:95], v[232:235], a[12:15], v[80:95]\n\t" \
  "ds_read_b128 v[224:227], %[lw0] offset:6144\n\t" \
  "buffer_load_dwordx4 a[52:55], %[l16], %[rs], %[t0] offen offset:1024 sc1\n\t" \
  "v_add_f32 v118, v118, v128\n\t" \
  "v_add_f32 v118, v118, v130\n\t" \
  "s_waitcnt lgkmcnt(5)\n\t" \
  "v_mfma_f32_32x32x16_bf16 v[96:111], v[236:239], a[12:15], v[96:111]\n\t" \
  "ds_read_b128 v[228:231], %[lw0] offset:38912\n\t" \
  "v_add_f32 v118, v118, v132\n\t" \
  "v_add_f32 v118, v118, v134\n\t" \
  "s_waitcnt vmcnt(9) lgkmcnt(5)\n\t" \
  "v_mfma_f32_32x32x16_bf16 v[80:95], v[208:211], a[16:19], v[80:95]\n\t" \
  "ds_read_b128 v[232:235], %[lw0] offset:7168\n\t" \
  "buffer_load_dwordx4 a[56:59], %[l16], %[rs], %[t0] offen offset:2048 sc1\n\t" \
  "v_mov_b32 v116, v118\n\t" \
  "s_nop 1\n\t" \
  "v_permlane32_swap_b32 v118, v116\n\t" \
  "s_waitcnt lgkmcnt(5)\n\t" \
  "v_mfma_f32_32x32x16_bf16 v[96:111], v[212:215], a[16:19], v[96:111]\n\t" \
  "ds_read_b128 v[236:239], %[lw0] offset:39936\n\t" \
  "v_add_f32 v118, v118, v116\n\t" \
  "v_mul_f32 v118, 0x3d800000, v118\n\t" \
  "s_waitcnt vmcnt(9) lgkmcnt(5)\n\t" \
  "v_mfma_f32_32x32x16_bf16 v[80:95], v[216:219], a[20:23], v[80:95]\n\t" \
  "ds_read_b128 v[208:211], %[lw0] offset:8192\n\t" \
  "buffer_load_dwordx4 a[60:63], %[l16], %[rs], %[t0] offen offset:3072 sc1\n\t" \
  "v_mul_f32 v116, v118, %[hm]\n\t" \
  "v_cvt_pk_bf16_f32 v117, v116, v116\n\t" \
  "s_waitcnt lgkmcnt(5)\n\t" \
  "v_mfma_f32_32x32x16_bf16 v[96:111], v[220:223], a[20:23], v[96:111]\n\t" \
  "ds_read_b128 v[212:215], %[lw0] offset:40960\n\t" \
  "v_mov_b32 v114, 0\n\t" \
  "v_lshlrev_b32 v117, 16, v117\n\t" \
  "s_waitcnt vmcnt(9) lgkmcnt(5)\n\t" \
  "v_mfma_f32_32x32x16_bf16 v[80:95], v[224:227], a[24:27], v[80:95]\n\t" \
  "ds_read_b128 v[216:219], %[lw0] offset:9216\n\t" \
  "s_add_u32 %[t0], %[t0], 4096\n\t" \
  "buffer_load_dwordx4 a[64:67], %[l16], %[rs], %[t0] offen sc1\n\t" \
  "v_mov_b32 v115, 0\n\t" \
  "v_sub_f32 v117, v116, v117\n\t" \
  "s_waitcnt lgkmcnt(5)\n\t" \
  "v_mfma_f32_32x32x16_bf16 v[96:111], v[228:231], a[24:27], v[96:111]\n\t" \
  "ds_read_b128 v[220:223], %[lw0] offset:41984\n\t" \
  "v_cvt_pk_bf16_f32 v113, v116, v114\n\t" \
  "v_cvt_pk_bf16_f32 v112, v116, v117\n\t" \
  "s_waitcnt vmcnt(9) lgkmcnt(5)\n\t" \
  "v_mfma_f32_32x32x16_bf16 v[80:95], v[232:235], a[28:31], v[80:95]\n\t" \
  "ds_read_b128 v[224:227], %[lw0] offset:10240\n\t" \
  "buffer_load_dwordx4 a[68:71], %[l16], %[rs], %[t0] offen offset:1024 sc1\n\t" \
  "v_add_f32 v138, v121, v123\n\t" \
  "v_add_f32 v138, v138, v125\n\t" \
  "s_waitcnt lgkmcnt(5)\n\t" \
  "v_mfma_f32_32x32x16_bf16 v[96:111], v[236:239], a[28:31], v[96:111]\n\t" \
  "ds_read_b128 v[228:231], %[lw0] offset:43008\n\t" \
  "v_add_f32 v138, v138, v127\n\t" \
  "v_add_f32 v138, v138, v129\n\t" \
  "s_waitcnt vmcnt(9) lgkmcnt(5)\n\t" \
  "v_mfma_f32_32x32x16_bf16 v[80:95], v[208:211], a[32:35], v[80:95]\n\t" \
  "ds_read_b128 v[232:235], %[lw0] offset:11264\n\t" \
  "buffer_load_dwordx4 a[72:75], %[l16], %[rs], %[t0] offen offset:2048 sc1\n\t" \
  "v_add_f32 v138, v138, v131\n\t" \
  "v_add_f32 v138, v138, v133\n\t" \
  "s_waitcnt lgkmcnt(5)\n\t" \
  "v_mfma_f32_32x32x16_bf16 v[96:111], v[212:215], a[32:35], v[96:111]\n\t" \
  "ds_read_b128 v[236:239], %[lw0] offset:44032\n\t" \
  "v_add_f32 v138, v138, v135\n\t" \
  "v_sub_f32 v116, v120, v118\n\t" \
  "s_waitcnt vmcnt(9) lgkmcnt(5)\n\t" \
  "v_mfma_f32_32x32x16_bf16 v[80:95], v[216:219], a[36:39], v[80:95]\n\t" \
  "ds_read_b128 v[208:211], %[lw0] offset:12288\n\t" \
  "buffer_load_dwordx4 a[76:79], %[l16], %[rs], %[t0] offen offset:3072 sc1\n\t" \
  "v_fma_f32 v137, v116, v116, 0\n\t" \
  "v_sub_f32 v116, v122, v118\n\t" \
  "s_waitcnt lgkmcnt(5)\n\t" \
  "v_mfma_f32_32x32x16_bf16 v[96:111], v[220:223], a[36:39], v[96:111]\n\t" \
  "ds_read_b128 v[212:215], %[lw0] offset:45056\n\t" \
  "v_fma_f32 v137, v116, v116, v137\n\t" \
  "v_sub_f32 v116, v124, v118\n\t" \
  "s_waitcnt vmcnt(9) lgkmcnt(5)\n\t" \
  "v_mfma_f32_32x32x16_bf16 v[80:95], v[224:227], a[40:43], v[80:95]\n\t" \
  "ds_read_b128 v[216:219], %[lw0] offset:13312\n\t" \
  "s_add_u32 %[t0], %[t0], 4096\n\t" \
  "buffer_load_dwordx4 a[80:83], %[l16], %[rs], %[t0] offen sc1\n\t" \
  "v_fma_f32 v137, v116, v116, v137\n\t" \
  "v_sub_f32 v116, v126, v118\n\t" \
  "s_waitcnt lgkmcnt(5)\n\t" \
  "v_mfma_f32_32x32x16_bf16 v[96:111], v[228:231], a[40:43], v[96:111]\n\t" \
  "ds_read_b128 v[220:223], %[lw0] offset:46080\n\t" \
  "v_fma_f32 v137, v116, v116, v137\n\t" \
  "v_sub_f32 v116, v128, v118\n\t" \
  "s_waitcnt vmcnt(9) lgkmcnt(5)\n\t" \
  "v_mfma_f32_32x32x16_bf16 v[80:95], v[232:235], a[44:47], v[80:95]\n\t" \
  "ds_read_b128 v[224:227], %[lw0] offset:14336\n\t" \
  "buffer_load_dwordx4 a[84:87], %[l16], %[rs], %[t0] offen offset:1024 sc1\n\t" \
  "v_fma_f32 v137, v116, v116, v137\n\t" \
  "v_sub_f32 v116, v130, v118\n\t" \
  "s_waitcnt lgkmcnt(5)\n\t" \
  "v_mfma_f32_32x32x16_bf16 v[96:111], v[236:239], a[44:47], v[96:111]\n\t" \
  "ds_read_b128 v[228:231], %[lw0] offset:47104\n\t" \
  "v_fma_f32 v137, v116, v116, v137\n\t" \
  "v_sub_f32 v116, v132, v118\n\t" \
  "s_waitcnt vmcnt(9) lgkmcnt(5)\n\t" \
  "v_mfma_f32_32x32x16_bf16 v[80:95], v[208:211], a[48:51], v[80:95]\n\t" \
  "ds_read_b128 v[232:235], %[lw0] offset:15360\n\t" \
  "buffer_load_dwordx4 a[88:91], %[l16], %[rs], %[t0] offen offset:2048 sc1\n\t" \
  "v_fma_f32 v137, v116, v116, v137\n\t" \
  "v_sub_f32 v116, v134, v118\n\t" \
  "s_waitcnt lgkmcnt(5)\n\t" \
  "v_mfma_f32_32x32x16_bf16 v[96:111], v[212:215], a[48:51], v[96:111]\n\t" \
  "ds_read_b128 v[236:239], %[lw0] offset:48128\n\t" \
  "v_fma_f32 v137, v116, v116, v137\n\t" \
  "v_fmac_f32 v138, 0x42000000, v137\n\t" \
  "s_waitcnt vmcnt(9) lgkmcnt(5)\n\t" \
  "v_mfma_f32_32x32x16_bf16 v[80:95], v[216:219], a[52:55], v[80:95]\n\t" \
  "s_barrier\n\t" \
  "ds_read_b128 v[208:211], %[lw0] offset:16384\n\t" \
  "buffer_load_dwordx4 a[92:95], %[l16], %[rs], %[t0] offen offset:3072 sc1\n\t" \
  "v_mov_b32 v116, v138\n\t" \
  "s_nop 1\n\t" \
  "v_permlane32_swap_b32 v138, v116\n\t" \
  "s_waitcnt lgkmcnt(5)\n\t" \
  "v_mfma_f32_32x32x16_bf16 v[96:111], v[220:223], a[52:55], v[96:111]\n\t" \
  "ds_read_b128 v[212:215], %[lw0] offset:49152\n\t" \
  "v_add_f32 v138, v138, v116\n\t" \
  "v_mov_b32 v116, 0x3727c5ac\n\t" \
  "s_waitcnt vmcnt(9) lgkmcnt(5)\n\t" \
  "v_mfma_f32_32x32x16_bf16 v[80:95], v[224:227], a[56:59], v[80:95]\n\t" \
  "ds_read_b128 v[216:219], %[lw0] offset:17408\n\t" \
  "s_add_u32 %[t0], %[t0], 4096\n\t" \
  "buffer_load_dwordx4 a[96:99], %[l16], %[rs], %[t0] offen sc1\n\t" \
  "v_fmac_f32 v116, 0x3b000000, v138\n\t" \
  "v_rsq_f32 v119, v116\n\t" \
  "s_waitcnt lgkmcnt(5)\n\t" \
  "v_mfma_f32_32x32x16_bf16 v[96:111], v[228:231], a[56:59], v[96:111]\n\t" \
  "ds_read_b128 v[220:223], %[lw0] offset:50176\n\t" \
  "s_nop 0\n\t" \
  "v_mul_f32 v136, 0x3eb8aa3b, v119\n\t" \
  "s_waitcnt vmcnt(9) lgkmcnt(5)\n\t" \
  "v_mfma_f32_32x32x16_bf16 v[80:95], v[232:235], a[60:63], v[80:95]\n\t" \
  "ds_read_b128 v[224:227], %[lw0] offset:18432\n\t" \
  "buffer_load_dwordx4 a[100:103], %[l16], %[rs], %[t0] offen offset:1024 sc1\n\t" \
  "ds_write_b32 %[ltw], v119\n\t" \
  "s_waitcnt lgkmcnt(0)\n\t" \
  "v_mfma_f32_32x32x16_bf16 v[96:111], v[236:239], a[60:63], v[96:111]\n\t" \
  "ds_read_b128 v[228:231], %[lw0] offset:51200\n\t" \
  "ds_read_b128 v[156:159], %[ltr]\n\t" \
  "ds_read_b128 v[160:163], %[ltr] offset:32\n\t" \
  "s_waitcnt vmcnt(9)\n\t" \
  "v_mfma_f32_32x32x16_bf16 v[80:95], v[208:211], a[64:67], v[80:95]\n\t" \
  "ds_read_b128 v[232:235], %[lw0] offset:19456\n\t" \
  "buffer_load_dwordx4 a[104:107], %[l16], %[rs], %[t0] offen offset:2048 sc1\n\t" \
  "ds_read_b128 v[164:167], %[ltr] offset:64\n\t" \
  "ds_read_b128 v[168:171], %[ltr] offset:96\n\t" \
  "v_mfma_f32_32x32x16_bf16 v[96:111], v[212:215], a[64:67], v[96:111]\n\t" \
  "ds_read_b128 v[236:239], %[lw0] offset:52224\n\t" \
  "s_waitcnt vmcnt(9)\n\t" \
  "v_mfma_f32_32x32x16_bf16 v[80:95], v[216:219], a[68:71], v[80:95]\n\t" \
  "ds_read_b128 v[208:211], %[lw0] offset:20480\n\t" \
  "buffer_load_dwordx4 a[108:111], %[l16], %[rs], %[t0] offen offset:3072 sc1\n\t" \
  "v_mfma_f32_32x32x16_bf16 v[96:111], v[220:223], a[68:71], v[96:111]\n\t" \
  "ds_read_b128 v[212:215], %[lw0] offset:53248\n\t" \
  "s_waitcnt vmcnt(9)\n\t" \
  "v_mfma_f32_32x32x16_bf16 v[80:95], v[224:227], a[72:75], v[80:95]\n\t" \
  "ds_read_b128 v[216:219], %[lw0] offset:21504\n\t" \
  "s_add_u32 %[t0], %[t0], 4096\n\t" \
  "buffer_load_dwordx4 a[112:115], %[l16], %[rs], %[t0] offen sc1\n\t" \
  "s_waitcnt lgkmcnt(9)\n\t" \
  "v_mfma_f32_32x32x16_bf16 v[96:111], v[228:231], a[72:75], v[96:111]\n\t" \
  "ds_read_b128 v[220:223], %[lw0] offset:54272\n\t" \
  "s_waitcnt vmcnt(9) lgkmcnt(7)\n\t" \
  "v_mfma_f32_32x32x16_bf16 v[80:95], v[232:235], a[76:79], v[80:95]\n\t" \
  "ds_read_b128 v[224:227], %[lw0] offset:22528\n\t" \
  "buffer_load_dwordx4 a[116:119], %[l16], %[rs], %[t0] offen offset:1024 sc1\n\t" \
  "s_waitcnt lgkmcnt(5)\n\t" \
  "v_mfma_f32_32x32x16_bf16 v[96:111], v[236:239], a[76:79], v[96:111]\n\t" \
  "ds_read_b128 v[228:231], %[lw0] offset:55296\n\t" \
  "s_waitcnt vmcnt(9) lgkmcnt(5)\n\t" \
  "v_mfma_f32_32x32x16_bf16 v[80:95], v[208:211], a[80:83], v[80:95]\n\t" \
  "ds_read_b128 v[232:235], %[lw0] offset:23552\n\t" \
  "buffer_load_dwordx4 a[120:123], %[l16], %[rs], %[t0] offen offset:2048 sc1\n\t" \
  "s_waitcnt lgkmcnt(5)\n\t" \
  "v_mfma_f32_32x32x16_bf16 v[96:111], v[212:215], a[80:83], v[96:111]\n\t" \
  "ds_read_b128 v[236:239], %[lw0] offset:56320\n\t" \
  "s_waitcnt vmcnt(9) lgkmcnt(5)\n\t" \
  "v_mfma_f32_32x32x16_bf16 v[80:95], v[216:219], a[84:87], v[80:95]\n\t" \
  "ds_read_b128 v[208:211], %[lw0] offset:24576\n\t" \
  "buffer_load_dwordx4 a[124:127], %[l16], %[rs], %[t0] offen offset:3072 sc1\n\t" \
  "s_waitcnt lgkmcnt(5)\n\t" \
  "v_mfma_f32_32x32x16_bf16 v[96:111], v[220:223], a[84:87], v[96:111]\n\t" \
  "ds_read_b128 v[212:215], %[lw0] offset:57344\n\t" \
  "s_waitcnt vmcnt(9) lgkmcnt(5)\n\t" \
  "v_mfma_f32_32x32x16_bf16 v[80:95], v[224:227], a[88:91], v[80:95]\n\t" \
  "ds_read_b128 v[216:219], %[lw0] offset:25600\n\t" \
  "s_waitcnt lgkmcnt(5)\n\t" \
  "v_mfma_f32_32x32x16_bf16 v[96:111], v[228:231], a[88:91], v[96:111]\n\t" \
  "ds_read_b128 v[220:223], %[lw0] offset:58368\n\t" \
  "s_waitcnt vmcnt(8) lgkmcnt(5)\n\t" \
  "v_mfma_f32_32x32x16_bf16 v[80:95], v[232:235], a[92:95], v[80:95]\n\t" \
  "ds_read_b128 v[224:227], %[lw0] offset:26624\n\t" \
  "s_waitcnt lgkmcnt(5)\n\t" \
  "v_mfma_f32_32x32x16_bf16 v[96:111], v[236:239], a[92:95], v[96:111]\n\t" \
  "ds_read_b128 v[228:231], %[lw0] offset:59392\n\t" \
  "s_waitcnt vmcnt(7) lgkmcnt(5)\n\t" \
  "v_mfma_f32_32x32x16_bf16 v[80:95], v[208:211], a[96:99], v[80:95]\n\t" \
  "ds_read_b128 v[232:235], %[lw0] offset:27648\n\t" \
  "s_waitcnt lgkmcnt(5)\n\t" \
  "v_mfma_f32_32x32x16_bf16 v[96:111], v[212:215], a[96:99], v[96:111]\n\t" \
  "ds_read_b128 v[236:239], %[lw0] offset:60416\n\t" \
  "s_waitcnt vmcnt(6) lgkmcnt(5)\n\t" \
  "v_mfma_f32_32x32x16_bf16 v[80:95], v[216:219], a[100:103], v[80:95]\n\t" \
  "ds_read_b128 v[208:211], %[lw0] offset:28672\n\t" \
  "s_waitcnt lgkmcnt(5)\n\t" \
  "v_mfma_f32_32x32x16_bf16 v[96:111], v[220:223], a[100:103], v[96:111]\n\t" \
  "ds_read_b128 v[212:215], %[lw0] offset:61440\n\t" \
  "s_waitcnt vmcnt(5) lgkmcnt(5)\n\t" \
  "v_mfma_f32_32x32x16_bf16 v[80:95], v[224:227], a[104:107], v[80:95]\n\t" \
  "ds_read_b128 v[216:219], %[lw0] offset:29696\n\t" \
  "s_waitcnt lgkmcnt(5)\n\t" \
  "v_mfma_f32_32x32x16_bf16 v[96:111], v[228:231], a[104:107], v[96:111]\n\t" \
  "ds_read_b128 v[220:223], %[lw0] offset:62464\n\t" \
  "s_waitcnt vmcnt(4) lgkmcnt(5)\n\t" \
  "v_mfma_f32_32x32x16_bf16 v[80:95], v[232:235], a[108:111], v[80:95]\n\t" \
  "ds_read_b128 v[224:227], %[lw0] offset:30720\n\t" \
  "s_waitcnt lgkmcnt(5)\n\t" \
  "v_mfma_f32_32x32x16_bf16 v[96:111], v[236:239], a[108:111], v[96:111]\n\t" \
  "ds_read_b128 v[228:231], %[lw0] offset:63488\n\t" \
  "s_waitcnt vmcnt(3) lgkmcnt(5)\n\t" \
  "v_mfma_f32_32x32x16_bf16 v[80:95], v[208:211], a[112:115], v[80:95]\n\t" \
  "ds_read_b128 v[232:235], %[lw0] offset:31744\n\t" \
  "s_waitcnt lgkmcnt(5)\n\t" \
  "v_mfma_f32_32x32x16_bf16 v[96:111], v[212:215], a[112:115], v[96:111]\n\t" \
  "ds_read_b128 v[236:239], %[lw0] offset:64512\n\t" \
  "s_waitcnt vmcnt(2) lgkmcnt(5)\n\t" \
  "v_mfma_f32_32x32x16_bf16 v[80:95], v[216:219], a[116:119], v[80:95]\n\t" \
  "ds_read_b128 v[208:211], %[lext]\n\t" \
  "s_waitcnt lgkmcnt(5)\n\t" \
  "v_mfma_f32_32x32x16_bf16 v[96:111], v[220:223], a[116:119], v[96:111]\n\t" \
  "ds_read_b128 v[212:215], %[lext] offset:1024\n\t" \
  "s_waitcnt vmcnt(1) lgkmcnt(5)\n\t" \
  "v_mfma_f32_32x32x16_bf16 v[80:95], v[224:227], a[120:123], v[80:95]\n\t" \
  "s_barrier\n\t" \
  "ds_read_b128 v[216:219], %[lw1]\n\t" \
  "s_waitcnt lgkmcnt(5)\n\t" \
  "v_mfma_f32_32x32x16_bf16 v[96:111], v[228:231], a[120:123], v[96:111]\n\t" \
  "ds_read_b128 v[220:223], %[lw1] offset:1024\n\t" \
  "s_waitcnt vmcnt(0) lgkmcnt(5)\n\t" \
  "v_mfma_f32_32x32x16_bf16 v[80:95], v[232:235], a[124:127], v[80:95]\n\t" \
  "ds_read_b128 v[224:227], %[lw1] offset:2048\n\t" \
  "s_waitcnt lgkmcnt(5)\n\t" \
  "v_mfma_f32_32x32x16_bf16 v[96:111], v[236:239], a[124:127], v[96:111]\n\t" \
  "ds_read_b128 v[228:231], %[lw1] offset:3072\n\t" \
  "s_waitcnt lgkmcnt(5)\n\t" \
  "v_mfma_f32_32x32x16_bf16 v[80:95], v[208:211], v[112:115], v[80:95]\n\t" \
  "ds_read_b128 v[232:235], %[lw1] offset:4096\n\t" \
  "s_waitcnt lgkmcnt(5)\n\t" \
  "v_mfma_f32_32x32x16_bf16 v[96:111], v[212:215], v[112:115], v[96:111]\n\t" \
  "ds_read_b128 v[236:239], %[lw1] offset:5120\n\t" \
  "s_waitcnt lgkmcnt(5)\n\t" \
  "v_mfma_f32_32x32x16_bf16 v[192:207], a[0:3], v[216:219], 0\n\t" \
  "ds_read_b128 v[208:211], %[lw1] offset:6144\n\t" \
  "s_waitcnt lgkmcnt(5)\n\t" \
  "v_mfma_f32_32x32x16_bf16 v[192:207], a[4:7], v[220:223], v[192:207]\n\t" \
  "ds_read_b128 v[212:215], %[lw1] offset:7168\n\t" \
  "s_waitcnt lgkmcnt(5)\n\t" \
  "v_mfma_f32_32x32x16_bf16 v[192:207], a[8:11], v[224:227], v[192:207]\n\t" \
  "ds_read_b128 v[216:219], %[lw1] offset:8192\n\t" \
  "v_mul_f32 v80, v136, v80\n\t" \
  "v_mul_f32 v81, v136, v81\n\t" \
  "v_mul_f32 v82, v136, v82\n\t" \
  "v_mul_f32 v83, v136, v83\n\t" \
  "s_waitcnt lgkmcnt(5)\n\t" \
  "v_mfma_f32_32x32x16_bf16 v[192:207], a[12:15], v[228:231], v[192:207]\n\t" \
  "ds_read_b128 v[220:223], %[lw1] offset:9216\n\t" \
  "v_mul_f32 v84, v136, v84\n\t" \
  "v_mul_f32 v85, v136, v85\n\t" \
  "v_mul_f32 v86, v136, v86\n\t" \
  "v_mul_f32 v87, v136, v87\n\t" \
  "s_waitcnt lgkmcnt(5)\n\t" \
  "v_mfma_f32_32x32x16_bf16 v[192:207], a[16:19], v[232:235], v[192:207]\n\t" \
  "ds_read_b128 v[224:227], %[lw1] offset:10240\n\t" \
  "v_cvt_pk_bf16_f32 v140, v80, v81\n\t" \
  "v_cvt_pk_bf16_f32 v141, v82, v83\n\t" \
  "v_cvt_pk_bf16_f32 v142, v84, v85\n\t" \
  "v_cvt_pk_bf16_f32 v143, v86, v87\n\t" \
  "s_waitcnt lgkmcnt(5)\n\t" \
  "v_mfma_f32_32x32x16_bf16 v[192:207], a[20:23], v[236:239], v[192:207]\n\t" \
  "ds_read_b128 v[228:231], %[lw1] offset:11264\n\t" \
  "buffer_store_dwordx4 v[140:143], %[l16], %[rs], %[sq] offen sc1\n\t" \
  "v_mul_f32 v88, v136, v88\n\t" \
  "v_mul_f32 v89, v136, v89\n\t" \
  "v_mul_f32 v90, v136, v90\n\t" \
  "s_waitcnt lgkmcnt(5)\n\t" \
  "v_mfma_f32_32x32x16_bf16 v[192:207], a[24:27], v[208:211], v[192:207]\n\t" \
  "ds_read_b128 v[232:235], %[lw1] offset:12288\n\t" \
  "v_mul_f32 v91, v136, v91\n\t" \
  "v_mul_f32 v92, v136, v92\n\t" \
  "v_mul_f32 v93, v136, v93\n\t" \
  "v_mul_f32 v94, v136, v94\n\t" \
  "s_waitcnt lgkmcnt(5)\n\t" \
  "v_mfma_f32_32x32x16_bf16 v[192:207], a[28:31], v[212:215], v[192:207]\n\t" \
  "ds_read_b128 v[236:239], %[lw1] offset:13312\n\t" \
  "v_mul_f32 v95, v136, v95\n\t" \
  "v_cvt_pk_bf16_f32 v144, v88, v89\n\t" \
  "v_cvt_pk_bf16_f32 v145, v90, v91\n\t" \
  "v_cvt_pk_bf16_f32 v146, v92, v93\n\t" \
  "s_waitcnt lgkmcnt(5)\n\t" \
  "v_mfma_f32_32x32x16_bf16 v[192:207], a[32:35], v[216:219], v[192:207]\n\t" \
  "ds_read_b128 v[208:211], %[lw1] offset:14336\n\t" \
  "v_cvt_pk_bf16_f32 v147, v94, v95\n\t" \
  "buffer_store_dwordx4 v[144:147], %[l16], %[rs], %[sq1] offen sc1\n\t" \
  "v_mul_f32 v96, v119, v96\n\t" \
  "v_mul_f32 v97, v119, v97\n\t" \
  "s_waitcnt lgkmcnt(5)\n\t" \
  "v_mfma_f32_32x32x16_bf16 v[192:207], a[36:39], v[220:223], v[192:207]\n\t" \
  "ds_read_b128 v[212:215], %[lw1] offset:15360\n\t" \
  "v_mul_f32 v98, v119, v98\n\t" \
  "v_mul_f32 v99, v119, v99\n\t" \
  "v_mul_f32 v100, v119, v100\n\t" \
  "v_mul_f32 v101, v119, v101\n\t" \
  "s_waitcnt lgkmcnt(5)\n\t" \
  "v_mfma_f32_32x32x16_bf16 v[192:207], a[40:43], v[224:227], v[192:207]\n\t" \
  "ds_read_b128 v[216:219], %[lw1] offset:16384\n\t" \
  "v_mul_f32 v102, v119, v102\n\t" \
  "v_mul_f32 v103, v119, v103\n\t" \
  "v_cvt_pk_bf16_f32 v148, v96, v97\n\t" \
  "v_cvt_pk_bf16_f32 v149, v98, v99\n\t" \
  "s_waitcnt lgkmcnt(5)\n\t" \
  "v_mfma_f32_32x32x16_bf16 v[192:207], a[44:47], v[228:231], v[192:207]\n\t" \
  "ds_read_b128 v[220:223], %[lw1] offset:17408\n\t" \
  "v_cvt_pk_bf16_f32 v150, v100, v101\n\t" \
  "v_cvt_pk_bf16_f32 v151, v102, v103\n\t" \
  "buffer_store_dwordx4 v[148:151], %[l16], %[rs], %[sk] offen sc1\n\t" \
  "v_mul_f32 v172, v96, v96\n\t" \
  "s_waitcnt lgkmcnt(5)\n\t" \
  "v_mfma_f32_32x32x16_bf16 v[192:207], a[48:51], v[232:235], v[192:207]\n\t" \
  "ds_read_b128 v[224:227], %[lw1] offset:18432\n\t" \
  "v_fmac_f32 v172, v97, v97\n\t" \
  "v_fmac_f32 v172, v98, v98\n\t" \
  "v_fmac_f32 v172, v99, v99\n\t" \
  "v_fmac_f32 v172, v100, v100\n\t" \
  "s_waitcnt lgkmcnt(5)\n\t" \
  "v_mfma_f32_32x32x16_bf16 v[192:207], a[52:55], v[236:239], v[192:207]\n\t" \
  "ds_read_b128 v[228:231], %[lw1] offset:19456\n\t" \
  "v_fmac_f32 v172, v101, v101\n\t" \
  "v_fmac_f32 v172, v102, v102\n\t" \
  "v_fmac_f32 v172, v103, v103\n\t" \
  "v_mov_b32 v173, v172\n\t" \
  "s_waitcnt lgkmcnt(5)\n\t" \
  "v_mfma_f32_32x32x16_bf16 v[192:207], a[56:59], v[208:211], v[192:207]\n\t" \
  "ds_read_b128 v[232:235], %[lw1] offset:20480\n\t" \
  "v_permlane32_swap_b32 v172, v173\n\t" \
  "v_add_f32 v172, v172, v173\n\t" \
  "s_nop 1\n\t" \
  "v_max_f32_dpp v172, v172, v172 row_shr:1 row_mask:0xf bank_mask:0xf\n\t" \
  "s_nop 1\n\t" \
  "v_max_f32_dpp v172, v172, v172 row_shr:2 row_mask:0xf bank_mask:0xf\n\t" \
  "s_waitcnt lgkmcnt(5)\n\t" \
  "v_mfma_f32_32x32x16_bf16 v[192:207], a[60:63], v[212:215], v[192:207]\n\t" \
  "ds_read_b128 v[236:239], %[lw1] offset:21504\n\t" \
  "v_max_f32_dpp v172, v172, v172 row_shr:4 row_mask:0xf bank_mask:0xf\n\t" \
  "s_nop 1\n\t" \
  "v_max_f32_dpp v172, v172, v172 row_shr:8 row_mask:0xf bank_mask:0xf\n\t" \
  "s_nop 1\n\t" \
  "v_max_f32_dpp v172, v172, v172 row_bcast:15 row_mask:0xa bank_mask:0xf\n\t" \
  "buffer_store_dword v172, %[vkm], %[rs], %[skm] offen sc1\n\t" \
  "s_waitcnt lgkmcnt(5)\n\t" \
  "v_mfma_f32_32x32x16_bf16 v[192:207], a[64:67], v[216:219], v[192:207]\n\t" \
  "ds_read_b128 v[208:211], %[lw1] offset:22528\n\t" \
  "v_mul_f32 v104, v119, v104\n\t" \
  "v_mul_f32 v105, v119, v105\n\t" \
  "v_mul_f32 v106, v119, v106\n\t" \
  "v_mul_f32 v107, v119, v107\n\t" \
  "s_waitcnt lgkmcnt(5)\n\t" \
  "v_mfma_f32_32x32x16_bf16 v[192:207], a[68:71], v[220:223], v[192:207]\n\t" \
  "ds_read_b128 v[212:215], %[lw1] offset:23552\n\t" \
  "v_mul_f32 v108, v119, v108\n\t" \
  "v_mul_f32 v109, v119, v109\n\t" \
  "v_mul_f32 v110, v119, v110\n\t" \
  "v_mul_f32 v111, v119, v111\n\t" \
  "s_waitcnt lgkmcnt(5)\n\t" \
  "v_mfma_f32_32x32x16_bf16 v[192:207], a[72:75], v[224:227], v[192:207]\n\t" \
  "ds_read_b128 v[216:219], %[lw1] offset:24576\n\t" \
  "v_cvt_pk_bf16_f32 v152, v104, v105\n\t" \
  "v_cvt_pk_bf16_f32 v153, v106, v107\n\t" \
  "v_cvt_pk_bf16_f32 v154, v108, v109\n\t" \
  "v_cvt_pk_bf16_f32 v155, v110, v111\n\t" \
  "s_waitcnt lgkmcnt(5)\n\t" \
  "v_mfma_f32_32x32x16_bf16 v[192:207], a[76:79], v[228:231], v[192:207]\n\t" \
  "ds_read_b128 v[220:223], %[lw1] offset:25600\n\t" \
  "buffer_store_dwordx4 v[152:155], %[l16], %[rs], %[sk1] offen sc1\n\t" \
  "v_mul_f32 v174, v104, v104\n\t" \
  "v_fmac_f32 v174, v105, v105\n\t" \
  "v_fmac_f32 v174, v106, v106\n\t" \
  "s_waitcnt lgkmcnt(5)\n\t" \
  "v_mfma_f32_32x32x16_bf16 v[192:207], a[80:83], v[232:235], v[192:207]\n\t" \
  "ds_read_b128 v[224:227], %[lw1] offset:26624\n\t" \
  "v_fmac_f32 v174, v107, v107\n\t" \
  "v_fmac_f32 v174, v108, v108\n\t" \
  "v_fmac_f32 v174, v109, v109\n\t" \
  "v_fmac_f32 v174, v110, v110\n\t" \
  "s_waitcnt lgkmcnt(5)\n\t" \
  "v_mfma_f32_32x32x16_bf16 v[192:207], a[84:87], v[236:239], v[192:207]\n\t" \
  "ds_read_b128 v[228:231], %[lw1] offset:27648\n\t" \
  "v_fmac_f32 v174, v111, v111\n\t" \
  "v_mov_b32 v175, v174\n\t" \
  "s_nop 1\n\t" \
  "v_permlane32_swap_b32 v174, v175\n\t" \
  "v_add_f32 v174, v174, v175\n\t" \
  "s_waitcnt lgkmcnt(5)\n\t" \
  "v_mfma_f32_32x32x16_bf16 v[192:207], a[88:91], v[208:211], v[192:207]\n\t" \
  "ds_read_b128 v[232:235], %[lw1] offset:28672\n\t" \
  "v_max_f32_dpp v174, v174, v174 row_shr:1 row_mask:0xf bank_mask:0xf\n\t" \
  "s_nop 1\n\t" \
  "v_max_f32_dpp v174, v174, v174 row_shr:2 row_mask:0xf bank_mask:0xf\n\t" \
  "s_nop 1\n\t" \
  "v_max_f32_dpp v174, v174, v174 row_shr:4 row_mask:0xf bank_mask:0xf\n\t" \
  "s_nop 1\n\t" \
  "v_max_f32_dpp v174, v174, v174 row_shr:8 row_mask:0xf bank_mask:0xf\n\t" \
  "s_waitcnt lgkmcnt(5)\n\t" \
  "v_mfma_f32_32x32x16_bf16 v[192:207], a[92:95], v[212:215], v[192:207]\n\t" \
  "ds_read_b128 v[236:239], %[lw1] offset:29696\n\t" \
  "v_max_f32_dpp v174, v174, v174 row_bcast:15 row_mask:0xa bank_mask:0xf\n\t" \
  "buffer_store_dword v174, %[vkm], %[rs], %[skm] offen offset:256 sc1\n\t" \
  "s_waitcnt lgkmcnt(5)\n\t" \
  "v_mfma_f32_32x32x16_bf16 v[192:207], a[96:99], v[216:219], v[192:207]\n\t" \
  "ds_read_b128 v[208:211], %[lw1] offset:30720\n\t" \
  "s_waitcnt lgkmcnt(5)\n\t" \
  "v_mfma_f32_32x32x16_bf16 v[192:207], a[100:103], v[220:223], v[192:207]\n\t" \
  "ds_read_b128 v[212:215], %[lw1] offset:31744\n\t" \
  "s_waitcnt lgkmcnt(5)\n\t" \
  "v_mfma_f32_32x32x16_bf16 v[192:207], a[104:107], v[224:227], v[192:207]\n\t" \
  "ds_read_b128 v[216:219], %[lext] offset:2048\n\t" \
  "s_waitcnt lgkmcnt(5)\n\t" \
  "v_mfma_f32_32x32x16_bf16 v[192:207], a[108:111], v[228:231], v[192:207]\n\t" \
  "s_waitcnt lgkmcnt(4)\n\t" \
  "v_mfma_f32_32x32x16_bf16 v[192:207], a[112:115], v[232:235], v[192:207]\n\t" \
  "s_waitcnt lgkmcnt(3)\n\t" \
  "v_mfma_f32_32x32x16_bf16 v[192:207], a[116:119], v[236:239], v[192:207]\n\t" \
  "s_waitcnt lgkmcnt(2)\n\t" \
  "v_mfma_f32_32x32x16_bf16 v[192:207], a[120:123], v[208:211], v[192:207]\n\t" \
  "s_waitcnt lgkmcnt(1)\n\t" \
  "v_mfma_f32_32x32x16_bf16 v[192:207], a[124:127], v[212:215], v[192:207]\n\t" \
  "s_waitcnt lgkmcnt(0)\n\t" \
  "v_mfma_f32_32x32x16_bf16 v[192:207], v[112:115], v[216:219], v[192:207]\n\t" \
  "s_nop 7\n\t" \
  "s_nop 3\n\t" \
  "v_mul_f32 v192, v156, v192\n\t" \
  "v_mul_f32 v193, v157, v193\n\t" \
  "v_mul_f32 v194, v158, v194\n\t" \
  "v_mul_f32 v195, v159, v195\n\t" \
  "v_mul_f32 v196, v160, v196\n\t" \
  "v_mul_f32 v197, v161, v197\n\t" \
  "v_mul_f32 v198, v162, v198\n\t" \
  "v_mul_f32 v199, v163, v199\n\t" \
  "v_cvt_pk_bf16_f32 v140, v192, v193\n\t" \
  "v_cvt_pk_bf16_f32 v141, v194, v195\n\t" \
  "v_cvt_pk_bf16_f32 v142, v196, v197\n\t" \
  "v_cvt_pk_bf16_f32 v143, v198, v199\n\t" \
  "buffer_store_dwordx4 v[140:143], %[vv], %[rs], %[svv] offen sc1\n\t" \
  "v_mul_f32 v200, v164, v200\n\t" \
  "v_mul_f32 v201, v165, v201\n\t" \
  "v_mul_f32 v202, v166, v202\n\t" \
  "v_mul_f32 v203, v167, v203\n\t" \
  "v_mul_f32 v204, v168, v204\n\t" \
  "v_mul_f32 v205, v169, v205\n\t" \
  "v_mul_f32 v206, v170, v206\n\t" \
  "v_mul_f32 v207, v171, v207\n\t" \
  "v_cvt_pk_bf16_f32 v144, v200, v201\n\t" \
  "v_cvt_pk_bf16_f32 v145, v202, v203\n\t" \
  "v_cvt_pk_bf16_f32 v146, v204, v205\n\t" \
  "v_cvt_pk_bf16_f32 v147, v206, v207\n\t" \
  "buffer_store_dwordx4 v[144:147], %[vv], %[rs], %[svv] offen offset:512 sc1\n\t"
#define O_ASM \
  "s_nop 4\n\t" \
  "s_mov_b32 %[t0], %[sa]\n\t" \
  "s_mov_b32 %[t1], %[sb]\n\t" \
  "buffer_load_dwordx4 v[208:211], %[l16], %[rs], %[t0] offen sc1\n\t" \
  "buffer_load_dwordx4 v[212:215], %[l16], %[rs], %[t1] offen sc1\n\t" \
  "buffer_load_dwordx4 v[216:219], %[l16], %[rs], %[t0] offen offset:1024 sc1\n\t" \
  "buffer_load_dwordx4 v[220:223], %[l16], %[rs], %[t1] offen offset:1024 sc1\n\t" \
  "buffer_load_dwordx4 v[224:227], %[l16], %[rs], %[t0] offen offset:2048 sc1\n\t" \
  "buffer_load_dwordx4 v[228:231], %[l16], %[rs], %[t1] offen offset:2048 sc1\n\t" \
  "buffer_load_dwordx4 v[232:235], %[l16], %[rs], %[t0] offen offset:3072 sc1\n\t" \
  "buffer_load_dwordx4 v[236:239], %[l16], %[rs], %[t1] offen offset:3072 sc1\n\t" \
  "s_waitcnt vmcnt(6)\n\t" \
  "v_mfma_f32_32x32x16_bf16 v[80:95], a[128:131], v[212:215], 0\n\t" \
  "s_add_u32 %[t0], %[t0], 4096\n\t" \
  "s_add_u32 %[t1], %[t1], 4096\n\t" \
  "buffer_load_dwordx4 v[240:243], %[l16], %[rs], %[t0] offen sc1\n\t" \
  "v_mfma_f32_32x32x16_bf16 v[96:111], a[132:135], v[208:211], 0\n\t" \
  "v_mfma_f32_32x32x16_bf16 v[192:207], a[132:135], v[212:215], 0\n\t" \
  "buffer_load_dwordx4 v[244:247], %[l16], %[rs], %[t1] offen sc1\n\t" \
  "v_mfma_f32_32x32x16_bf16 v[64:79], a[128:131], v[208:211], v[64:79]\n\t" \
  "s_waitcnt vmcnt(6)\n\t" \
  "v_mfma_f32_32x32x16_bf16 v[80:95], a[136:139], v[220:223], v[80:95]\n\t" \
  "buffer_load_dwordx4 v[248:251], %[l16], %[rs], %[t0] offen offset:1024 sc1\n\t" \
  "v_mfma_f32_32x32x16_bf16 v[96:111], a[140:143], v[216:219], v[96:111]\n\t" \
  "v_mfma_f32_32x32x16_bf16 v[192:207], a[140:143], v[220:223], v[192:207]\n\t" \
  "buffer_load_dwordx4 v[252:255], %[l16], %[rs], %[t1] offen offset:1024 sc1\n\t" \
  "v_mfma_f32_32x32x16_bf16 v[64:79], a[136:139], v[216:219], v[64:79]\n\t" \
  "s_waitcnt vmcnt(6)\n\t" \
  "v_mfma_f32_32x32x16_bf16 v[80:95], a[144:147], v[228:231], v[80:95]\n\t" \
  "buffer_load_dwordx4 v[208:211], %[l16], %[rs], %[t0] offen offset:2048 sc1\n\t" \
  "v_mfma_f32_32x32x16_bf16 v[96:111], a[148:151], v[224:227], v[96:111]\n\t" \
  "v_mfma_f32_32x32x16_bf16 v[192:207], a[148:151], v[228:231], v[192:207]\n\t" \
  "buffer_load_dwordx4 v[212:215], %[l16], %[rs], %[t1] offen offset:2048 sc1\n\t" \
  "v_mfma_f32_32x32x16_bf16 v[64:79], a[144:147], v[224:227], v[64:79]\n\t" \
  "s_waitcnt vmcnt(6)\n\t" \
  "v_mfma_f32_32x32x16_bf16 v[80:95], a[152:155], v[236:239], v[80:95]\n\t" \
  "buffer_load_dwordx4 v[216:219], %[l16], %[rs], %[t0] offen offset:3072 sc1\n\t" \
  "v_mfma_f32_32x32x16_bf16 v[96:111], a[156:159], v[232:235], v[96:111]\n\t" \
  "v_mfma_f32_32x32x16_bf16 v[192:207], a[156:159], v[236:239], v[192:207]\n\t" \
  "buffer_load_dwordx4 v[220:223], %[l16], %[rs], %[t1] offen offset:3072 sc1\n\t" \
  "v_mfma_f32_32x32x16_bf16 v[64:79], a[152:155], v[232:235], v[64:79]\n\t" \
  "s_waitcnt vmcnt(6)\n\t" \
  "v_mfma_f32_32x32x16_bf16 v[80:95], a[160:163], v[244:247], v[80:95]\n\t" \
  "v_mfma_f32_32x32x16_bf16 v[96:111], a[164:167], v[240:243], v[96:111]\n\t" \
  "v_mfma_f32_32x32x16_bf16 v[192:207], a[164:167], v[244:247], v[192:207]\n\t" \
  "v_mfma_f32_32x32x16_bf16 v[64:79], a[160:163], v[240:243], v[64:79]\n\t" \
  "s_waitcnt vmcnt(4)\n\t" \
  "v_mfma_f32_32x32x16_bf16 v[80:95], a[168:171], v[252:255], v[80:95]\n\t" \
  "v_mfma_f32_32x32x16_bf16 v[96:111], a[172:175], v[248:251], v[96:111]\n\t" \
  "v_mfma_f32_32x32x16_bf16 v[192:207], a[172:175], v[252:255], v[192:207]\n\t" \
  "v_mfma_f32_32x32x16_bf16 v[64:79], a[168:171], v[248:251], v[64:79]\n\t" \
  "s_waitcnt vmcnt(2)\n\t" \
  "v_mfma_f32_32x32x16_bf16 v[80:95], a[176:179], v[212:215], v[80:95]\n\t" \
  "v_mfma_f32_32x32x16_bf16 v[96:111], a[180:183], v[208:211], v[96:111]\n\t" \
  "v_mfma_f32_32x32x16_bf16 v[192:207], a[180:183], v[212:215], v[192:207]\n\t" \
  "v_mfma_f32_32x32x16_bf16 v[64:79], a[176:179], v[208:211], v[64:79]\n\t" \
  "s_waitcnt vmcnt(0)\n\t" \
  "v_mfma_f32_32x32x16_bf16 v[80:95], a[184:187], v[220:223], v[80:95]\n\t" \
  "v_mfma_f32_32x32x16_bf16 v[96:111], a[188:191], v[216:219], v[96:111]\n\t" \
  "v_mfma_f32_32x32x16_bf16 v[192:207], a[188:191], v[220:223], v[192:207]\n\t" \
  "v_mfma_f32_32x32x16_bf16 v[64:79], a[184:187], v[216:219], v[64:79]\n\t" \
  "s_nop 7\n\t" \
  "s_nop 0\n\t" \
  "ds_write_b128 %[r1], v[80:83]\n\t" \
  "ds_write_b128 %[r1], v[84:87] offset:1024\n\t" \
  "ds_write_b128 %[r1], v[88:91] offset:2048\n\t" \
  "ds_write_b128 %[r1], v[92:95] offset:3072\n\t" \
  "ds_write_b128 %[r2], v[96:99]\n\t" \
  "ds_write_b128 %[r2], v[100:103] offset:1024\n\t" \
  "ds_write_b128 %[r2], v[104:107] offset:2048\n\t" \
  "ds_write_b128 %[r2], v[108:111] offset:3072\n\t" \
  "ds_write_b128 %[r3], v[192:195]\n\t" \
  "ds_write_b128 %[r3], v[196:199] offset:1024\n\t" \
  "ds_write_b128 %[r3], v[200:203] offset:2048\n\t" \
  "ds_write_b128 %[r3], v[204:207] offset:3072\n\t" \
  "s_waitcnt lgkmcnt(0)\n\t" \
  "s_barrier\n\t" \
  "ds_read_b128 v[80:83], %[rr] offset:0\n\t" \
  "ds_read_b128 v[84:87], %[rr] offset:1024\n\t" \
  "ds_read_b128 v[88:91], %[rr] offset:2048\n\t" \
  "ds_read_b128 v[92:95], %[rr] offset:3072\n\t" \
  "ds_read_b128 v[96:99], %[rr] offset:4096\n\t" \
  "ds_read_b128 v[100:103], %[rr] offset:5120\n\t" \
  "ds_read_b128 v[104:107], %[rr] offset:6144\n\t" \
  "ds_read_b128 v[108:111], %[rr] offset:7168\n\t" \
  "ds_read_b128 v[192:195], %[rr] offset:8192\n\t" \
  "ds_read_b128 v[196:199], %[rr] offset:9216\n\t" \
  "ds_read_b128 v[200:203], %[rr] offset:10240\n\t" \
  "ds_read_b128 v[204:207], %[rr] offset:11264\n\t" \
  "s_waitcnt lgkmcnt(11)\n\t" \
  "v_add_f32 v64, v64, v80\n\t" \
  "v_add_f32 v65, v65, v81\n\t" \
  "v_add_f32 v66, v66, v82\n\t" \
  "v_add_f32 v67, v67, v83\n\t" \
  "s_waitcnt lgkmcnt(10)\n\t" \
  "v_add_f32 v68, v68, v84\n\t" \
  "v_add_f32 v69, v69, v85\n\t" \
  "v_add_f32 v70, v70, v86\n\t" \
  "v_add_f32 v71, v71, v87\n\t" \
  "s_waitcnt lgkmcnt(9)\n\t" \
  "v_add_f32 v72, v72, v88\n\t" \
  "v_add_f32 v73, v73, v89\n\t" \
  "v_add_f32 v74, v74, v90\n\t" \
  "v_add_f32 v75, v75, v91\n\t" \
  "s_waitcnt lgkmcnt(8)\n\t" \
  "v_add_f32 v76, v76, v92\n\t" \
  "v_add_f32 v77, v77, v93\n\t" \
  "v_add_f32 v78, v78, v94\n\t" \
  "v_add_f32 v79, v79, v95\n\t" \
  "s_waitcnt lgkmcnt(7)\n\t" \
  "v_add_f32 v64, v64, v96\n\t" \
  "v_add_f32 v65, v65, v97\n\t" \
  "v_add_f32 v66, v66, v98\n\t" \
  "v_add_f32 v67, v67, v99\n\t" \
  "s_waitcnt lgkmcnt(6)\n\t" \
  "v_add_f32 v68, v68, v100\n\t" \
  "v_add_f32 v69, v69, v101\n\t" \
  "v_add_f32 v70, v70, v102\n\t" \
  "v_add_f32 v71, v71, v103\n\t" \
  "s_waitcnt lgkmcnt(5)\n\t" \
  "v_add_f32 v72, v72, v104\n\t" \
  "v_add_f32 v73, v73, v105\n\t" \
  "v_add_f32 v74, v74, v106\n\t" \
  "v_add_f32 v75, v75, v107\n\t" \
  "s_waitcnt lgkmcnt(4)\n\t" \
  "v_add_f32 v76, v76, v108\n\t" \
  "v_add_f32 v77, v77, v109\n\t" \
  "v_add_f32 v78, v78, v110\n\t" \
  "v_add_f32 v79, v79, v111\n\t" \
  "s_waitcnt lgkmcnt(3)\n\t" \
  "v_add_f32 v64, v64, v192\n\t" \
  "v_add_f32 v65, v65, v193\n\t" \
  "v_add_f32 v66, v66, v194\n\t" \
  "v_add_f32 v67, v67, v195\n\t" \
  "s_waitcnt lgkmcnt(2)\n\t" \
  "v_add_f32 v68, v68, v196\n\t" \
  "v_add_f32 v69, v69, v197\n\t" \
  "v_add_f32 v70, v70, v198\n\t" \
  "v_add_f32 v71, v71, v199\n\t" \
  "s_waitcnt lgkmcnt(1)\n\t" \
  "v_add_f32 v72, v72, v200\n\t" \
  "v_add_f32 v73, v73, v201\n\t" \
  "v_add_f32 v74, v74, v202\n\t" \
  "v_add_f32 v75, v75, v203\n\t" \
  "s_waitcnt lgkmcnt(0)\n\t" \
  "v_add_f32 v76, v76, v204\n\t" \
  "v_add_f32 v77, v77, v205\n\t" \
  "v_add_f32 v78, v78, v206\n\t" \
  "v_add_f32 v79, v79, v207\n\t" \
  "v_cvt_pk_bf16_f32 v224, v64, v65\n\t" \
  "v_cvt_pk_bf16_f32 v225, v66, v67\n\t" \
  "v_cvt_pk_bf16_f32 v226, v68, v69\n\t" \
  "v_cvt_pk_bf16_f32 v227, v70, v71\n\t" \
  "buffer_store_dwordx4 v[224:227], %[l16], %[rs], %[sx] offen sc1\n\t" \
  "v_cvt_pk_bf16_f32 v228, v72, v73\n\t" \
  "v_cvt_pk_bf16_f32 v229, v74, v75\n\t" \
  "v_cvt_pk_bf16_f32 v230, v76, v77\n\t" \
  "v_cvt_pk_bf16_f32 v231, v78, v79\n\t" \
  "buffer_store_dwordx4 v[228:231], %[l16], %[rs], %[sx] offen offset:1024 sc1\n\t" \
  "v_add_f32 v232, v64, v65\n\t" \
  "v_add_f32 v233, v66, v67\n\t" \
  "v_add_f32 v232, v232, v68\n\t" \
  "v_add_f32 v233, v233, v69\n\t" \
  "v_add_f32 v232, v232, v70\n\t" \
  "v_add_f32 v233, v233, v71\n\t" \
  "v_add_f32 v232, v232, v72\n\t" \
  "v_add_f32 v233, v233, v73\n\t" \
  "v_add_f32 v232, v232, v74\n\t" \
  "v_add_f32 v233, v233, v75\n\t" \
  "v_add_f32 v232, v232, v76\n\t" \
  "v_add_f32 v233, v233, v77\n\t" \
  "v_add_f32 v232, v232, v78\n\t" \
  "v_add_f32 v233, v233, v79\n\t" \
  "v_add_f32 v232, v232, v233\n\t" \
  "v_mov_b32 v233, v232\n\t" \
  "s_nop 1\n\t" \
  "v_permlane32_swap_b32 v232, v233\n\t" \
  "v_add_f32 v232, v232, v233\n\t" \
  "v_mul_f32 v234, 0x3d000000, v232\n\t" \
  "v_sub_f32 v238, v64, v234\n\t" \
  "v_sub_f32 v239, v65, v234\n\t" \
  "v_fma_f32 v235, v238, v238, 0\n\t" \
  "v_fma_f32 v236, v239, v239, 0\n\t" \
  "v_sub_f32 v238, v66, v234\n\t" \
  "v_sub_f32 v239, v67, v234\n\t" \
  "v_fma_f32 v235, v238, v238, v235\n\t" \
  "v_fma_f32 v236, v239, v239, v236\n\t" \
  "v_sub_f32 v238, v68, v234\n\t" \
  "v_sub_f32 v239, v69, v234\n\t" \
  "v_fma_f32 v235, v238, v238, v235\n\t" \
  "v_fma_f32 v236, v239, v239, v236\n\t" \
  "v_sub_f32 v238, v70, v234\n\t" \
  "v_sub_f32 v239, v71, v234\n\t" \
  "v_fma_f32 v235, v238, v238, v235\n\t" \
  "v_fma_f32 v236, v239, v239, v236\n\t" \
  "v_sub_f32 v238, v72, v234\n\t" \
  "v_sub_f32 v239, v73, v234\n\t" \
  "v_fma_f32 v235, v238, v238, v235\n\t" \
  "v_fma_f32 v236, v239, v239, v236\n\t" \
  "v_sub_f32 v238, v74, v234\n\t" \
  "v_sub_f32 v239, v75, v234\n\t" \
  "v_fma_f32 v235, v238, v238, v235\n\t" \
  "v_fma_f32 v236, v239, v239, v236\n\t" \
  "v_sub_f32 v238, v76, v234\n\t" \
  "v_sub_f32 v239, v77, v234\n\t" \
  "v_fma_f32 v235, v238, v238, v235\n\t" \
  "v_fma_f32 v236, v239, v239, v236\n\t" \
  "v_sub_f32 v238, v78, v234\n\t" \
  "v_sub_f32 v239, v79, v234\n\t" \
  "v_fma_f32 v235, v238, v238, v235\n\t" \
  "v_fma_f32 v236, v239, v239, v236\n\t" \
  "v_add_f32 v235, v235, v236\n\t" \
  "v_mov_b32 v236, v235\n\t" \
  "s_nop 1\n\t" \
  "v_permlane32_swap_b32 v235, v236\n\t" \
  "v_add_f32 v235, v235, v236\n\t" \
  "buffer_store_dwordx2 v[234:235], %[vst], %[rs], %[sxs] offen sc1\n\t"
#define W1DUMP_ASM \
  "ds_write_b128 %[lw], a[0:3]\n\t" \
  "ds_write_b128 %[lw], a[4:7] offset:1024\n\t" \
  "ds_write_b128 %[lw], a[8:11] offset:2048\n\t" \
  "ds_write_b128 %[lw], a[12:15] offset:3072\n\t" \
  "ds_write_b128 %[lw], a[16:19] offset:4096\n\t" \
  "ds_write_b128 %[lw], a[20:23] offset:5120\n\t" \
  "ds_write_b128 %[lw], a[24:27] offset:6144\n\t" \
  "ds_write_b128 %[lw], a[28:31] offset:7168\n\t" \
  "ds_write_b128 %[lw], a[32:35] offset:8192\n\t" \
  "ds_write_b128 %[lw], a[36:39] offset:9216\n\t" \
  "ds_write_b128 %[lw], a[40:43] offset:10240\n\t" \
  "ds_write_b128 %[lw], a[44:47] offset:11264\n\t" \
  "ds_write_b128 %[lw], a[48:51] offset:12288\n\t" \
  "ds_write_b128 %[lw], a[52:55] offset:13312\n\t" \
  "ds_write_b128 %[lw], a[56:59] offset:14336\n\t" \
  "ds_write_b128 %[lw], a[60:63] offset:15360\n\t" \
  "ds_write_b128 %[lw], a[64:67] offset:16384\n\t" \
  "ds_write_b128 %[lw], a[68:71] offset:17408\n\t" \
  "ds_write_b128 %[lw], a[72:75] offset:18432\n\t" \
  "ds_write_b128 %[lw], a[76:79] offset:19456\n\t" \
  "ds_write_b128 %[lw], a[80:83] offset:20480\n\t" \
  "ds_write_b128 %[lw], a[84:87] offset:21504\n\t" \
  "ds_write_b128 %[lw], a[88:91] offset:22528\n\t" \
  "ds_write_b128 %[lw], a[92:95] offset:23552\n\t" \
  "ds_write_b128 %[lw], a[96:99] offset:24576\n\t" \
  "ds_write_b128 %[lw], a[100:103] offset:25600\n\t" \
  "ds_write_b128 %[lw], a[104:107] offset:26624\n\t" \
  "ds_write_b128 %[lw], a[108:111] offset:27648\n\t" \
  "ds_write_b128 %[lw], a[112:115] offset:28672\n\t" \
  "ds_write_b128 %[lw], a[116:119] offset:29696\n\t" \
  "ds_write_b128 %[lw], a[120:123] offset:30720\n\t" \
  "ds_write_b128 %[lw], a[124:127] offset:31744\n\t" \
  "s_waitcnt lgkmcnt(0)\n\t"
#define F1_ASM \
  "s_nop 4\n\t" \
  "s_add_u32 %[sh1], %[sh0], 4096\n\t" \
  "buffer_load_dwordx2 v[208:209], %[sv], %[rs], %[sst] offen sc1\n\t" \
  "buffer_load_dwordx2 v[210:211], %[sv], %[rs], %[sst] offen offset:256 sc1\n\t" \
  "buffer_load_dwordx2 v[212:213], %[sv], %[rs], %[sst] offen offset:512 sc1\n\t" \
  "buffer_load_dwordx2 v[214:215], %[sv], %[rs], %[sst] offen offset:768 sc1\n\t" \
  "buffer_load_dwordx2 v[216:217], %[sv], %[rs], %[sst] offen offset:1024 sc1\n\t" \
  "buffer_load_dwordx2 v[218:219], %[sv], %[rs], %[sst] offen offset:1280 sc1\n\t" \
  "buffer_load_dwordx2 v[220:221], %[sv], %[rs], %[sst] offen offset:1536 sc1\n\t" \
  "buffer_load_dwordx2 v[222:223], %[sv], %[rs], %[sst] offen offset:1792 sc1\n\t" \
  "buffer_load_dwordx2 v[192:193], %[sv], %[rs], %[sst2] offen sc1\n\t" \
  "buffer_load_dwordx2 v[194:195], %[sv], %[rs], %[sst2] offen offset:256 sc1\n\t" \
  "buffer_load_dwordx2 v[196:197], %[sv], %[rs], %[sst2] offen offset:512 sc1\n\t" \
  "buffer_load_dwordx2 v[198:199], %[sv], %[rs], %[sst2] offen offset:768 sc1\n\t" \
  "buffer_load_dwordx2 v[200:201], %[sv], %[rs], %[sst2] offen offset:1024 sc1\n\t" \
  "buffer_load_dwordx2 v[202:203], %[sv], %[rs], %[sst2] offen offset:1280 sc1\n\t" \
  "buffer_load_dwordx2 v[204:205], %[sv], %[rs], %[sst2] offen offset:1536 sc1\n\t" \
  "buffer_load_dwordx2 v[206:207], %[sv], %[rs], %[sst2] offen offset:1792 sc1\n\t" \
  "s_mov_b32 %[t0], %[sact]\n\t" \
  "buffer_load_dwordx4 a[0:3], %[l16], %[rs], %[t0] offen sc1\n\t" \
  "buffer_load_dwordx4 a[4:7], %[l16], %[rs], %[t0] offen offset:1024 sc1\n\t" \
  "buffer_load_dwordx4 a[8:11], %[l16], %[rs], %[t0] offen offset:2048 sc1\n\t" \
  "buffer_load_dwordx4 a[12:15], %[l16], %[rs], %[t0] offen offset:3072 sc1\n\t" \
  "s_add_u32 %[t0], %[t0], 4096\n\t" \
  "buffer_load_dwordx4 a[16:19], %[l16], %[rs], %[t0] offen sc1\n\t" \
  "buffer_load_dwordx4 a[20:23], %[l16], %[rs], %[t0] offen offset:1024 sc1\n\t" \
  "buffer_load_dwordx4 a[24:27], %[l16], %[rs], %[t0] offen offset:2048 sc1\n\t" \
  "buffer_load_dwordx4 a[28:31], %[l16], %[rs], %[t0] offen offset:3072 sc1\n\t" \
  "s_add_u32 %[t0], %[t0], 4096\n\t" \
  "buffer_load_dwordx4 a[32:35], %[l16], %[rs], %[t0] offen sc1\n\t" \
  "buffer_load_dwordx4 a[36:39], %[l16], %[rs], %[t0] offen offset:1024 sc1\n\t" \
  "ds_read_b128 v[224:227], %[lw0]\n\t" \
  "ds_read_b128 v[228:231], %[lw0] offset:32768\n\t" \
  "ds_read_b128 v[232:235], %[lw0] offset:1024\n\t" \
  "ds_read_b128 v[236:239], %[lw0] offset:33792\n\t" \
  "ds_read_b128 v[240:243], %[lw0] offset:2048\n\t" \
  "ds_read_b128 v[244:247], %[lw0] offset:34816\n\t" \
  "s_waitcnt vmcnt(9) lgkmcnt(5)\n\t" \
  "v_mfma_f32_32x32x16_bf16 v[80:95], v[224:227], a[0:3], 0\n\t" \
  "ds_read_b128 v[248:251], %[lw0] offset:3072\n\t" \
  "buffer_load_dwordx4 a[40:43], %[l16], %[rs], %[t0] offen offset:2048 sc1\n\t" \
  "s_waitcnt lgkmcnt(5)\n\t" \
  "v_mfma_f32_32x32x16_bf16 v[96:111], v[228:231], a[0:3], 0\n\t" \
  "ds_read_b128 v[252:255], %[lw0] offset:35840\n\t" \
  "s_waitcnt vmcnt(9) lgkmcnt(5)\n\t" \
  "v_mfma_f32_32x32x16_bf16 v[80:95], v[232:235], a[4:7], v[80:95]\n\t" \
  "ds_read_b128 v[224:227], %[lw0] offset:4096\n\t" \
  "buffer_load_dwordx4 a[44:47], %[l16], %[rs], %[t0] offen offset:3072 sc1\n\t" \
  "s_waitcnt lgkmcnt(5)\n\t" \
  "v_mfma_f32_32x32x16_bf16 v[96:111], v[236:239], a[4:7], v[96:111]\n\t" \
  "ds_read_b128 v[228:231], %[lw0] offset:36864\n\t" \
  "s_waitcnt vmcnt(9) lgkmcnt(5)\n\t" \
  "v_mfma_f32_32x32x16_bf16 v[80:95], v[240:243], a[8:11], v[80:95]\n\t" \
  "ds_read_b128 v[232:235], %[lw0] offset:5120\n\t" \
  "s_add_u32 %[t0], %[t0], 4096\n\t" \
  "buffer_load_dwordx4 a[48:51], %[l16], %[rs], %[t0] offen sc1\n\t" \
  "v_add_f32 v118, v208, v210\n\t" \
  "s_waitcnt lgkmcnt(5)\n\t" \
  "v_mfma_f32_32x32x16_bf16 v[96:111], v[244:247], a[8:11], v[96:111]\n\t" \
  "ds_read_b128 v[236:239], %[lw0] offset:37888\n\t" \
  "v_add_f32 v118, v118, v212\n\t" \
  "v_add_f32 v118, v118, v214\n\t" \
  "s_waitcnt vmcnt(9) lgkmcnt(5)\n\t" \
  "v_mfma_f32_32x32x16_bf16 v[80:95], v[248:251], a[12:15], v[80:95]\n\t" \
  "ds_read_b128 v[240:243], %[lw0] offset:6144\n\t" \
  "buffer_load_dwordx4 a[52:55], %[l16], %[rs], %[t0] offen offset:1024 sc1\n\t" \
  "v_add_f32 v118, v118, v216\n\t" \
  "v_add_f32 v118, v118, v218\n\t" \
  "s_waitcnt lgkmcnt(5)\n\t" \
  "v_mfma_f32_32x32x16_bf16 v[96:111], v[252:255], a[12:15], v[96:111]\n\t" \
  "ds_read_b128 v[244:247], %[lw0] offset:38912\n\t" \
  "v_add_f32 v118, v118, v220\n\t" \
  "v_add_f32 v118, v118, v222\n\t" \
  "s_waitcnt vmcnt(9) lgkmcnt(5)\n\t" \
  "v_mfma_f32_32x32x16_bf16 v[80:95], v[224:227], a[16:19], v[80:95]\n\t" \
  "ds_read_b128 v[248:251], %[lw0] offset:7168\n\t" \
  "buffer_load_dwordx4 a[56:59], %[l16], %[rs], %[t0] offen offset:2048 sc1\n\t" \
  "v_mov_b32 v116, v118\n\t" \
  "s_nop 1\n\t" \
  "v_permlane32_swap_b32 v118, v116\n\t" \
  "s_waitcnt lgkmcnt(5)\n\t" \
  "v_mfma_f32_32x32x16_bf16 v[96:111], v[228:231], a[16:19], v[96:111]\n\t" \
  "ds_read_b128 v[252:255], %[lw0] offset:39936\n\t" \
  "v_add_f32 v118, v118, v116\n\t" \
  "v_mul_f32 v118, 0x3d800000, v118\n\t" \
  "s_waitcnt vmcnt(9) lgkmcnt(5)\n\t" \
  "v_mfma_f32_32x32x16_bf16 v[80:95], v[232:235], a[20:23], v[80:95]\n\t" \
  "ds_read_b128 v[224:227], %[lw0] offset:8192\n\t" \
  "buffer_load_dwordx4 a[60:63], %[l16], %[rs], %[t0] offen offset:3072 sc1\n\t" \
  "v_mul_f32 v116, v118, %[hm]\n\t" \
  "v_cvt_pk_bf16_f32 v117, v116, v116\n\t" \
  "s_waitcnt lgkmcnt(5)\n\t" \
  "v_mfma_f32_32x32x16_bf16 v[96:111], v[236:239], a[20:23], v[96:111]\n\t" \
  "ds_read_b128 v[228:231], %[lw0] offset:40960\n\t" \
  "v_mov_b32 v114, 0\n\t" \
  "v_lshlrev_b32 v117, 16, v117\n\t" \
  "s_waitcnt vmcnt(9) lgkmcnt(5)\n\t" \
  "v_mfma_f32_32x32x16_bf16 v[80:95], v[240:243], a[24:27], v[80:95]\n\t" \
  "ds_read_b128 v[232:235], %[lw0] offset:9216\n\t" \
  "s_add_u32 %[t0], %[t0], 4096\n\t" \
  "buffer_load_dwordx4 a[64:67], %[l16], %[rs], %[t0] offen sc1\n\t" \
  "v_mov_b32 v115, 0\n\t" \
  "v_sub_f32 v117, v116, v117\n\t" \
  "s_waitcnt lgkmcnt(5)\n\t" \
  "v_mfma_f32_32x32x16_bf16 v[96:111], v[244:247], a[24:27], v[96:111]\n\t" \
  "ds_read_b128 v[236:239], %[lw0] offset:41984\n\t" \
  "v_cvt_pk_bf16_f32 v113, v116, v114\n\t" \
  "v_cvt_pk_bf16_f32 v112, v116, v117\n\t" \
  "s_waitcnt vmcnt(9) lgkmcnt(5)\n\t" \
  "v_mfma_f32_32x32x16_bf16 v[80:95], v[248:251], a[28:31], v[80:95]\n\t" \
  "ds_read_b128 v[240:243], %[lw0] offset:10240\n\t" \
  "buffer_load_dwordx4 a[68:71], %[l16], %[rs], %[t0] offen offset:1024 sc1\n\t" \
  "v_add_f32 v118, v192, v194\n\t" \
  "s_waitcnt lgkmcnt(5)\n\t" \
  "v_mfma_f32_32x32x16_bf16 v[96:111], v[252:255], a[28:31], v[96:111]\n\t" \
  "ds_read_b128 v[244:247], %[lw0] offset:43008\n\t" \
  "v_add_f32 v118, v118, v196\n\t" \
  "v_add_f32 v118, v118, v198\n\t" \
  "s_waitcnt vmcnt(9) lgkmcnt(5)\n\t" \
  "v_mfma_f32_32x32x16_bf16 v[80:95], v[224:227], a[32:35], v[80:95]\n\t" \
  "ds_read_b128 v[248:251], %[lw0] offset:11264\n\t" \
  "buffer_load_dwordx4 a[72:75], %[l16], %[rs], %[t0] offen offset:2048 sc1\n\t" \
  "v_add_f32 v118, v118, v200\n\t" \
  "v_add_f32 v118, v118, v202\n\t" \
  "s_waitcnt lgkmcnt(5)\n\t" \
  "v_mfma_f32_32x32x16_bf16 v[96:111], v[228:231], a[32:35], v[96:111]\n\t" \
  "ds_read_b128 v[252:255], %[lw0] offset:44032\n\t" \
  "v_add_f32 v118, v118, v204\n\t" \
  "v_add_f32 v118, v118, v206\n\t" \
  "s_waitcnt vmcnt(9) lgkmcnt(5)\n\t" \
  "v_mfma_f32_32x32x16_bf16 v[80:95], v[232:235], a[36:39], v[80:95]\n\t" \
  "ds_read_b128 v[224:227], %[lw0] offset:12288\n\t" \
  "buffer_load_dwordx4 a[76:79], %[l16], %[rs], %[t0] offen offset:3072 sc1\n\t" \
  "v_mov_b32 v116, v118\n\t" \
  "s_nop 1\n\t" \
  "v_permlane32_swap_b32 v118, v116\n\t" \
  "s_waitcnt lgkmcnt(5)\n\t" \
  "v_mfma_f32_32x32x16_bf16 v[96:111], v[236:239], a[36:39], v[96:111]\n\t" \
  "ds_read_b128 v[228:231], %[lw0] offset:45056\n\t" \
  "v_add_f32 v118, v118, v116\n\t" \
  "v_mul_f32 v118, 0x3d800000, v118\n\t" \
  "s_waitcnt vmcnt(9) lgkmcnt(5)\n\t" \
  "v_mfma_f32_32x32x16_bf16 v[80:95], v[240:243], a[40:43], v[80:95]\n\t" \
  "ds_read_b128 v[232:235], %[lw0] offset:13312\n\t" \
  "s_add_u32 %[t0], %[t0], 4096\n\t" \
  "buffer_load_dwordx4 a[80:83], %[l16], %[rs], %[t0] offen sc1\n\t" \
  "v_add_f32 v208, v193, v195\n\t" \
  "v_add_f32 v208, v208, v197\n\t" \
  "s_waitcnt lgkmcnt(5)\n\t" \
  "v_mfma_f32_32x32x16_bf16 v[96:111], v[244:247], a[40:43], v[96:111]\n\t" \
  "ds_read_b128 v[236:239], %[lw0] offset:46080\n\t" \
  "v_add_f32 v208, v208, v199\n\t" \
  "v_add_f32 v208, v208, v201\n\t" \
  "s_waitcnt vmcnt(9) lgkmcnt(5)\n\t" \
  "v_mfma_f32_32x32x16_bf16 v[80:95], v[248:251], a[44:47], v[80:95]\n\t" \
  "ds_read_b128 v[240:243], %[lw0] offset:14336\n\t" \
  "buffer_load_dwordx4 a[84:87], %[l16], %[rs], %[t0] offen offset:1024 sc1\n\t" \
  "v_add_f32 v208, v208, v203\n\t" \
  "v_add_f32 v208, v208, v205\n\t" \
  "s_waitcnt lgkmcnt(5)\n\t" \
  "v_mfma_f32_32x32x16_bf16 v[96:111], v[252:255], a[44:47], v[96:111]\n\t" \
  "ds_read_b128 v[244:247], %[lw0] offset:47104\n\t" \
  "v_add_f32 v208, v208, v207\n\t" \
  "v_sub_f32 v116, v192, v118\n\t" \
  "s_waitcnt vmcnt(9) lgkmcnt(5)\n\t" \
  "v_mfma_f32_32x32x16_bf16 v[80:95], v[224:227], a[48:51], v[80:95]\n\t" \
  "ds_read_b128 v[248:251], %[lw0] offset:15360\n\t" \
  "buffer_load_dwordx4 a[88:91], %[l16], %[rs], %[t0] offen offset:2048 sc1\n\t" \
  "v_fma_f32 v209, v116, v116, 0\n\t" \
  "v_sub_f32 v116, v194, v118\n\t" \
  "s_waitcnt lgkmcnt(5)\n\t" \
  "v_mfma_f32_32x32x16_bf16 v[96:111], v[228:231], a[48:51], v[96:111]\n\t" \
  "ds_read_b128 v[252:255], %[lw0] offset:48128\n\t" \
  "v_fma_f32 v209, v116, v116, v209\n\t" \
  "v_sub_f32 v116, v196, v118\n\t" \
  "s_waitcnt vmcnt(9) lgkmcnt(5)\n\t" \
  "v_mfma_f32_32x32x16_bf16 v[80:95], v[232:235], a[52:55], v[80:95]\n\t" \
  "ds_read_b128 v[224:227], %[lw0] offset:16384\n\t" \
  "buffer_load_dwordx4 a[92:95], %[l16], %[rs], %[t0] offen offset:3072 sc1\n\t" \
  "v_fma_f32 v209, v116, v116, v209\n\t" \
  "v_sub_f32 v116, v198, v118\n\t" \
  "s_waitcnt lgkmcnt(5)\n\t" \
  "v_mfma_f32_32x32x16_bf16 v[96:111], v[236:239], a[52:55], v[96:111]\n\t" \
  "ds_read_b128 v[228:231], %[lw0] offset:49152\n\t" \
  "v_fma_f32 v209, v116, v116, v209\n\t" \
  "v_sub_f32 v116, v200, v118\n\t" \
  "s_waitcnt vmcnt(9) lgkmcnt(5)\n\t" \
  "v_mfma_f32_32x32x16_bf16 v[80:95], v[240:243], a[56:59], v[80:95]\n\t" \
  "ds_read_b128 v[232:235], %[lw0] offset:17408\n\t" \
  "s_add_u32 %[t0], %[t0], 4096\n\t" \
  "buffer_load_dwordx4 a[96:99], %[l16], %[rs], %[t0] offen sc1\n\t" \
  "v_fma_f32 v209, v116, v116, v209\n\t" \
  "v_sub_f32 v116, v202, v118\n\t" \
  "s_waitcnt lgkmcnt(5)\n\t" \
  "v_mfma_f32_32x32x16_bf16 v[96:111], v[244:247], a[56:59], v[96:111]\n\t" \
  "ds_read_b128 v[236:239], %[lw0] offset:50176\n\t" \
  "v_fma_f32 v209, v116, v116, v209\n\t" \
  "v_sub_f32 v116, v204, v118\n\t" \
  "s_waitcnt vmcnt(9) lgkmcnt(5)\n\t" \
  "v_mfma_f32_32x32x16_bf16 v[80:95], v[248:251], a[60:63], v[80:95]\n\t" \
  "ds_read_b128 v[240:243], %[lw0] offset:18432\n\t" \
  "buffer_load_dwordx4 a[100:103], %[l16], %[rs], %[t0] offen offset:1024 sc1\n\t" \
  "v_fma_f32 v209, v116, v116, v209\n\t" \
  "v_sub_f32 v116, v206, v118\n\t" \
  "s_waitcnt lgkmcnt(5)\n\t" \
  "v_mfma_f32_32x32x16_bf16 v[96:111], v[252:255], a[60:63], v[96:111]\n\t" \
  "ds_read_b128 v[244:247], %[lw0] offset:51200\n\t" \
  "v_fma_f32 v209, v116, v116, v209\n\t" \
  "v_fmac_f32 v208, 0x42000000, v209\n\t" \
  "s_waitcnt vmcnt(9) lgkmcnt(5)\n\t" \
  "v_mfma_f32_32x32x16_bf16 v[80:95], v[224:227], a[64:67], v[80:95]\n\t" \
  "ds_read_b128 v[248:251], %[lw0] offset:19456\n\t" \
  "buffer_load_dwordx4 a[104:107], %[l16], %[rs], %[t0] offen offset:2048 sc1\n\t" \
  "v_mov_b32 v116, v208\n\t" \
  "s_nop 1\n\t" \
  "v_permlane32_swap_b32 v208, v116\n\t" \
  "s_waitcnt lgkmcnt(5)\n\t" \
  "v_mfma_f32_32x32x16_bf16 v[96:111], v[228:231], a[64:67], v[96:111]\n\t" \
  "ds_read_b128 v[252:255], %[lw0] offset:52224\n\t" \
  "v_add_f32 v208, v208, v116\n\t" \
  "v_mov_b32 v116, 0x3727c5ac\n\t" \
  "s_waitcnt vmcnt(9) lgkmcnt(5)\n\t" \
  "v_mfma_f32_32x32x16_bf16 v[80:95], v[232:235], a[68:71], v[80:95]\n\t" \
  "ds_read_b128 v[224:227], %[lw0] offset:20480\n\t" \
  "buffer_load_dwordx4 a[108:111], %[l16], %[rs], %[t0] offen offset:3072 sc1\n\t" \
  "v_fmac_f32 v116, 0x3b000000, v208\n\t" \
  "v_rsq_f32 v119, v116\n\t" \
  "s_waitcnt lgkmcnt(5)\n\t" \
  "v_mfma_f32_32x32x16_bf16 v[96:111], v[236:239], a[68:71], v[96:111]\n\t" \
  "ds_read_b128 v[228:231], %[lw0] offset:53248\n\t" \
  "s_nop 0\n\t" \
  "s_waitcnt vmcnt(9) lgkmcnt(5)\n\t" \
  "v_mfma_f32_32x32x16_bf16 v[80:95], v[240:243], a[72:75], v[80:95]\n\t" \
  "ds_read_b128 v[232:235], %[lw0] offset:21504\n\t" \
  "s_add_u32 %[t0], %[t0], 4096\n\t" \
  "buffer_load_dwordx4 a[112:115], %[l16], %[rs], %[t0] offen sc1\n\t" \
  "s_waitcnt lgkmcnt(5)\n\t" \
  "v_mfma_f32_32x32x16_bf16 v[96:111], v[244:247], a[72:75], v[96:111]\n\t" \
  "ds_read_b128 v[236:239], %[lw0] offset:54272\n\t" \
  "s_waitcnt vmcnt(9) lgkmcnt(5)\n\t" \
  "v_mfma_f32_32x32x16_bf16 v[80:95], v[248:251], a[76:79], v[80:95]\n\t" \
  "ds_read_b128 v[240:243], %[lw0] offset:22528\n\t" \
  "buffer_load_dwordx4 a[116:119], %[l16], %[rs], %[t0] offen offset:1024 sc1\n\t" \
  "s_waitcnt lgkmcnt(5)\n\t" \
  "v_mfma_f32_32x32x16_bf16 v[96:111], v[252:255], a[76:79], v[96:111]\n\t" \
  "ds_read_b128 v[244:247], %[lw0] offset:55296\n\t" \
  "s_waitcnt vmcnt(9) lgkmcnt(5)\n\t" \
  "v_mfma_f32_32x32x16_bf16 v[80:95], v[224:227], a[80:83], v[80:95]\n\t" \
  "ds_read_b128 v[248:251], %[lw0] offset:23552\n\t" \
  "buffer_load_dwordx4 a[120:123], %[l16], %[rs], %[t0] offen offset:2048 sc1\n\t" \
  "s_waitcnt lgkmcnt(5)\n\t" \
  "v_mfma_f32_32x32x16_bf16 v[96:111], v[228:231], a[80:83], v[96:111]\n\t" \
  "ds_read_b128 v[252:255], %[lw0] offset:56320\n\t" \
  "s_waitcnt vmcnt(9) lgkmcnt(5)\n\t" \
  "v_mfma_f32_32x32x16_bf16 v[80:95], v[232:235], a[84:87], v[80:95]\n\t" \
  "ds_read_b128 v[224:227], %[lw0] offset:24576\n\t" \
  "buffer_load_dwordx4 a[124:127], %[l16], %[rs], %[t0] offen offset:3072 sc1\n\t" \
  "s_waitcnt lgkmcnt(5)\n\t" \
  "v_mfma_f32_32x32x16_bf16 v[96:111], v[236:239], a[84:87], v[96:111]\n\t" \
  "ds_read_b128 v[228:231], %[lw0] offset:57344\n\t" \
  "s_waitcnt vmcnt(9) lgkmcnt(5)\n\t" \
  "v_mfma_f32_32x32x16_bf16 v[80:95], v[240:243], a[88:91], v[80:95]\n\t" \
  "ds_read_b128 v[232:235], %[lw0] offset:25600\n\t" \
  "s_waitcnt lgkmcnt(5)\n\t" \
  "v_mfma_f32_32x32x16_bf16 v[96:111], v[244:247], a[88:91], v[96:111]\n\t" \
  "ds_read_b128 v[236:239], %[lw0] offset:58368\n\t" \
  "s_waitcnt vmcnt(8) lgkmcnt(5)\n\t" \
  "v_mfma_f32_32x32x16_bf16 v[80:95], v[248:251], a[92:95], v[80:95]\n\t" \
  "ds_read_b128 v[240:243], %[lw0] offset:26624\n\t" \
  "s_waitcnt lgkmcnt(5)\n\t" \
  "v_mfma_f32_32x32x16_bf16 v[96:111], v[252:255], a[92:95], v[96:111]\n\t" \
  "ds_read_b128 v[244:247], %[lw0] offset:59392\n\t" \
  "s_waitcnt vmcnt(7) lgkmcnt(5)\n\t" \
  "v_mfma_f32_32x32x16_bf16 v[80:95], v[224:227], a[96:99], v[80:95]\n\t" \
  "ds_read_b128 v[248:251], %[lw0] offset:27648\n\t" \
  "s_waitcnt lgkmcnt(5)\n\t" \
  "v_mfma_f32_32x32x16_bf16 v[96:111], v[228:231], a[96:99], v[96:111]\n\t" \
  "ds_read_b128 v[252:255], %[lw0] offset:60416\n\t" \
  "s_waitcnt vmcnt(6) lgkmcnt(5)\n\t" \
  "v_mfma_f32_32x32x16_bf16 v[80:95], v[232:235], a[100:103], v[80:95]\n\t" \
  "ds_read_b128 v[224:227], %[lw0] offset:28672\n\t" \
  "s_waitcnt lgkmcnt(5)\n\t" \
  "v_mfma_f32_32x32x16_bf16 v[96:111], v[236:239], a[100:103], v[96:111]\n\t" \
  "ds_read_b128 v[228:231], %[lw0] offset:61440\n\t" \
  "s_waitcnt vmcnt(5) lgkmcnt(5)\n\t" \
  "v_mfma_f32_32x32x16_bf16 v[80:95], v[240:243], a[104:107], v[80:95]\n\t" \
  "ds_read_b128 v[232:235], %[lw0] offset:29696\n\t" \
  "s_waitcnt lgkmcnt(5)\n\t" \
  "v_mfma_f32_32x32x16_bf16 v[96:111], v[244:247], a[104:107], v[96:111]\n\t" \
  "ds_read_b128 v[236:239], %[lw0] offset:62464\n\t" \
  "s_waitcnt vmcnt(4) lgkmcnt(5)\n\t" \
  "v_mfma_f32_32x32x16_bf16 v[80:95], v[248:251], a[108:111], v[80:95]\n\t" \
  "ds_read_b128 v[240:243], %[lw0] offset:30720\n\t" \
  "s_waitcnt lgkmcnt(5)\n\t" \
  "v_mfma_f32_32x32x16_bf16 v[96:111], v[252:255], a[108:111], v[96:111]\n\t" \
  "ds_read_b128 v[244:247], %[lw0] offset:63488\n\t" \
  "s_waitcnt vmcnt(3) lgkmcnt(5)\n\t" \
  "v_mfma_f32_32x32x16_bf16 v[80:95], v[224:227], a[112:115], v[80:95]\n\t" \
  "ds_read_b128 v[248:251], %[lw0] offset:31744\n\t" \
  "s_waitcnt lgkmcnt(5)\n\t" \
  "v_mfma_f32_32x32x16_bf16 v[96:111], v[228:231], a[112:115], v[96:111]\n\t" \
  "ds_read_b128 v[252:255], %[lw0] offset:64512\n\t" \
  "s_waitcnt vmcnt(2) lgkmcnt(5)\n\t" \
  "v_mfma_f32_32x32x16_bf16 v[80:95], v[232:235], a[116:119], v[80:95]\n\t" \
  "ds_read_b128 v[224:227], %[lext]\n\t" \
  "s_waitcnt lgkmcnt(5)\n\t" \
  "v_mfma_f32_32x32x16_bf16 v[96:111], v[236:239], a[116:119], v[96:111]\n\t" \
  "ds_read_b128 v[228:231], %[lext] offset:1024\n\t" \
  "s_waitcnt vmcnt(1) lgkmcnt(5)\n\t" \
  "v_mfma_f32_32x32x16_bf16 v[80:95], v[240:243], a[120:123], v[80:95]\n\t" \
  "ds_read_b128 v[232:235], %[lw1]\n\t" \
  "s_waitcnt lgkmcnt(5)\n\t" \
  "v_mfma_f32_32x32x16_bf16 v[96:111], v[244:247], a[120:123], v[96:111]\n\t" \
  "ds_read_b128 v[236:239], %[lw1] offset:1024\n\t" \
  "s_waitcnt vmcnt(0) lgkmcnt(5)\n\t" \
  "v_mfma_f32_32x32x16_bf16 v[80:95], v[248:251], a[124:127], v[80:95]\n\t" \
  "ds_read_b128 v[240:243], %[lw1] offset:2048\n\t" \
  "s_waitcnt lgkmcnt(5)\n\t" \
  "v_mfma_f32_32x32x16_bf16 v[96:111], v[252:255], a[124:127], v[96:111]\n\t" \
  "ds_read_b128 v[244:247], %[lw1] offset:3072\n\t" \
  "s_waitcnt lgkmcnt(5)\n\t" \
  "v_mfma_f32_32x32x16_bf16 v[80:95], v[224:227], v[112:115], v[80:95]\n\t" \
  "ds_read_b128 v[248:251], %[lw1] offset:4096\n\t" \
  "s_waitcnt lgkmcnt(5)\n\t" \
  "v_mfma_f32_32x32x16_bf16 v[96:111], v[228:231], v[112:115], v[96:111]\n\t" \
  "ds_read_b128 v[252:255], %[lw1] offset:5120\n\t" \
  "s_waitcnt lgkmcnt(5)\n\t" \
  "v_mfma_f32_32x32x16_bf16 v[192:207], v[232:235], a[0:3], 0\n\t" \
  "ds_read_b128 v[224:227], %[lw1] offset:6144\n\t" \
  "s_waitcnt lgkmcnt(5)\n\t" \
  "v_mfma_f32_32x32x16_bf16 v[192:207], v[236:239], a[4:7], v[192:207]\n\t" \
  "ds_read_b128 v[228:231], %[lw1] offset:7168\n\t" \
  "s_waitcnt lgkmcnt(5)\n\t" \
  "v_mfma_f32_32x32x16_bf16 v[192:207], v[240:243], a[8:11], v[192:207]\n\t" \
  "ds_read_b128 v[232:235], %[lw1] offset:8192\n\t" \
  "v_cvt_pk_bf16_f32 v80, v80, v81\n\t" \
  "s_waitcnt lgkmcnt(5)\n\t" \
  "v_mfma_f32_32x32x16_bf16 v[192:207], v[244:247], a[12:15], v[192:207]\n\t" \
  "ds_read_b128 v[236:239], %[lw1] offset:9216\n\t" \
  "v_cvt_pk_bf16_f32 v81, v82, v83\n\t" \
  "s_waitcnt lgkmcnt(5)\n\t" \
  "v_mfma_f32_32x32x16_bf16 v[192:207], v[248:251], a[16:19], v[192:207]\n\t" \
  "ds_read_b128 v[240:243], %[lw1] offset:10240\n\t" \
  "v_cvt_pk_bf16_f32 v82, v84, v85\n\t" \
  "s_waitcnt lgkmcnt(5)\n\t" \
  "v_mfma_f32_32x32x16_bf16 v[192:207], v[252:255], a[20:23], v[192:207]\n\t" \
  "ds_read_b128 v[244:247], %[lw1] offset:11264\n\t" \
  "v_cvt_pk_bf16_f32 v83, v86, v87\n\t" \
  "s_waitcnt lgkmcnt(5)\n\t" \
  "v_mfma_f32_32x32x16_bf16 v[192:207], v[224:227], a[24:27], v[192:207]\n\t" \
  "ds_read_b128 v[248:251], %[lw1] offset:12288\n\t" \
  "v_pk_max_i16 v80, v80, 0\n\t" \
  "s_waitcnt lgkmcnt(5)\n\t" \
  "v_mfma_f32_32x32x16_bf16 v[192:207], v[228:231], a[28:31], v[192:207]\n\t" \
  "ds_read_b128 v[252:255], %[lw1] offset:13312\n\t" \
  "v_pk_max_i16 v81, v81, 0\n\t" \
  "s_waitcnt lgkmcnt(5)\n\t" \
  "v_mfma_f32_32x32x16_bf16 v[192:207], v[232:235], a[32:35], v[192:207]\n\t" \
  "ds_read_b128 v[224:227], %[lw1] offset:14336\n\t" \
  "v_pk_max_i16 v82, v82, 0\n\t" \
  "s_waitcnt lgkmcnt(5)\n\t" \
  "v_mfma_f32_32x32x16_bf16 v[192:207], v[236:239], a[36:39], v[192:207]\n\t" \
  "ds_read_b128 v[228:231], %[lw1] offset:15360\n\t" \
  "v_pk_max_i16 v83, v83, 0\n\t" \
  "s_waitcnt lgkmcnt(5)\n\t" \
  "v_mfma_f32_32x32x16_bf16 v[192:207], v[240:243], a[40:43], v[192:207]\n\t" \
  "ds_read_b128 v[232:235], %[lw1] offset:16384\n\t" \
  "buffer_store_dwordx4 v[80:83], %[l16], %[rs], %[sh0] offen sc1\n\t" \
  "s_waitcnt lgkmcnt(5)\n\t" \
  "v_mfma_f32_32x32x16_bf16 v[192:207], v[244:247], a[44:47], v[192:207]\n\t" \
  "ds_read_b128 v[236:239], %[lw1] offset:17408\n\t" \
  "v_cvt_pk_bf16_f32 v88, v88, v89\n\t" \
  "s_waitcnt lgkmcnt(5)\n\t" \
  "v_mfma_f32_32x32x16_bf16 v[192:207], v[248:251], a[48:51], v[192:207]\n\t" \
  "ds_read_b128 v[240:243], %[lw1] offset:18432\n\t" \
  "v_cvt_pk_bf16_f32 v89, v90, v91\n\t" \
  "s_waitcnt lgkmcnt(5)\n\t" \
  "v_mfma_f32_32x32x16_bf16 v[192:207], v[252:255], a[52:55], v[192:207]\n\t" \
  "ds_read_b128 v[244:247], %[lw1] offset:19456\n\t" \
  "v_cvt_pk_bf16_f32 v90, v92, v93\n\t" \
  "s_waitcnt lgkmcnt(5)\n\t" \
  "v_mfma_f32_32x32x16_bf16 v[192:207], v[224:227], a[56:59], v[192:207]\n\t" \
  "ds_read_b128 v[248:251], %[lw1] offset:20480\n\t" \
  "v_cvt_pk_bf16_f32 v91, v94, v95\n\t" \
  "s_waitcnt lgkmcnt(5)\n\t" \
  "v_mfma_f32_32x32x16_bf16 v[192:207], v[228:231], a[60:63], v[192:207]\n\t" \
  "ds_read_b128 v[252:255], %[lw1] offset:21504\n\t" \
  "v_pk_max_i16 v88, v88, 0\n\t" \
  "s_waitcnt lgkmcnt(5)\n\t" \
  "v_mfma_f32_32x32x16_bf16 v[192:207], v[232:235], a[64:67], v[192:207]\n\t" \
  "ds_read_b128 v[224:227], %[lw1] offset:22528\n\t" \
  "v_pk_max_i16 v89, v89, 0\n\t" \
  "s_waitcnt lgkmcnt(5)\n\t" \
  "v_mfma_f32_32x32x16_bf16 v[192:207], v[236:239], a[68:71], v[192:207]\n\t" \
  "ds_read_b128 v[228:231], %[lw1] offset:23552\n\t" \
  "v_pk_max_i16 v90, v90, 0\n\t" \
  "s_waitcnt lgkmcnt(5)\n\t" \
  "v_mfma_f32_32x32x16_bf16 v[192:207], v[240:243], a[72:75], v[192:207]\n\t" \
  "ds_read_b128 v[232:235], %[lw1] offset:24576\n\t" \
  "v_pk_max_i16 v91, v91, 0\n\t" \
  "s_waitcnt lgkmcnt(5)\n\t" \
  "v_mfma_f32_32x32x16_bf16 v[192:207], v[244:247], a[76:79], v[192:207]\n\t" \
  "ds_read_b128 v[236:239], %[lw1] offset:25600\n\t" \
  "buffer_store_dwordx4 v[88:91], %[l16], %[rs], %[sh0] offen offset:1024 sc1\n\t" \
  "s_waitcnt lgkmcnt(5)\n\t" \
  "v_mfma_f32_32x32x16_bf16 v[192:207], v[248:251], a[80:83], v[192:207]\n\t" \
  "ds_read_b128 v[240:243], %[lw1] offset:26624\n\t" \
  "v_cvt_pk_bf16_f32 v96, v96, v97\n\t" \
  "s_waitcnt lgkmcnt(5)\n\t" \
  "v_mfma_f32_32x32x16_bf16 v[192:207], v[252:255], a[84:87], v[192:207]\n\t" \
  "ds_read_b128 v[244:247], %[lw1] offset:27648\n\t" \
  "v_cvt_pk_bf16_f32 v97, v98, v99\n\t" \
  "s_waitcnt lgkmcnt(5)\n\t" \
  "v_mfma_f32_32x32x16_bf16 v[192:207], v[224:227], a[88:91], v[192:207]\n\t" \
  "ds_read_b128 v[248:251], %[lw1] offset:28672\n\t" \
  "v_cvt_pk_bf16_f32 v98, v100, v101\n\t" \
  "s_waitcnt lgkmcnt(5)\n\t" \
  "v_mfma_f32_32x32x16_bf16 v[192:207], v[228:231], a[92:95], v[192:207]\n\t" \
  "ds_read_b128 v[252:255], %[lw1] offset:29696\n\t" \
  "v_cvt_pk_bf16_f32 v99, v102, v103\n\t" \
  "s_waitcnt lgkmcnt(5)\n\t" \
  "v_mfma_f32_32x32x16_bf16 v[192:207], v[232:235], a[96:99], v[192:207]\n\t" \
  "ds_read_b128 v[224:227], %[lw1] offset:30720\n\t" \
  "v_pk_max_i16 v96, v96, 0\n\t" \
  "s_waitcnt lgkmcnt(5)\n\t" \
  "v_mfma_f32_32x32x16_bf16 v[192:207], v[236:239], a[100:103], v[192:207]\n\t" \
  "ds_read_b128 v[228:231], %[lw1] offset:31744\n\t" \
  "v_pk_max_i16 v97, v97, 0\n\t" \
  "s_waitcnt lgkmcnt(5)\n\t" \
  "v_mfma_f32_32x32x16_bf16 v[192:207], v[240:243], a[104:107], v[192:207]\n\t" \
  "ds_read_b128 v[232:235], %[lext] offset:2048\n\t" \
  "v_pk_max_i16 v98, v98, 0\n\t" \
  "s_waitcnt lgkmcnt(5)\n\t" \
  "v_mfma_f32_32x32x16_bf16 v[192:207], v[244:247], a[108:111], v[192:207]\n\t" \
  "ds_read_b128 v[236:239], %[lw1] offset:32768\n\t" \
  "v_pk_max_i16 v99, v99, 0\n\t" \
  "s_waitcnt lgkmcnt(5)\n\t" \
  "v_mfma_f32_32x32x16_bf16 v[192:207], v[248:251], a[112:115], v[192:207]\n\t" \
  "ds_read_b128 v[240:243], %[lw1] offset:33792\n\t" \
  "buffer_store_dwordx4 v[96:99], %[l16], %[rs], %[sh0] offen offset:2048 sc1\n\t" \
  "s_waitcnt lgkmcnt(5)\n\t" \
  "v_mfma_f32_32x32x16_bf16 v[192:207], v[252:255], a[116:119], v[192:207]\n\t" \
  "ds_read_b128 v[244:247], %[lw1] offset:34816\n\t" \
  "v_cvt_pk_bf16_f32 v104, v104, v105\n\t" \
  "s_waitcnt lgkmcnt(5)\n\t" \
  "v_mfma_f32_32x32x16_bf16 v[192:207], v[224:227], a[120:123], v[192:207]\n\t" \
  "ds_read_b128 v[248:251], %[lw1] offset:35840\n\t" \
  "v_cvt_pk_bf16_f32 v105, v106, v107\n\t" \
  "s_waitcnt lgkmcnt(5)\n\t" \
  "v_mfma_f32_32x32x16_bf16 v[192:207], v[228:231], a[124:127], v[192:207]\n\t" \
  "ds_read_b128 v[252:255], %[lw1] offset:36864\n\t" \
  "v_cvt_pk_bf16_f32 v106, v108, v109\n\t" \
  "s_waitcnt lgkmcnt(5)\n\t" \
  "v_mfma_f32_32x32x16_bf16 v[192:207], v[232:235], v[112:115], v[192:207]\n\t" \
  "ds_read_b128 v[224:227], %[lw1] offset:37888\n\t" \
  "v_cvt_pk_bf16_f32 v107, v110, v111\n\t" \
  "s_waitcnt lgkmcnt(5)\n\t" \
  "v_mfma_f32_32x32x16_bf16 v[208:223], v[236:239], a[0:3], 0\n\t" \
  "ds_read_b128 v[228:231], %[lw1] offset:38912\n\t" \
  "v_pk_max_i16 v104, v104, 0\n\t" \
  "s_waitcnt lgkmcnt(5)\n\t" \
  "v_mfma_f32_32x32x16_bf16 v[208:223], v[240:243], a[4:7], v[208:223]\n\t" \
  "ds_read_b128 v[232:235], %[lw1] offset:39936\n\t" \
  "v_pk_max_i16 v105, v105, 0\n\t" \
  "s_waitcnt lgkmcnt(5)\n\t" \
  "v_mfma_f32_32x32x16_bf16 v[208:223], v[244:247], a[8:11], v[208:223]\n\t" \
  "ds_read_b128 v[236:239], %[lw1] offset:40960\n\t" \
  "v_pk_max_i16 v106, v106, 0\n\t" \
  "s_waitcnt lgkmcnt(5)\n\t" \
  "v_mfma_f32_32x32x16_bf16 v[208:223], v[248:251], a[12:15], v[208:223]\n\t" \
  "ds_read_b128 v[240:243], %[lw1] offset:41984\n\t" \
  "v_pk_max_i16 v107, v107, 0\n\t" \
  "s_waitcnt lgkmcnt(5)\n\t" \
  "v_mfma_f32_32x32x16_bf16 v[208:223], v[252:255], a[16:19], v[208:223]\n\t" \
  "ds_read_b128 v[244:247], %[lw1] offset:43008\n\t" \
  "buffer_store_dwordx4 v[104:107], %[l16], %[rs], %[sh0] offen offset:3072 sc1\n\t" \
  "s_waitcnt lgkmcnt(5)\n\t" \
  "v_mfma_f32_32x32x16_bf16 v[208:223], v[224:227], a[20:23], v[208:223]\n\t" \
  "ds_read_b128 v[248:251], %[lw1] offset:44032\n\t" \
  "v_cvt_pk_bf16_f32 v192, v192, v193\n\t" \
  "s_waitcnt lgkmcnt(5)\n\t" \
  "v_mfma_f32_32x32x16_bf16 v[208:223], v[228:231], a[24:27], v[208:223]\n\t" \
  "ds_read_b128 v[252:255], %[lw1] offset:45056\n\t" \
  "v_cvt_pk_bf16_f32 v193, v194, v195\n\t" \
  "s_waitcnt lgkmcnt(5)\n\t" \
  "v_mfma_f32_32x32x16_bf16 v[208:223], v[232:235], a[28:31], v[208:223]\n\t" \
  "ds_read_b128 v[224:227], %[lw1] offset:46080\n\t" \
  "v_cvt_pk_bf16_f32 v194, v196, v197\n\t" \
  "s_waitcnt lgkmcnt(5)\n\t" \
  "v_mfma_f32_32x32x16_bf16 v[208:223], v[236:239], a[32:35], v[208:223]\n\t" \
  "ds_read_b128 v[228:231], %[lw1] offset:47104\n\t" \
  "v_cvt_pk_bf16_f32 v195, v198, v199\n\t" \
  "s_waitcnt lgkmcnt(5)\n\t" \
  "v_mfma_f32_32x32x16_bf16 v[208:223], v[240:243], a[36:39], v[208:223]\n\t" \
  "ds_read_b128 v[232:235], %[lw1] offset:48128\n\t" \
  "v_pk_max_i16 v192, v192, 0\n\t" \
  "s_waitcnt lgkmcnt(5)\n\t" \
  "v_mfma_f32_32x32x16_bf16 v[208:223], v[244:247], a[40:43], v[208:223]\n\t" \
  "ds_read_b128 v[236:239], %[lw1] offset:49152\n\t" \
  "v_pk_max_i16 v193, v193, 0\n\t" \
  "s_waitcnt lgkmcnt(5)\n\t" \
  "v_mfma_f32_32x32x16_bf16 v[208:223], v[248:251], a[44:47], v[208:223]\n\t" \
  "ds_read_b128 v[240:243], %[lw1] offset:50176\n\t" \
  "v_pk_max_i16 v194, v194, 0\n\t" \
  "s_waitcnt lgkmcnt(5)\n\t" \
  "v_mfma_f32_32x32x16_bf16 v[208:223], v[252:255], a[48:51], v[208:223]\n\t" \
  "ds_read_b128 v[244:247], %[lw1] offset:51200\n\t" \
  "v_pk_max_i16 v195, v195, 0\n\t" \
  "s_waitcnt lgkmcnt(5)\n\t" \
  "v_mfma_f32_32x32x16_bf16 v[208:223], v[224:227], a[52:55], v[208:223]\n\t" \
  "ds_read_b128 v[248:251], %[lw1] offset:52224\n\t" \
  "buffer_store_dwordx4 v[192:195], %[l16], %[rs], %[sh1] offen sc1\n\t" \
  "s_waitcnt lgkmcnt(5)\n\t" \
  "v_mfma_f32_32x32x16_bf16 v[208:223], v[228:231], a[56:59], v[208:223]\n\t" \
  "ds_read_b128 v[252:255], %[lw1] offset:53248\n\t" \
  "v_cvt_pk_bf16_f32 v200, v200, v201\n\t" \
  "s_waitcnt lgkmcnt(5)\n\t" \
  "v_mfma_f32_32x32x16_bf16 v[208:223], v[232:235], a[60:63], v[208:223]\n\t" \
  "ds_read_b128 v[224:227], %[lw1] offset:54272\n\t" \
  "v_cvt_pk_bf16_f32 v201, v202, v203\n\t" \
  "s_waitcnt lgkmcnt(5)\n\t" \
  "v_mfma_f32_32x32x16_bf16 v[208:223], v[236:239], a[64:67], v[208:223]\n\t" \
  "ds_read_b128 v[228:231], %[lw1] offset:55296\n\t" \
  "v_cvt_pk_bf16_f32 v202, v204, v205\n\t" \
  "s_waitcnt lgkmcnt(5)\n\t" \
  "v_mfma_f32_32x32x16_bf16 v[208:223], v[240:243], a[68:71], v[208:223]\n\t" \
  "ds_read_b128 v[232:235], %[lw1] offset:56320\n\t" \
  "v_cvt_pk_bf16_f32 v203, v206, v207\n\t" \
  "s_waitcnt lgkmcnt(5)\n\t" \
  "v_mfma_f32_32x32x16_bf16 v[208:223], v[244:247], a[72:75], v[208:223]\n\t" \
  "ds_read_b128 v[236:239], %[lw1] offset:57344\n\t" \
  "v_pk_max_i16 v200, v200, 0\n\t" \
  "s_waitcnt lgkmcnt(5)\n\t" \
  "v_mfma_f32_32x32x16_bf16 v[208:223], v[248:251], a[76:79], v[208:223]\n\t" \
  "ds_read_b128 v[240:243], %[lw1] offset:58368\n\t" \
  "v_pk_max_i16 v201, v201, 0\n\t" \
  "s_waitcnt lgkmcnt(5)\n\t" \
  "v_mfma_f32_32x32x16_bf16 v[208:223], v[252:255], a[80:83], v[208:223]\n\t" \
  "ds_read_b128 v[244:247], %[lw1] offset:59392\n\t" \
  "v_pk_max_i16 v202, v202, 0\n\t" \
  "s_waitcnt lgkmcnt(5)\n\t" \
  "v_mfma_f32_32x32x16_bf16 v[208:223], v[224:227], a[84:87], v[208:223]\n\t" \
  "ds_read_b128 v[248:251], %[lw1] offset:60416\n\t" \
  "v_pk_max_i16 v203, v203, 0\n\t" \
  "s_waitcnt lgkmcnt(5)\n\t" \
  "v_mfma_f32_32x32x16_bf16 v[208:223], v[228:231], a[88:91], v[208:223]\n\t" \
  "ds_read_b128 v[252:255], %[lw1] offset:61440\n\t" \
  "buffer_store_dwordx4 v[200:203], %[l16], %[rs], %[sh1] offen offset:1024 sc1\n\t" \
  "s_waitcnt lgkmcnt(5)\n\t" \
  "v_mfma_f32_32x32x16_bf16 v[208:223], v[232:235], a[92:95], v[208:223]\n\t" \
  "ds_read_b128 v[224:227], %[lw1] offset:62464\n\t" \
  "s_waitcnt lgkmcnt(5)\n\t" \
  "v_mfma_f32_32x32x16_bf16 v[208:223], v[236:239], a[96:99], v[208:223]\n\t" \
  "ds_read_b128 v[228:231], %[lw1] offset:63488\n\t" \
  "s_waitcnt lgkmcnt(5)\n\t" \
  "v_mfma_f32_32x32x16_bf16 v[208:223], v[240:243], a[100:103], v[208:223]\n\t" \
  "ds_read_b128 v[232:235], %[lw1] offset:64512\n\t" \
  "s_waitcnt lgkmcnt(5)\n\t" \
  "v_mfma_f32_32x32x16_bf16 v[208:223], v[244:247], a[104:107], v[208:223]\n\t" \
  "ds_read_b128 v[236:239], %[lext] offset:3072\n\t" \
  "s_waitcnt lgkmcnt(5)\n\t" \
  "v_mfma_f32_32x32x16_bf16 v[208:223], v[248:251], a[108:111], v[208:223]\n\t" \
  "s_waitcnt lgkmcnt(4)\n\t" \
  "v_mfma_f32_32x32x16_bf16 v[208:223], v[252:255], a[112:115], v[208:223]\n\t" \
  "s_waitcnt lgkmcnt(3)\n\t" \
  "v_mfma_f32_32x32x16_bf16 v[208:223], v[224:227], a[116:119], v[208:223]\n\t" \
  "s_waitcnt lgkmcnt(2)\n\t" \
  "v_mfma_f32_32x32x16_bf16 v[208:223], v[228:231], a[120:123], v[208:223]\n\t" \
  "s_waitcnt lgkmcnt(1)\n\t" \
  "v_mfma_f32_32x32x16_bf16 v[208:223], v[232:235], a[124:127], v[208:223]\n\t" \
  "s_waitcnt lgkmcnt(0)\n\t" \
  "v_mfma_f32_32x32x16_bf16 v[208:223], v[236:239], v[112:115], v[208:223]\n\t" \
  "s_nop 7\n\t" \
  "s_nop 3\n\t" \
  "v_cvt_pk_bf16_f32 v208, v208, v209\n\t" \
  "v_cvt_pk_bf16_f32 v209, v210, v211\n\t" \
  "v_cvt_pk_bf16_f32 v210, v212, v213\n\t" \
  "v_cvt_pk_bf16_f32 v211, v214, v215\n\t" \
  "v_pk_max_i16 v208, v208, 0\n\t" \
  "v_pk_max_i16 v209, v209, 0\n\t" \
  "v_pk_max_i16 v210, v210, 0\n\t" \
  "v_pk_max_i16 v211, v211, 0\n\t" \
  "buffer_store_dwordx4 v[208:211], %[l16], %[rs], %[sh1] offen offset:2048 sc1\n\t" \
  "v_cvt_pk_bf16_f32 v216, v216, v217\n\t" \
  "v_cvt_pk_bf16_f32 v217, v218, v219\n\t" \
  "v_cvt_pk_bf16_f32 v218, v220, v221\n\t" \
  "v_cvt_pk_bf16_f32 v219, v222, v223\n\t" \
  "v_pk_max_i16 v216, v216, 0\n\t" \
  "v_pk_max_i16 v217, v217, 0\n\t" \
  "v_pk_max_i16 v218, v218, 0\n\t" \
  "v_pk_max_i16 v219, v219, 0\n\t" \
  "buffer_store_dwordx4 v[216:219], %[l16], %[rs], %[sh1] offen offset:3072 sc1\n\t"
#define RSTD2_ASM \
  "s_nop 4\n\t" \
  "buffer_load_dwordx2 v[192:193], %[sv], %[rs], %[sst2] offen sc1\n\t" \
  "buffer_load_dwordx2 v[194:195], %[sv], %[rs], %[sst2] offen offset:256 sc1\n\t" \
  "buffer_load_dwordx2 v[196:197], %[sv], %[rs], %[sst2] offen offset:512 sc1\n\t" \
  "buffer_load_dwordx2 v[198:199], %[sv], %[rs], %[sst2] offen offset:768 sc1\n\t" \
  "buffer_load_dwordx2 v[200:201], %[sv], %[rs], %[sst2] offen offset:1024 sc1\n\t" \
  "buffer_load_dwordx2 v[202:203], %[sv], %[rs], %[sst2] offen offset:1280 sc1\n\t" \
  "buffer_load_dwordx2 v[204:205], %[sv], %[rs], %[sst2] offen offset:1536 sc1\n\t" \
  "buffer_load_dwordx2 v[206:207], %[sv], %[rs], %[sst2] offen offset:1792 sc1\n\t" \
  "s_waitcnt vmcnt(0)\n\t" \
  "v_add_f32 v118, v192, v194\n\t" \
  "v_add_f32 v118, v118, v196\n\t" \
  "v_add_f32 v118, v118, v198\n\t" \
  "v_add_f32 v118, v118, v200\n\t" \
  "v_add_f32 v118, v118, v202\n\t" \
  "v_add_f32 v118, v118, v204\n\t" \
  "v_add_f32 v118, v118, v206\n\t" \
  "v_mov_b32 v116, v118\n\t" \
  "s_nop 1\n\t" \
  "v_permlane32_swap_b32 v118, v116\n\t" \
  "v_add_f32 v118, v118, v116\n\t" \
  "v_mul_f32 v118, 0x3d800000, v118\n\t" \
  "v_add_f32 v208, v193, v195\n\t" \
  "v_add_f32 v208, v208, v197\n\t" \
  "v_add_f32 v208, v208, v199\n\t" \
  "v_add_f32 v208, v208, v201\n\t" \
  "v_add_f32 v208, v208, v203\n\t" \
  "v_add_f32 v208, v208, v205\n\t" \
  "v_add_f32 v208, v208, v207\n\t" \
  "v_sub_f32 v116, v192, v118\n\t" \
  "v_fma_f32 v209, v116, v116, 0\n\t" \
  "v_sub_f32 v116, v194, v118\n\t" \
  "v_fma_f32 v209, v116, v116, v209\n\t" \
  "v_sub_f32 v116, v196, v118\n\t" \
  "v_fma_f32 v209, v116, v116, v209\n\t" \
  "v_sub_f32 v116, v198, v118\n\t" \
  "v_fma_f32 v209, v116, v116, v209\n\t" \
  "v_sub_f32 v116, v200, v118\n\t" \
  "v_fma_f32 v209, v116, v116, v209\n\t" \
  "v_sub_f32 v116, v202, v118\n\t" \
  "v_fma_f32 v209, v116, v116, v209\n\t" \
  "v_sub_f32 v116, v204, v118\n\t" \
  "v_fma_f32 v209, v116, v116, v209\n\t" \
  "v_sub_f32 v116, v206, v118\n\t" \
  "v_fma_f32 v209, v116, v116, v209\n\t" \
  "v_fmac_f32 v208, 0x42000000, v209\n\t" \
  "v_mov_b32 v116, v208\n\t" \
  "s_nop 1\n\t" \
  "v_permlane32_swap_b32 v208, v116\n\t" \
  "v_add_f32 v208, v208, v116\n\t" \
  "v_mov_b32 v116, 0x3727c5ac\n\t" \
  "v_fmac_f32 v116, 0x3b000000, v208\n\t" \
  "v_rsq_f32 v119, v116\n\t" \
  "s_nop 0\n\t"
#define W2O_ASM \
  "s_nop 4\n\t" \
  "s_add_u32 %[wa], %[wa], 16384\n\t" \
  "buffer_load_dwordx4 a[128:131], %[l16], %[rs], %[wa] offen offset:1024 sc1\n\t" \
  "s_add_u32 %[wb], %[wb], 16384\n\t" \
  "buffer_load_dwordx4 a[132:135], %[l16], %[rs], %[wb] offen offset:1024 sc1\n\t" \
  "buffer_load_dwordx4 a[136:139], %[l16], %[rs], %[wa] offen offset:2048 sc1\n\t" \
  "buffer_load_dwordx4 a[140:143], %[l16], %[rs], %[wb] offen offset:2048 sc1\n\t" \
  "buffer_load_dwordx4 a[144:147], %[l16], %[rs], %[wa] offen offset:3072 sc1\n\t" \
  "buffer_load_dwordx4 a[148:151], %[l16], %[rs], %[wb] offen offset:3072 sc1\n\t" \
  "s_add_u32 %[wa], %[wa], 4096\n\t" \
  "buffer_load_dwordx4 a[152:155], %[l16], %[rs], %[wa] offen sc1\n\t" \
  "s_add_u32 %[wb], %[wb], 4096\n\t" \
  "buffer_load_dwordx4 a[156:159], %[l16], %[rs], %[wb] offen sc1\n\t" \
  "buffer_load_dwordx4 a[160:163], %[l16], %[rs], %[wa] offen offset:1024 sc1\n\t" \
  "buffer_load_dwordx4 a[164:167], %[l16], %[rs], %[wb] offen offset:1024 sc1\n\t" \
  "buffer_load_dwordx4 a[168:171], %[l16], %[rs], %[wa] offen offset:2048 sc1\n\t" \
  "buffer_load_dwordx4 a[172:175], %[l16], %[rs], %[wb] offen offset:2048 sc1\n\t" \
  "buffer_load_dwordx4 a[176:179], %[l16], %[rs], %[wa] offen offset:3072 sc1\n\t" \
  "buffer_load_dwordx4 a[180:183], %[l16], %[rs], %[wb] offen offset:3072 sc1\n\t" \
  "s_add_u32 %[wa], %[wa], 4096\n\t" \
  "buffer_load_dwordx4 a[184:187], %[l16], %[rs], %[wa] offen sc1\n\t" \
  "s_add_u32 %[wb], %[wb], 4096\n\t" \
  "buffer_load_dwordx4 a[188:191], %[l16], %[rs], %[wb] offen sc1\n\t"
#define W2OD_ASM \
  "s_nop 4\n\t" \
  "s_add_u32 %[wa], %[wa], 16384\n\t" \
  "buffer_load_dwordx4 a[128:131], %[l16], %[rs], %[wa] offen offset:1024 sc1\n\t" \
  "ds_write_b128 %[lw], a[0:3]\n\t" \
  "ds_write_b128 %[lw], a[4:7] offset:1024\n\t" \
  "s_add_u32 %[wb], %[wb], 16384\n\t" \
  "buffer_load_dwordx4 a[132:135], %[l16], %[rs], %[wb] offen offset:1024 sc1\n\t" \
  "ds_write_b128 %[lw], a[8:11] offset:2048\n\t" \
  "ds_write_b128 %[lw], a[12:15] offset:3072\n\t" \
  "buffer_load_dwordx4 a[136:139], %[l16], %[rs], %[wa] offen offset:2048 sc1\n\t" \
  "ds_write_b128 %[lw], a[16:19] offset:4096\n\t" \
  "ds_write_b128 %[lw], a[20:23] offset:5120\n\t" \
  "buffer_load_dwordx4 a[140:143], %[l16], %[rs], %[wb] offen offset:2048 sc1\n\t" \
  "ds_write_b128 %[lw], a[24:27] offset:6144\n\t" \
  "ds_write_b128 %[lw], a[28:31] offset:7168\n\t" \
  "buffer_load_dwordx4 a[144:147], %[l16], %[rs], %[wa] offen offset:3072 sc1\n\t" \
  "ds_write_b128 %[lw], a[32:35] offset:8192\n\t" \
  "ds_write_b128 %[lw], a[36:39] offset:9216\n\t" \
  "buffer_load_dwordx4 a[148:151], %[l16], %[rs], %[wb] offen offset:3072 sc1\n\t" \
  "ds_write_b128 %[lw], a[40:43] offset:10240\n\t" \
  "ds_write_b128 %[lw], a[44:47] offset:11264\n\t" \
  "s_add_u32 %[wa], %[wa], 4096\n\t" \
  "buffer_load_dwordx4 a[152:155], %[l16], %[rs], %[wa] offen sc1\n\t" \
  "ds_write_b128 %[lw], a[48:51] offset:12288\n\t" \
  "ds_write_b128 %[lw], a[52:55] offset:13312\n\t" \
  "s_add_u32 %[wb], %[wb], 4096\n\t" \
  "buffer_load_dwordx4 a[156:159], %[l16], %[rs], %[wb] offen sc1\n\t" \
  "ds_write_b128 %[lw], a[56:59] offset:14336\n\t" \
  "ds_write_b128 %[lw], a[60:63] offset:15360\n\t" \
  "buffer_load_dwordx4 a[160:163], %[l16], %[rs], %[wa] offen offset:1024 sc1\n\t" \
  "ds_write_b128 %[lw], a[64:67] offset:16384\n\t" \
  "ds_write_b128 %[lw], a[68:71] offset:17408\n\t" \
  "buffer_load_dwordx4 a[164:167], %[l16], %[rs], %[wb] offen offset:1024 sc1\n\t" \
  "ds_write_b128 %[lw], a[72:75] offset:18432\n\t" \
  "ds_write_b128 %[lw], a[76:79] offset:19456\n\t" \
  "buffer_load_dwordx4 a[168:171], %[l16], %[rs], %[wa] offen offset:2048 sc1\n\t" \
  "ds_write_b128 %[lw], a[80:83] offset:20480\n\t" \
  "ds_write_b128 %[lw], a[84:87] offset:21504\n\t" \
  "buffer_load_dwordx4 a[172:175], %[l16], %[rs], %[wb] offen offset:2048 sc1\n\t" \
  "ds_write_b128 %[lw], a[88:91] offset:22528\n\t" \
  "ds_write_b128 %[lw], a[92:95] offset:23552\n\t" \
  "buffer_load_dwordx4 a[176:179], %[l16], %[rs], %[wa] offen offset:3072 sc1\n\t" \
  "ds_write_b128 %[lw], a[96:99] offset:24576\n\t" \
  "ds_write_b128 %[lw], a[100:103] offset:25600\n\t" \
  "buffer_load_dwordx4 a[180:183], %[l16], %[rs], %[wb] offen offset:3072 sc1\n\t" \
  "ds_write_b128 %[lw], a[104:107] offset:26624\n\t" \
  "ds_write_b128 %[lw], a[108:111] offset:27648\n\t" \
  "s_add_u32 %[wa], %[wa], 4096\n\t" \
  "buffer_load_dwordx4 a[184:187], %[l16], %[rs], %[wa] offen sc1\n\t" \
  "ds_write_b128 %[lw], a[112:115] offset:28672\n\t" \
  "ds_write_b128 %[lw], a[116:119] offset:29696\n\t" \
  "s_add_u32 %[wb], %[wb], 4096\n\t" \
  "buffer_load_dwordx4 a[188:191], %[l16], %[rs], %[wb] offen sc1\n\t" \
  "ds_write_b128 %[lw], a[120:123] offset:30720\n\t" \
  "ds_write_b128 %[lw], a[124:127] offset:31744\n\t" \
  "s_waitcnt lgkmcnt(0)\n\t"
#define W2F_ASM \
  "s_nop 4\n\t" \
  "s_add_u32 %[wa], %[wa], 24576\n\t" \
  "buffer_load_dwordx4 a[0:3], %[l16], %[rs], %[wa] offen offset:1024 sc1\n\t" \
  "s_add_u32 %[wb], %[wb], 24576\n\t" \
  "buffer_load_dwordx4 a[4:7], %[l16], %[rs], %[wb] offen offset:1024 sc1\n\t" \
  "buffer_load_dwordx4 a[8:11], %[l16], %[rs], %[wa] offen offset:2048 sc1\n\t" \
  "buffer_load_dwordx4 a[12:15], %[l16], %[rs], %[wb] offen offset:2048 sc1\n\t" \
  "buffer_load_dwordx4 a[16:19], %[l16], %[rs], %[wa] offen offset:3072 sc1\n\t" \
  "buffer_load_dwordx4 a[20:23], %[l16], %[rs], %[wb] offen offset:3072 sc1\n\t"
#define F2_ASM \
  "s_nop 4\n\t" \
  "s_mov_b32 %[t0], %[sha]\n\t" \
  "s_mov_b32 %[t1], %[shb]\n\t" \
  "buffer_load_dwordx4 a[64:67], %[l16], %[rs], %[t0] offen sc1\n\t" \
  "buffer_load_dwordx4 a[68:71], %[l16], %[rs], %[t1] offen sc1\n\t" \
  "buffer_load_dwordx4 a[72:75], %[l16], %[rs], %[t0] offen offset:1024 sc1\n\t" \
  "buffer_load_dwordx4 a[76:79], %[l16], %[rs], %[t1] offen offset:1024 sc1\n\t" \
  "buffer_load_dwordx4 a[80:83], %[l16], %[rs], %[t0] offen offset:2048 sc1\n\t" \
  "buffer_load_dwordx4 a[84:87], %[l16], %[rs], %[t1] offen offset:2048 sc1\n\t" \
  "buffer_load_dwordx4 a[88:91], %[l16], %[rs], %[t0] offen offset:3072 sc1\n\t" \
  "buffer_load_dwordx4 a[92:95], %[l16], %[rs], %[t1] offen offset:3072 sc1\n\t" \
  "s_add_u32 %[t0], %[t0], 4096\n\t" \
  "s_add_u32 %[t1], %[t1], 4096\n\t" \
  "buffer_load_dwordx4 a[96:99], %[l16], %[rs], %[t0] offen sc1\n\t" \
  "buffer_load_dwordx4 a[100:103], %[l16], %[rs], %[t1] offen sc1\n\t" \
  "buffer_load_dwordx4 a[104:107], %[l16], %[rs], %[t0] offen offset:1024 sc1\n\t" \
  "buffer_load_dwordx4 a[108:111], %[l16], %[rs], %[t1] offen offset:1024 sc1\n\t" \
  "buffer_load_dwordx4 a[112:115], %[l16], %[rs], %[t0] offen offset:2048 sc1\n\t" \
  "buffer_load_dwordx4 a[116:119], %[l16], %[rs], %[t1] offen offset:2048 sc1\n\t" \
  "s_waitcnt vmcnt(12)\n\t" \
  "v_mfma_f32_32x32x16_bf16 v[96:111], v[120:123], a[68:71], 0\n\t" \
  "v_mfma_f32_32x32x16_bf16 v[192:207], v[124:127], a[64:67], 0\n\t" \
  "buffer_load_dwordx4 a[120:123], %[l16], %[rs], %[t0] offen offset:3072 sc1\n\t" \
  "v_mfma_f32_32x32x16_bf16 v[208:223], v[124:127], a[68:71], 0\n\t" \
  "v_mfma_f32_32x32x16_bf16 v[80:95], v[120:123], a[64:67], 0\n\t" \
  "buffer_load_dwordx4 a[124:127], %[l16], %[rs], %[t1] offen offset:3072 sc1\n\t" \
  "s_waitcnt vmcnt(12)\n\t" \
  "v_mfma_f32_32x32x16_bf16 v[96:111], v[128:131], a[76:79], v[96:111]\n\t" \
  "v_mfma_f32_32x32x16_bf16 v[192:207], v[132:135], a[72:75], v[192:207]\n\t" \
  "s_add_u32 %[t0], %[t0], 4096\n\t" \
  "s_add_u32 %[t1], %[t1], 4096\n\t" \
  "buffer_load_dwordx4 a[64:67], %[l16], %[rs], %[t0] offen sc1\n\t" \
  "v_mfma_f32_32x32x16_bf16 v[208:223], v[132:135], a[76:79], v[208:223]\n\t" \
  "v_mfma_f32_32x32x16_bf16 v[80:95], v[128:131], a[72:75], v[80:95]\n\t" \
  "buffer_load_dwordx4 a[68:71], %[l16], %[rs], %[t1] offen sc1\n\t" \
  "s_waitcnt vmcnt(12)\n\t" \
  "v_mfma_f32_32x32x16_bf16 v[96:111], v[136:139], a[84:87], v[96:111]\n\t" \
  "v_mfma_f32_32x32x16_bf16 v[192:207], v[140:143], a[80:83], v[192:207]\n\t" \
  "buffer_load_dwordx4 a[72:75], %[l16], %[rs], %[t0] offen offset:1024 sc1\n\t" \
  "v_mfma_f32_32x32x16_bf16 v[208:223], v[140:143], a[84:87], v[208:223]\n\t" \
  "v_mfma_f32_32x32x16_bf16 v[80:95], v[136:139], a[80:83], v[80:95]\n\t" \
  "buffer_load_dwordx4 a[76:79], %[l16], %[rs], %[t1] offen offset:1024 sc1\n\t" \
  "s_waitcnt vmcnt(12)\n\t" \
  "v_mfma_f32_32x32x16_bf16 v[96:111], v[144:147], a[92:95], v[96:111]\n\t" \
  "v_mfma_f32_32x32x16_bf16 v[192:207], v[148:151], a[88:91], v[192:207]\n\t" \
  "buffer_load_dwordx4 a[80:83], %[l16], %[rs], %[t0] offen offset:2048 sc1\n\t" \
  "v_mfma_f32_32x32x16_bf16 v[208:223], v[148:151], a[92:95], v[208:223]\n\t" \
  "v_mfma_f32_32x32x16_bf16 v[80:95], v[144:147], a[88:91], v[80:95]\n\t" \
  "buffer_load_dwordx4 a[84:87], %[l16], %[rs], %[t1] offen offset:2048 sc1\n\t" \
  "s_waitcnt vmcnt(12)\n\t" \
  "v_mfma_f32_32x32x16_bf16 v[96:111], v[152:155], a[100:103], v[96:111]\n\t" \
  "v_mfma_f32_32x32x16_bf16 v[192:207], v[156:159], a[96:99], v[192:207]\n\t" \
  "buffer_load_dwordx4 a[88:91], %[l16], %[rs], %[t0] offen offset:3072 sc1\n\t" \
  "v_mfma_f32_32x32x16_bf16 v[208:223], v[156:159], a[100:103], v[208:223]\n\t" \
  "v_mfma_f32_32x32x16_bf16 v[80:95], v[152:155], a[96:99], v[80:95]\n\t" \
  "buffer_load_dwordx4 a[92:95], %[l16], %[rs], %[t1] offen offset:3072 sc1\n\t" \
  "s_waitcnt vmcnt(12)\n\t" \
  "v_mfma_f32_32x32x16_bf16 v[96:111], v[160:163], a[108:111], v[96:111]\n\t" \
  "v_mfma_f32_32x32x16_bf16 v[192:207], v[164:167], a[104:107], v[192:207]\n\t" \
  "s_add_u32 %[t0], %[t0], 4096\n\t" \
  "s_add_u32 %[t1], %[t1], 4096\n\t" \
  "buffer_load_dwordx4 a[96:99], %[l16], %[rs], %[t0] offen sc1\n\t" \
  "v_mfma_f32_32x32x16_bf16 v[208:223], v[164:167], a[108:111], v[208:223]\n\t" \
  "v_mfma_f32_32x32x16_bf16 v[80:95], v[160:163], a[104:107], v[80:95]\n\t" \
  "buffer_load_dwordx4 a[100:103], %[l16], %[rs], %[t1] offen sc1\n\t" \
  "s_waitcnt vmcnt(12)\n\t" \
  "v_mfma_f32_32x32x16_bf16 v[96:111], v[168:171], a[116:119], v[96:111]\n\t" \
  "v_mfma_f32_32x32x16_bf16 v[192:207], v[172:175], a[112:115], v[192:207]\n\t" \
  "buffer_load_dwordx4 a[104:107], %[l16], %[rs], %[t0] offen offset:1024 sc1\n\t" \
  "v_mfma_f32_32x32x16_bf16 v[208:223], v[172:175], a[116:119], v[208:223]\n\t" \
  "v_mfma_f32_32x32x16_bf16 v[80:95], v[168:171], a[112:115], v[80:95]\n\t" \
  "buffer_load_dwordx4 a[108:111], %[l16], %[rs], %[t1] offen offset:1024 sc1\n\t" \
  "s_waitcnt vmcnt(12)\n\t" \
  "v_mfma_f32_32x32x16_bf16 v[96:111], v[176:179], a[124:127], v[96:111]\n\t" \
  "v_mfma_f32_32x32x16_bf16 v[192:207], v[180:183], a[120:123], v[192:207]\n\t" \
  "buffer_load_dwordx4 a[112:115], %[l16], %[rs], %[t0] offen offset:2048 sc1\n\t" \
  "v_mfma_f32_32x32x16_bf16 v[208:223], v[180:183], a[124:127], v[208:223]\n\t" \
  "v_mfma_f32_32x32x16_bf16 v[80:95], v[176:179], a[120:123], v[80:95]\n\t" \
  "buffer_load_dwordx4 a[116:119], %[l16], %[rs], %[t1] offen offset:2048 sc1\n\t" \
  "s_waitcnt vmcnt(12)\n\t" \
  "v_mfma_f32_32x32x16_bf16 v[96:111], v[184:187], a[68:71], v[96:111]\n\t" \
  "v_mfma_f32_32x32x16_bf16 v[192:207], v[188:191], a[64:67], v[192:207]\n\t" \
  "buffer_load_dwordx4 a[120:123], %[l16], %[rs], %[t0] offen offset:3072 sc1\n\t" \
  "v_mfma_f32_32x32x16_bf16 v[208:223], v[188:191], a[68:71], v[208:223]\n\t" \
  "v_mfma_f32_32x32x16_bf16 v[80:95], v[184:187], a[64:67], v[80:95]\n\t" \
  "buffer_load_dwordx4 a[124:127], %[l16], %[rs], %[t1] offen offset:3072 sc1\n\t" \
  "s_waitcnt vmcnt(12)\n\t" \
  "v_mfma_f32_32x32x16_bf16 v[96:111], a[192:195], a[76:79], v[96:111]\n\t" \
  "v_mfma_f32_32x32x16_bf16 v[192:207], a[196:199], a[72:75], v[192:207]\n\t" \
  "s_add_u32 %[t0], %[t0], 4096\n\t" \
  "s_add_u32 %[t1], %[t1], 4096\n\t" \
  "buffer_load_dwordx4 a[64:67], %[l16], %[rs], %[t0] offen sc1\n\t" \
  "v_mfma_f32_32x32x16_bf16 v[208:223], a[196:199], a[76:79], v[208:223]\n\t" \
  "v_mfma_f32_32x32x16_bf16 v[80:95], a[192:195], a[72:75], v[80:95]\n\t" \
  "buffer_load_dwordx4 a[68:71], %[l16], %[rs], %[t1] offen sc1\n\t" \
  "s_waitcnt vmcnt(12)\n\t" \
  "v_mfma_f32_32x32x16_bf16 v[96:111], a[200:203], a[84:87], v[96:111]\n\t" \
  "v_mfma_f32_32x32x16_bf16 v[192:207], a[204:207], a[80:83], v[192:207]\n\t" \
  "buffer_load_dwordx4 a[72:75], %[l16], %[rs], %[t0] offen offset:1024 sc1\n\t" \
  "v_mfma_f32_32x32x16_bf16 v[208:223], a[204:207], a[84:87], v[208:223]\n\t" \
  "v_mfma_f32_32x32x16_bf16 v[80:95], a[200:203], a[80:83], v[80:95]\n\t" \
  "buffer_load_dwordx4 a[76:79], %[l16], %[rs], %[t1] offen offset:1024 sc1\n\t" \
  "s_waitcnt vmcnt(12)\n\t" \
  "v_mfma_f32_32x32x16_bf16 v[96:111], a[208:211], a[92:95], v[96:111]\n\t" \
  "v_mfma_f32_32x32x16_bf16 v[192:207], a[212:215], a[88:91], v[192:207]\n\t" \
  "buffer_load_dwordx4 a[80:83], %[l16], %[rs], %[t0] offen offset:2048 sc1\n\t" \
  "v_mfma_f32_32x32x16_bf16 v[208:223], a[212:215], a[92:95], v[208:223]\n\t" \
  "v_mfma_f32_32x32x16_bf16 v[80:95], a[208:211], a[88:91], v[80:95]\n\t" \
  "buffer_load_dwordx4 a[84:87], %[l16], %[rs], %[t1] offen offset:2048 sc1\n\t" \
  "s_waitcnt vmcnt(12)\n\t" \
  "v_mfma_f32_32x32x16_bf16 v[96:111], a[216:219], a[100:103], v[96:111]\n\t" \
  "v_mfma_f32_32x32x16_bf16 v[192:207], a[220:223], a[96:99], v[192:207]\n\t" \
  "buffer_load_dwordx4 a[88:91], %[l16], %[rs], %[t0] offen offset:3072 sc1\n\t" \
  "v_mfma_f32_32x32x16_bf16 v[208:223], a[220:223], a[100:103], v[208:223]\n\t" \
  "v_mfma_f32_32x32x16_bf16 v[80:95], a[216:219], a[96:99], v[80:95]\n\t" \
  "buffer_load_dwordx4 a[92:95], %[l16], %[rs], %[t1] offen offset:3072 sc1\n\t" \
  "s_waitcnt vmcnt(12)\n\t" \
  "v_mfma_f32_32x32x16_bf16 v[96:111], a[224:227], a[108:111], v[96:111]\n\t" \
  "v_mfma_f32_32x32x16_bf16 v[192:207], a[228:231], a[104:107], v[192:207]\n\t" \
  "s_add_u32 %[t0], %[t0], 4096\n\t" \
  "s_add_u32 %[t1], %[t1], 4096\n\t" \
  "buffer_load_dwordx4 a[96:99], %[l16], %[rs], %[t0] offen sc1\n\t" \
  "v_mfma_f32_32x32x16_bf16 v[208:223], a[228:231], a[108:111], v[208:223]\n\t" \
  "v_mfma_f32_32x32x16_bf16 v[80:95], a[224:227], a[104:107], v[80:95]\n\t" \
  "buffer_load_dwordx4 a[100:103], %[l16], %[rs], %[t1] offen sc1\n\t" \
  "s_waitcnt vmcnt(12)\n\t" \
  "v_mfma_f32_32x32x16_bf16 v[96:111], a[232:235], a[116:119], v[96:111]\n\t" \
  "v_mfma_f32_32x32x16_bf16 v[192:207], a[236:239], a[112:115], v[192:207]\n\t" \
  "buffer_load_dwordx4 a[104:107], %[l16], %[rs], %[t0] offen offset:1024 sc1\n\t" \
  "v_mfma_f32_32x32x16_bf16 v[208:223], a[236:239], a[116:119], v[208:223]\n\t" \
  "v_mfma_f32_32x32x16_bf16 v[80:95], a[232:235], a[112:115], v[80:95]\n\t" \
  "buffer_load_dwordx4 a[108:111], %[l16], %[rs], %[t1] offen offset:1024 sc1\n\t" \
  "s_waitcnt vmcnt(12)\n\t" \
  "v_mfma_f32_32x32x16_bf16 v[96:111], a[240:243], a[124:127], v[96:111]\n\t" \
  "v_mfma_f32_32x32x16_bf16 v[192:207], a[244:247], a[120:123], v[192:207]\n\t" \
  "buffer_load_dwordx4 a[112:115], %[l16], %[rs], %[t0] offen offset:2048 sc1\n\t" \
  "v_mfma_f32_32x32x16_bf16 v[208:223], a[244:247], a[124:127], v[208:223]\n\t" \
  "v_mfma_f32_32x32x16_bf16 v[80:95], a[240:243], a[120:123], v[80:95]\n\t" \
  "buffer_load_dwordx4 a[116:119], %[l16], %[rs], %[t1] offen offset:2048 sc1\n\t" \
  "s_waitcnt vmcnt(12)\n\t" \
  "v_mfma_f32_32x32x16_bf16 v[96:111], a[248:251], a[68:71], v[96:111]\n\t" \
  "v_mfma_f32_32x32x16_bf16 v[192:207], a[252:255], a[64:67], v[192:207]\n\t" \
  "buffer_load_dwordx4 a[120:123], %[l16], %[rs], %[t0] offen offset:3072 sc1\n\t" \
  "v_mfma_f32_32x32x16_bf16 v[208:223], a[252:255], a[68:71], v[208:223]\n\t" \
  "v_mfma_f32_32x32x16_bf16 v[80:95], a[248:251], a[64:67], v[80:95]\n\t" \
  "buffer_load_dwordx4 a[124:127], %[l16], %[rs], %[t1] offen offset:3072 sc1\n\t" \
  "s_waitcnt vmcnt(12)\n\t" \
  "v_mfma_f32_32x32x16_bf16 v[96:111], a[128:131], a[76:79], v[96:111]\n\t" \
  "v_mfma_f32_32x32x16_bf16 v[192:207], a[132:135], a[72:75], v[192:207]\n\t" \
  "s_add_u32 %[t0], %[t0], 4096\n\t" \
  "s_add_u32 %[t1], %[t1], 4096\n\t" \
  "buffer_load_dwordx4 a[64:67], %[l16], %[rs], %[t0] offen sc1\n\t" \
  "v_mfma_f32_32x32x16_bf16 v[208:223], a[132:135], a[76:79], v[208:223]\n\t" \
  "v_mfma_f32_32x32x16_bf16 v[80:95], a[128:131], a[72:75], v[80:95]\n\t" \
  "buffer_load_dwordx4 a[68:71], %[l16], %[rs], %[t1] offen sc1\n\t" \
  "s_waitcnt vmcnt(12)\n\t" \
  "v_mfma_f32_32x32x16_bf16 v[96:111], a[136:139], a[84:87], v[96:111]\n\t" \
  "v_mfma_f32_32x32x16_bf16 v[192:207], a[140:143], a[80:83], v[192:207]\n\t" \
  "buffer_load_dwordx4 a[72:75], %[l16], %[rs], %[t0] offen offset:1024 sc1\n\t" \
  "v_mfma_f32_32x32x16_bf16 v[208:223], a[140:143], a[84:87], v[208:223]\n\t" \
  "v_mfma_f32_32x32x16_bf16 v[80:95], a[136:139], a[80:83], v[80:95]\n\t" \
  "buffer_load_dwordx4 a[76:79], %[l16], %[rs], %[t1] offen offset:1024 sc1\n\t" \
  "s_waitcnt vmcnt(12)\n\t" \
  "v_mfma_f32_32x32x16_bf16 v[96:111], a[144:147], a[92:95], v[96:111]\n\t" \
  "v_mfma_f32_32x32x16_bf16 v[192:207], a[148:151], a[88:91], v[192:207]\n\t" \
  "buffer_load_dwordx4 a[80:83], %[l16], %[rs], %[t0] offen offset:2048 sc1\n\t" \
  "v_mfma_f32_32x32x16_bf16 v[208:223], a[148:151], a[92:95], v[208:223]\n\t" \
  "v_mfma_f32_32x32x16_bf16 v[80:95], a[144:147], a[88:91], v[80:95]\n\t" \
  "buffer_load_dwordx4 a[84:87], %[l16], %[rs], %[t1] offen offset:2048 sc1\n\t" \
  "s_waitcnt vmcnt(12)\n\t" \
  "v_mfma_f32_32x32x16_bf16 v[96:111], a[152:155], a[100:103], v[96:111]\n\t" \
  "v_mfma_f32_32x32x16_bf16 v[192:207], a[156:159], a[96:99], v[192:207]\n\t" \
  "buffer_load_dwordx4 a[88:91], %[l16], %[rs], %[t0] offen offset:3072 sc1\n\t" \
  "v_mfma_f32_32x32x16_bf16 v[208:223], a[156:159], a[100:103], v[208:223]\n\t" \
  "v_mfma_f32_32x32x16_bf16 v[80:95], a[152:155], a[96:99], v[80:95]\n\t" \
  "buffer_load_dwordx4 a[92:95], %[l16], %[rs], %[t1] offen offset:3072 sc1\n\t" \
  "s_waitcnt vmcnt(12)\n\t" \
  "v_mfma_f32_32x32x16_bf16 v[96:111], a[160:163], a[108:111], v[96:111]\n\t" \
  "v_mfma_f32_32x32x16_bf16 v[192:207], a[164:167], a[104:107], v[192:207]\n\t" \
  "v_mfma_f32_32x32x16_bf16 v[208:223], a[164:167], a[108:111], v[208:223]\n\t" \
  "v_mfma_f32_32x32x16_bf16 v[80:95], a[160:163], a[104:107], v[80:95]\n\t" \
  "s_waitcnt vmcnt(10)\n\t" \
  "v_mfma_f32_32x32x16_bf16 v[96:111], a[168:171], a[116:119], v[96:111]\n\t" \
  "v_mfma_f32_32x32x16_bf16 v[192:207], a[172:175], a[112:115], v[192:207]\n\t" \
  "v_mfma_f32_32x32x16_bf16 v[208:223], a[172:175], a[116:119], v[208:223]\n\t" \
  "v_mfma_f32_32x32x16_bf16 v[80:95], a[168:171], a[112:115], v[80:95]\n\t" \
  "s_waitcnt vmcnt(8)\n\t" \
  "v_mfma_f32_32x32x16_bf16 v[96:111], a[176:179], a[124:127], v[96:111]\n\t" \
  "v_mfma_f32_32x32x16_bf16 v[192:207], a[180:183], a[120:123], v[192:207]\n\t" \
  "v_mfma_f32_32x32x16_bf16 v[208:223], a[180:183], a[124:127], v[208:223]\n\t" \
  "v_mfma_f32_32x32x16_bf16 v[80:95], a[176:179], a[120:123], v[80:95]\n\t" \
  "s_waitcnt vmcnt(6)\n\t" \
  "v_mfma_f32_32x32x16_bf16 v[96:111], a[184:187], a[68:71], v[96:111]\n\t" \
  "v_mfma_f32_32x32x16_bf16 v[192:207], a[188:191], a[64:67], v[192:207]\n\t" \
  "v_mfma_f32_32x32x16_bf16 v[208:223], a[188:191], a[68:71], v[208:223]\n\t" \
  "v_mfma_f32_32x32x16_bf16 v[80:95], a[184:187], a[64:67], v[80:95]\n\t" \
  "s_waitcnt vmcnt(4)\n\t" \
  "v_mfma_f32_32x32x16_bf16 v[96:111], a[0:3], a[76:79], v[96:111]\n\t" \
  "v_mfma_f32_32x32x16_bf16 v[192:207], a[4:7], a[72:75], v[192:207]\n\t" \
  "v_mfma_f32_32x32x16_bf16 v[208:223], a[4:7], a[76:79], v[208:223]\n\t" \
  "v_mfma_f32_32x32x16_bf16 v[80:95], a[0:3], a[72:75], v[80:95]\n\t" \
  "s_waitcnt vmcnt(2)\n\t" \
  "v_mfma_f32_32x32x16_bf16 v[96:111], a[8:11], a[84:87], v[96:111]\n\t" \
  "v_mfma_f32_32x32x16_bf16 v[192:207], a[12:15], a[80:83], v[192:207]\n\t" \
  "v_mfma_f32_32x32x16_bf16 v[208:223], a[12:15], a[84:87], v[208:223]\n\t" \
  "v_mfma_f32_32x32x16_bf16 v[80:95], a[8:11], a[80:83], v[80:95]\n\t" \
  "s_waitcnt vmcnt(0)\n\t" \
  "v_mfma_f32_32x32x16_bf16 v[96:111], a[16:19], a[92:95], v[96:111]\n\t" \
  "v_mfma_f32_32x32x16_bf16 v[192:207], a[20:23], a[88:91], v[192:207]\n\t" \
  "v_mfma_f32_32x32x16_bf16 v[208:223], a[20:23], a[92:95], v[208:223]\n\t" \
  "v_mfma_f32_32x32x16_bf16 v[80:95], a[16:19], a[88:91], v[80:95]\n\t" \
  "s_nop 7\n\t" \
  "s_nop 0\n\t" \
  "ds_write_b128 %[r1], v[96:99]\n\t" \
  "ds_write_b128 %[r1], v[100:103] offset:1024\n\t" \
  "ds_write_b128 %[r1], v[104:107] offset:2048\n\t" \
  "ds_write_b128 %[r1], v[108:111] offset:3072\n\t" \
  "ds_write_b128 %[r2], v[192:195]\n\t" \
  "ds_write_b128 %[r2], v[196:199] offset:1024\n\t" \
  "ds_write_b128 %[r2], v[200:203] offset:2048\n\t" \
  "ds_write_b128 %[r2], v[204:207] offset:3072\n\t" \
  "ds_write_b128 %[r3], v[208:211]\n\t" \
  "ds_write_b128 %[r3], v[212:215] offset:1024\n\t" \
  "ds_write_b128 %[r3], v[216:219] offset:2048\n\t" \
  "ds_write_b128 %[r3], v[220:223] offset:3072\n\t" \
  "s_waitcnt lgkmcnt(0)\n\t" \
  "s_barrier\n\t" \
  "ds_read_b128 v[96:99], %[rr] offset:0\n\t" \
  "ds_read_b128 v[100:103], %[rr] offset:1024\n\t" \
  "ds_read_b128 v[104:107], %[rr] offset:2048\n\t" \
  "ds_read_b128 v[108:111], %[rr] offset:3072\n\t" \
  "ds_read_b128 v[192:195], %[rr] offset:4096\n\t" \
  "ds_read_b128 v[196:199], %[rr] offset:5120\n\t" \
  "ds_read_b128 v[200:203], %[rr] offset:6144\n\t" \
  "ds_read_b128 v[204:207], %[rr] offset:7168\n\t" \
  "ds_read_b128 v[208:211], %[rr] offset:8192\n\t" \
  "ds_read_b128 v[212:215], %[rr] offset:9216\n\t" \
  "ds_read_b128 v[216:219], %[rr] offset:10240\n\t" \
  "ds_read_b128 v[220:223], %[rr] offset:11264\n\t" \
  "s_waitcnt lgkmcnt(11)\n\t" \
  "v_add_f32 v80, v80, v96\n\t" \
  "v_add_f32 v81, v81, v97\n\t" \
  "v_add_f32 v82, v82, v98\n\t" \
  "v_add_f32 v83, v83, v99\n\t" \
  "s_waitcnt lgkmcnt(10)\n\t" \
  "v_add_f32 v84, v84, v100\n\t" \
  "v_add_f32 v85, v85, v101\n\t" \
  "v_add_f32 v86, v86, v102\n\t" \
  "v_add_f32 v87, v87, v103\n\t" \
  "s_waitcnt lgkmcnt(9)\n\t" \
  "v_add_f32 v88, v88, v104\n\t" \
  "v_add_f32 v89, v89, v105\n\t" \
  "v_add_f32 v90, v90, v106\n\t" \
  "v_add_f32 v91, v91, v107\n\t" \
  "s_waitcnt lgkmcnt(8)\n\t" \
  "v_add_f32 v92, v92, v108\n\t" \
  "v_add_f32 v93, v93, v109\n\t" \
  "v_add_f32 v94, v94, v110\n\t" \
  "v_add_f32 v95, v95, v111\n\t" \
  "s_waitcnt lgkmcnt(7)\n\t" \
  "v_add_f32 v80, v80, v192\n\t" \
  "v_add_f32 v81, v81, v193\n\t" \
  "v_add_f32 v82, v82, v194\n\t" \
  "v_add_f32 v83, v83, v195\n\t" \
  "s_waitcnt lgkmcnt(6)\n\t" \
  "v_add_f32 v84, v84, v196\n\t" \
  "v_add_f32 v85, v85, v197\n\t" \
  "v_add_f32 v86, v86, v198\n\t" \
  "v_add_f32 v87, v87, v199\n\t" \
  "s_waitcnt lgkmcnt(5)\n\t" \
  "v_add_f32 v88, v88, v200\n\t" \
  "v_add_f32 v89, v89, v201\n\t" \
  "v_add_f32 v90, v90, v202\n\t" \
  "v_add_f32 v91, v91, v203\n\t" \
  "s_waitcnt lgkmcnt(4)\n\t" \
  "v_add_f32 v92, v92, v204\n\t" \
  "v_add_f32 v93, v93, v205\n\t" \
  "v_add_f32 v94, v94, v206\n\t" \
  "v_add_f32 v95, v95, v207\n\t" \
  "s_waitcnt lgkmcnt(3)\n\t" \
  "v_add_f32 v80, v80, v208\n\t" \
  "v_add_f32 v81, v81, v209\n\t" \
  "v_add_f32 v82, v82, v210\n\t" \
  "v_add_f32 v83, v83, v211\n\t" \
  "s_waitcnt lgkmcnt(2)\n\t" \
  "v_add_f32 v84, v84, v212\n\t" \
  "v_add_f32 v85, v85, v213\n\t" \
  "v_add_f32 v86, v86, v214\n\t" \
  "v_add_f32 v87, v87, v215\n\t" \
  "s_waitcnt lgkmcnt(1)\n\t" \
  "v_add_f32 v88, v88, v216\n\t" \
  "v_add_f32 v89, v89, v217\n\t" \
  "v_add_f32 v90, v90, v218\n\t" \
  "v_add_f32 v91, v91, v219\n\t" \
  "s_waitcnt lgkmcnt(0)\n\t" \
  "v_add_f32 v92, v92, v220\n\t" \
  "v_add_f32 v93, v93, v221\n\t" \
  "v_add_f32 v94, v94, v222\n\t" \
  "v_add_f32 v95, v95, v223\n\t" \
  "v_fmac_f32 v64, v119, v80\n\t" \
  "v_fmac_f32 v65, v119, v81\n\t" \
  "v_fmac_f32 v66, v119, v82\n\t" \
  "v_fmac_f32 v67, v119, v83\n\t" \
  "v_fmac_f32 v68, v119, v84\n\t" \
  "v_fmac_f32 v69, v119, v85\n\t" \
  "v_fmac_f32 v70, v119, v86\n\t" \
  "v_fmac_f32 v71, v119, v87\n\t" \
  "v_fmac_f32 v72, v119, v88\n\t" \
  "v_fmac_f32 v73, v119, v89\n\t" \
  "v_fmac_f32 v74, v119, v90\n\t" \
  "v_fmac_f32 v75, v119, v91\n\t" \
  "v_fmac_f32 v76, v119, v92\n\t" \
  "v_fmac_f32 v77, v119, v93\n\t" \
  "v_fmac_f32 v78, v119, v94\n\t" \
  "v_fmac_f32 v79, v119, v95\n\t"
#define STOREX_ASM \
  "s_nop 4\n\t" \
  "v_cvt_pk_bf16_f32 v224, v64, v65\n\t" \
  "v_cvt_pk_bf16_f32 v225, v66, v67\n\t" \
  "v_cvt_pk_bf16_f32 v226, v68, v69\n\t" \
  "v_cvt_pk_bf16_f32 v227, v70, v71\n\t" \
  "buffer_store_dwordx4 v[224:227], %[l16], %[rs], %[sx] offen sc1\n\t" \
  "v_cvt_pk_bf16_f32 v228, v72, v73\n\t" \
  "v_cvt_pk_bf16_f32 v229, v74, v75\n\t" \
  "v_cvt_pk_bf16_f32 v230, v76, v77\n\t" \
  "v_cvt_pk_bf16_f32 v231, v78, v79\n\t" \
  "buffer_store_dwordx4 v[228:231], %[l16], %[rs], %[sx] offen offset:1024 sc1\n\t" \
  "v_add_f32 v232, v64, v65\n\t" \
  "v_add_f32 v233, v66, v67\n\t" \
  "v_add_f32 v232, v232, v68\n\t" \
  "v_add_f32 v233, v233, v69\n\t" \
  "v_add_f32 v232, v232, v70\n\t" \
  "v_add_f32 v233, v233, v71\n\t" \
  "v_add_f32 v232, v232, v72\n\t" \
  "v_add_f32 v233, v233, v73\n\t" \
  "v_add_f32 v232, v232, v74\n\t" \
  "v_add_f32 v233, v233, v75\n\t" \
  "v_add_f32 v232, v232, v76\n\t" \
  "v_add_f32 v233, v233, v77\n\t" \
  "v_add_f32 v232, v232, v78\n\t" \
  "v_add_f32 v233, v233, v79\n\t" \
  "v_add_f32 v232, v232, v233\n\t" \
  "v_mov_b32 v233, v232\n\t" \
  "s_nop 1\n\t" \
  "v_permlane32_swap_b32 v232, v233\n\t" \
  "v_add_f32 v232, v232, v233\n\t" \
  "v_mul_f32 v234, 0x3d000000, v232\n\t" \
  "v_sub_f32 v238, v64, v234\n\t" \
  "v_sub_f32 v239, v65, v234\n\t" \
  "v_fma_f32 v235, v238, v238, 0\n\t" \
  "v_fma_f32 v236, v239, v239, 0\n\t" \
  "v_sub_f32 v238, v66, v234\n\t" \
  "v_sub_f32 v239, v67, v234\n\t" \
  "v_fma_f32 v235, v238, v238, v235\n\t" \
  "v_fma_f32 v236, v239, v239, v236\n\t" \
  "v_sub_f32 v238, v68, v234\n\t" \
  "v_sub_f32 v239, v69, v234\n\t" \
  "v_fma_f32 v235, v238, v238, v235\n\t" \
  "v_fma_f32 v236, v239, v239, v236\n\t" \
  "v_sub_f32 v238, v70, v234\n\t" \
  "v_sub_f32 v239, v71, v234\n\t" \
  "v_fma_f32 v235, v238, v238, v235\n\t" \
  "v_fma_f32 v236, v239, v239, v236\n\t" \
  "v_sub_f32 v238, v72, v234\n\t" \
  "v_sub_f32 v239, v73, v234\n\t" \
  "v_fma_f32 v235, v238, v238, v235\n\t" \
  "v_fma_f32 v236, v239, v239, v236\n\t" \
  "v_sub_f32 v238, v74, v234\n\t" \
  "v_sub_f32 v239, v75, v234\n\t" \
  "v_fma_f32 v235, v238, v238, v235\n\t" \
  "v_fma_f32 v236, v239, v239, v236\n\t" \
  "v_sub_f32 v238, v76, v234\n\t" \
  "v_sub_f32 v239, v77, v234\n\t" \
  "v_fma_f32 v235, v238, v238, v235\n\t" \
  "v_fma_f32 v236, v239, v239, v236\n\t" \
  "v_sub_f32 v238, v78, v234\n\t" \
  "v_sub_f32 v239, v79, v234\n\t" \
  "v_fma_f32 v235, v238, v238, v235\n\t" \
  "v_fma_f32 v236, v239, v239, v236\n\t" \
  "v_add_f32 v235, v235, v236\n\t" \
  "v_mov_b32 v236, v235\n\t" \
  "s_nop 1\n\t" \
  "v_permlane32_swap_b32 v235, v236\n\t" \
  "v_add_f32 v235, v235, v236\n\t" \
  "buffer_store_dwordx2 v[234:235], %[vst], %[rs], %[sxs] offen sc1\n\t"
#define OUT_ASM \
  "s_nop 4\n\t" \
  "v_cvt_pk_bf16_f32 v224, v64, v65\n\t" \
  "v_cvt_pk_bf16_f32 v225, v66, v67\n\t" \
  "v_cvt_pk_bf16_f32 v226, v68, v69\n\t" \
  "v_cvt_pk_bf16_f32 v227, v70, v71\n\t" \
  "v_cvt_pk_bf16_f32 v228, v72, v73\n\t" \
  "v_cvt_pk_bf16_f32 v229, v74, v75\n\t" \
  "v_cvt_pk_bf16_f32 v230, v76, v77\n\t" \
  "v_cvt_pk_bf16_f32 v231, v78, v79\n\t" \
  "v_permlane32_swap_b32 v224, v226\n\t" \
  "v_permlane32_swap_b32 v225, v227\n\t" \
  "v_permlane32_swap_b32 v228, v230\n\t" \
  "v_permlane32_swap_b32 v229, v231\n\t" \
  "buffer_store_dwordx4 v[224:227], %[vo], %[ro], 0 offen\n\t" \
  "buffer_store_dwordx4 v[228:231], %[vo], %[ro], 0 offen offset:32\n\t"
#define P0_ISSUE_ASM \
  "s_nop 4\n\t" \
  "buffer_load_dwordx2 v[88:89], %[vz], %[rs], 0 offen sc1\n\t" \
  "buffer_load_dwordx2 v[80:81], %[vo], %[rx], 0 offen\n\t" \
  "buffer_load_dwordx2 v[82:83], %[vo], %[rx], 0 offen offset:16\n\t" \
  "buffer_load_dwordx2 v[84:85], %[vo], %[rx], 0 offen offset:32\n\t" \
  "buffer_load_dwordx2 v[86:87], %[vo], %[rx], 0 offen offset:48\n\t" \
  "buffer_load_dwordx4 v[96:99], %[vc], %[rc], %[tc] offen\n\t" \
  "s_add_u32 %[tc], %[tc], %[cstep]\n\t" \
  "buffer_load_dwordx4 v[100:103], %[vc], %[rc], %[tc] offen\n\t" \
  "s_add_u32 %[tc], %[tc], %[cstep]\n\t" \
  "buffer_load_dwordx4 v[104:107], %[vc], %[rc], %[tc] offen\n\t" \
  "s_add_u32 %[tc], %[tc], %[cstep]\n\t" \
  "buffer_load_dwordx4 v[108:111], %[vc], %[rc], %[tc] offen\n\t" \
  "s_add_u32 %[tc], %[tc], %[cstep]\n\t"
#define P0_COMP_ASM \
  "s_nop 4\n\t" \
  "s_waitcnt vmcnt(6)\n\t" \
  "v_readfirstlane_b32 %[tg0], v88\n\t" \
  "v_readfirstlane_b32 %[tg1], v89\n\t" \
  "v_lshlrev_b32 v64, 16, v80\n\t" \
  "v_and_b32 v65, 0xffff0000, v80\n\t" \
  "v_lshlrev_b32 v66, 16, v81\n\t" \
  "v_and_b32 v67, 0xffff0000, v81\n\t" \
  "v_lshlrev_b32 v68, 16, v82\n\t" \
  "v_and_b32 v69, 0xffff0000, v82\n\t" \
  "v_lshlrev_b32 v70, 16, v83\n\t" \
  "v_and_b32 v71, 0xffff0000, v83\n\t" \
  "v_lshlrev_b32 v72, 16, v84\n\t" \
  "v_and_b32 v73, 0xffff0000, v84\n\t" \
  "v_lshlrev_b32 v74, 16, v85\n\t" \
  "v_and_b32 v75, 0xffff0000, v85\n\t" \
  "v_lshlrev_b32 v76, 16, v86\n\t" \
  "v_and_b32 v77, 0xffff0000, v86\n\t" \
  "v_lshlrev_b32 v78, 16, v87\n\t" \
  "v_and_b32 v79, 0xffff0000, v87\n\t" \
  "v_cvt_pk_bf16_f32 v224, v64, v65\n\t" \
  "v_cvt_pk_bf16_f32 v225, v66, v67\n\t" \
  "v_cvt_pk_bf16_f32 v226, v68, v69\n\t" \
  "v_cvt_pk_bf16_f32 v227, v70, v71\n\t" \
  "buffer_store_dwordx4 v[224:227], %[l16], %[rs], %[sx] offen sc1\n\t" \
  "v_cvt_pk_bf16_f32 v228, v72, v73\n\t" \
  "v_cvt_pk_bf16_f32 v229, v74, v75\n\t" \
  "v_cvt_pk_bf16_f32 v230, v76, v77\n\t" \
  "v_cvt_pk_bf16_f32 v231, v78, v79\n\t" \
  "buffer_store_dwordx4 v[228:231], %[l16], %[rs], %[sx] offen offset:1024 sc1\n\t" \
  "v_add_f32 v232, v64, v65\n\t" \
  "v_add_f32 v233, v66, v67\n\t" \
  "v_add_f32 v232, v232, v68\n\t" \
  "v_add_f32 v233, v233, v69\n\t" \
  "v_add_f32 v232, v232, v70\n\t" \
  "v_add_f32 v233, v233, v71\n\t" \
  "v_add_f32 v232, v232, v72\n\t" \
  "v_add_f32 v233, v233, v73\n\t" \
  "v_add_f32 v232, v232, v74\n\t" \
  "v_add_f32 v233, v233, v75\n\t" \
  "v_add_f32 v232, v232, v76\n\t" \
  "v_add_f32 v233, v233, v77\n\t" \
  "v_add_f32 v232, v232, v78\n\t" \
  "v_add_f32 v233, v233, v79\n\t" \
  "v_add_f32 v232, v232, v233\n\t" \
  "v_mov_b32 v233, v232\n\t" \
  "s_nop 1\n\t" \
  "v_permlane32_swap_b32 v232, v233\n\t" \
  "v_add_f32 v232, v232, v233\n\t" \
  "v_mul_f32 v234, 0x3d000000, v232\n\t" \
  "v_sub_f32 v238, v64, v234\n\t" \
  "v_sub_f32 v239, v65, v234\n\t" \
  "v_fma_f32 v235, v238, v238, 0\n\t" \
  "v_fma_f32 v236, v239, v239, 0\n\t" \
  "v_sub_f32 v238, v66, v234\n\t" \
  "v_sub_f32 v239, v67, v234\n\t" \
  "v_fma_f32 v235, v238, v238, v235\n\t" \
  "v_fma_f32 v236, v239, v239, v236\n\t" \
  "v_sub_f32 v238, v68, v234\n\t" \
  "v_sub_f32 v239, v69, v234\n\t" \
  "v_fma_f32 v235, v238, v238, v235\n\t" \
  "v_fma_f32 v236, v239, v239, v236\n\t" \
  "v_sub_f32 v238, v70, v234\n\t" \
  "v_sub_f32 v239, v71, v234\n\t" \
  "v_fma_f32 v235, v238, v238, v235\n\t" \
  "v_fma_f32 v236, v239, v239, v236\n\t" \
  "v_sub_f32 v238, v72, v234\n\t" \
  "v_sub_f32 v239, v73, v234\n\t" \
  "v_fma_f32 v235, v238, v238, v235\n\t" \
  "v_fma_f32 v236, v239, v239, v236\n\t" \
  "v_sub_f32 v238, v74, v234\n\t" \
  "v_sub_f32 v239, v75, v234\n\t" \
  "v_fma_f32 v235, v238, v238, v235\n\t" \
  "v_fma_f32 v236, v239, v239, v236\n\t" \
  "v_sub_f32 v238, v76, v234\n\t" \
  "v_sub_f32 v239, v77, v234\n\t" \
  "v_fma_f32 v235, v238, v238, v235\n\t" \
  "v_fma_f32 v236, v239, v239, v236\n\t" \
  "v_sub_f32 v238, v78, v234\n\t" \
  "v_sub_f32 v239, v79, v234\n\t" \
  "v_fma_f32 v235, v238, v238, v235\n\t" \
  "v_fma_f32 v236, v239, v239, v236\n\t" \
  "v_add_f32 v235, v235, v236\n\t" \
  "v_mov_b32 v236, v235\n\t" \
  "s_nop 1\n\t" \
  "v_permlane32_swap_b32 v235, v236\n\t" \
  "v_add_f32 v235, v235, v236\n\t" \
  "buffer_store_dwordx2 v[234:235], %[vst], %[rs], %[sxs] offen sc1\n\t" \
  "v_lshlrev_b32 v224, 16, v96\n\t" \
  "v_and_b32 v225, 0xffff0000, v96\n\t" \
  "v_mov_b32 v128, v224\n\t" \
  "v_mov_b32 v129, v225\n\t" \
  "v_lshlrev_b32 v224, 16, v97\n\t" \
  "v_and_b32 v225, 0xffff0000, v97\n\t" \
  "v_mov_b32 v130, v224\n\t" \
  "v_mov_b32 v131, v225\n\t" \
  "v_lshlrev_b32 v224, 16, v98\n\t" \
  "v_and_b32 v225, 0xffff0000, v98\n\t" \
  "v_mov_b32 v132, v224\n\t" \
  "v_mov_b32 v133, v225\n\t" \
  "v_lshlrev_b32 v224, 16, v99\n\t" \
  "v_and_b32 v225, 0xffff0000, v99\n\t" \
  "v_mov_b32 v134, v224\n\t" \
  "v_mov_b32 v135, v225\n\t" \
  "v_lshlrev_b32 v224, 16, v100\n\t" \
  "v_and_b32 v225, 0xffff0000, v100\n\t" \
  "v_add_f32 v128, v128, v224\n\t" \
  "v_add_f32 v129, v129, v225\n\t" \
  "v_lshlrev_b32 v224, 16, v101\n\t" \
  "v_and_b32 v225, 0xffff0000, v101\n\t" \
  "v_add_f32 v130, v130, v224\n\t" \
  "v_add_f32 v131, v131, v225\n\t" \
  "v_lshlrev_b32 v224, 16, v102\n\t" \
  "v_and_b32 v225, 0xffff0000, v102\n\t" \
  "v_add_f32 v132, v132, v224\n\t" \
  "v_add_f32 v133, v133, v225\n\t" \
  "v_lshlrev_b32 v224, 16, v103\n\t" \
  "v_and_b32 v225, 0xffff0000, v103\n\t" \
  "v_add_f32 v134, v134, v224\n\t" \
  "v_add_f32 v135, v135, v225\n\t" \
  "v_lshlrev_b32 v224, 16, v104\n\t" \
  "v_and_b32 v225, 0xffff0000, v104\n\t" \
  "v_add_f32 v128, v128, v224\n\t" \
  "v_add_f32 v129, v129, v225\n\t" \
  "v_lshlrev_b32 v224, 16, v105\n\t" \
  "v_and_b32 v225, 0xffff0000, v105\n\t" \
  "v_add_f32 v130, v130, v224\n\t" \
  "v_add_f32 v131, v131, v225\n\t" \
  "v_lshlrev_b32 v224, 16, v106\n\t" \
  "v_and_b32 v225, 0xffff0000, v106\n\t" \
  "v_add_f32 v132, v132, v224\n\t" \
  "v_add_f32 v133, v133, v225\n\t" \
  "v_lshlrev_b32 v224, 16, v107\n\t" \
  "v_and_b32 v225, 0xffff0000, v107\n\t" \
  "v_add_f32 v134, v134, v224\n\t" \
  "v_add_f32 v135, v135, v225\n\t" \
  "v_lshlrev_b32 v224, 16, v108\n\t" \
  "v_and_b32 v225, 0xffff0000, v108\n\t" \
  "v_add_f32 v128, v128, v224\n\t" \
  "v_add_f32 v129, v129, v225\n\t" \
  "v_lshlrev_b32 v224, 16, v109\n\t" \
  "v_and_b32 v225, 0xffff0000, v109\n\t" \
  "v_add_f32 v130, v130, v224\n\t" \
  "v_add_f32 v131, v131, v225\n\t" \
  "v_lshlrev_b32 v224, 16, v110\n\t" \
  "v_and_b32 v225, 0xffff0000, v110\n\t" \
  "v_add_f32 v132, v132, v224\n\t" \
  "v_add_f32 v133, v133, v225\n\t" \
  "v_lshlrev_b32 v224, 16, v111\n\t" \
  "v_and_b32 v225, 0xffff0000, v111\n\t" \
  "v_add_f32 v134, v134, v224\n\t" \
  "v_add_f32 v135, v135, v225\n\t" \
  "v_add_f32_dpp v128, v128, v128 row_ror:4 row_mask:0xf bank_mask:0xf\n\t" \
  "v_add_f32_dpp v129, v129, v129 row_ror:4 row_mask:0xf bank_mask:0xf\n\t" \
  "v_add_f32_dpp v130, v130, v130 row_ror:4 row_mask:0xf bank_mask:0xf\n\t" \
  "v_add_f32_dpp v131, v131, v131 row_ror:4 row_mask:0xf bank_mask:0xf\n\t" \
  "v_add_f32_dpp v132, v132, v132 row_ror:4 row_mask:0xf bank_mask:0xf\n\t" \
  "v_add_f32_dpp v133, v133, v133 row_ror:4 row_mask:0xf bank_mask:0xf\n\t" \
  "v_add_f32_dpp v134, v134, v134 row_ror:4 row_mask:0xf bank_mask:0xf\n\t" \
  "v_add_f32_dpp v135, v135, v135 row_ror:4 row_mask:0xf bank_mask:0xf\n\t" \
  "v_add_f32_dpp v128, v128, v128 row_ror:8 row_mask:0xf bank_mask:0xf\n\t" \
  "v_add_f32_dpp v129, v129, v129 row_ror:8 row_mask:0xf bank_mask:0xf\n\t" \
  "v_add_f32_dpp v130, v130, v130 row_ror:8 row_mask:0xf bank_mask:0xf\n\t" \
  "v_add_f32_dpp v131, v131, v131 row_ror:8 row_mask:0xf bank_mask:0xf\n\t" \
  "v_add_f32_dpp v132, v132, v132 row_ror:8 row_mask:0xf bank_mask:0xf\n\t" \
  "v_add_f32_dpp v133, v133, v133 row_ror:8 row_mask:0xf bank_mask:0xf\n\t" \
  "v_add_f32_dpp v134, v134, v134 row_ror:8 row_mask:0xf bank_mask:0xf\n\t" \
  "v_add_f32_dpp v135, v135, v135 row_ror:8 row_mask:0xf bank_mask:0xf\n\t" \
  "v_mov_b32 v224, v128\n\t" \
  "v_mov_b32 v225, v129\n\t" \
  "v_mov_b32 v226, v130\n\t" \
  "v_mov_b32 v227, v131\n\t" \
  "v_mov_b32 v228, v132\n\t" \
  "v_mov_b32 v229, v133\n\t" \
  "v_mov_b32 v230, v134\n\t" \
  "v_mov_b32 v231, v135\n\t" \
  "v_permlane16_swap_b32 v128, v224\n\t" \
  "v_permlane16_swap_b32 v129, v225\n\t" \
  "v_permlane16_swap_b32 v130, v226\n\t" \
  "v_permlane16_swap_b32 v131, v227\n\t" \
  "v_permlane16_swap_b32 v132, v228\n\t" \
  "v_permlane16_swap_b32 v133, v229\n\t" \
  "v_permlane16_swap_b32 v134, v230\n\t" \
  "v_permlane16_swap_b32 v135, v231\n\t" \
  "v_add_f32 v128, v128, v224\n\t" \
  "v_add_f32 v129, v129, v225\n\t" \
  "v_add_f32 v130, v130, v226\n\t" \
  "v_add_f32 v131, v131, v227\n\t" \
  "v_add_f32 v132, v132, v228\n\t" \
  "v_add_f32 v133, v133, v229\n\t" \
  "v_add_f32 v134, v134, v230\n\t" \
  "v_add_f32 v135, v135, v231\n\t" \
  "v_mov_b32 v224, v128\n\t" \
  "v_mov_b32 v225, v129\n\t" \
  "v_mov_b32 v226, v130\n\t" \
  "v_mov_b32 v227, v131\n\t" \
  "v_mov_b32 v228, v132\n\t" \
  "v_mov_b32 v229, v133\n\t" \
  "v_mov_b32 v230, v134\n\t" \
  "v_mov_b32 v231, v135\n\t" \
  "v_permlane32_swap_b32 v128, v224\n\t" \
  "v_permlane32_swap_b32 v129, v225\n\t" \
  "v_permlane32_swap_b32 v130, v226\n\t" \
  "v_permlane32_swap_b32 v131, v227\n\t" \
  "v_permlane32_swap_b32 v132, v228\n\t" \
  "v_permlane32_swap_b32 v133, v229\n\t" \
  "v_permlane32_swap_b32 v134, v230\n\t" \
  "v_permlane32_swap_b32 v135, v231\n\t" \
  "v_add_f32 v128, v128, v224\n\t" \
  "v_add_f32 v129, v129, v225\n\t" \
  "v_add_f32 v130, v130, v226\n\t" \
  "v_add_f32 v131, v131, v227\n\t" \
  "v_add_f32 v132, v132, v228\n\t" \
  "v_add_f32 v133, v133, v229\n\t" \
  "v_add_f32 v134, v134, v230\n\t" \
  "v_add_f32 v135, v135, v231\n\t" \
  "ds_write_b128 %[lcs], v[128:131]\n\t" \
  "ds_write_b128 %[lcs], v[132:135] offset:16\n\t" \
  "s_waitcnt vmcnt(3)\n\t" \
  "ds_read_b64_tr_b16 v[136:137], %[ltr] offset:0\n\t" \
  "ds_read_b64_tr_b16 v[138:139], %[ltr] offset:1024\n\t" \
  "ds_read_b64_tr_b16 v[140:141], %[ltr] offset:64\n\t" \
  "ds_read_b64_tr_b16 v[142:143], %[ltr] offset:1088\n\t" \
  "ds_read_b64_tr_b16 v[144:145], %[ltr] offset:8192\n\t" \
  "ds_read_b64_tr_b16 v[146:147], %[ltr] offset:9216\n\t" \
  "ds_read_b64_tr_b16 v[148:149], %[ltr] offset:8256\n\t" \
  "ds_read_b64_tr_b16 v[150:151], %[ltr] offset:9280\n\t" \
  "ds_read_b64_tr_b16 v[152:153], %[ltr] offset:16384\n\t" \
  "ds_read_b64_tr_b16 v[154:155], %[ltr] offset:17408\n\t" \
  "ds_read_b64_tr_b16 v[156:157], %[ltr] offset:16448\n\t" \
  "ds_read_b64_tr_b16 v[158:159], %[ltr] offset:17472\n\t" \
  "s_waitcnt lgkmcnt(0)\n\t" \
  "buffer_store_dwordx4 v[136:139], %[l16], %[rs], %[d0a] offen sc1\n\t" \
  "buffer_store_dwordx4 v[140:143], %[l16], %[rs], %[d0b] offen sc1\n\t" \
  "buffer_store_dwordx4 v[144:147], %[l16], %[rs], %[d1a] offen sc1\n\t" \
  "buffer_store_dwordx4 v[148:151], %[l16], %[rs], %[d1b] offen sc1\n\t" \
  "buffer_store_dwordx4 v[152:155], %[l16], %[rs], %[d2a] offen sc1\n\t" \
  "buffer_store_dwordx4 v[156:159], %[l16], %[rs], %[d2b] offen sc1\n\t" \
  "s_barrier\n\t" \
  "ds_read_b32 v240, %[lcr] offset:0\n\t" \
  "ds_read_b32 v241, %[lcr] offset:128\n\t" \
  "ds_read_b32 v242, %[lcr] offset:256\n\t" \
  "ds_read_b32 v243, %[lcr] offset:384\n\t" \
  "s_waitcnt lgkmcnt(0)\n\t" \
  "v_add_f32 v240, v240, v241\n\t" \
  "v_add_f32 v242, v242, v243\n\t" \
  "v_add_f32 v240, v240, v242\n\t" \
  "buffer_store_dword v240, %[vcs], %[rs], %[scs] offen sc1\n\t"
#define P0_TAIL_ASM \
  "s_nop 4\n\t" \
  "buffer_load_dword v240, %[l4], %[rs], %[sexq] offen sc1\n\t" \
  "buffer_load_dword v241, %[l4], %[rs], %[sexq] offen offset:128 sc1\n\t" \
  "buffer_load_dword v242, %[l4], %[rs], %[sex1] offen sc1\n\t" \
  "buffer_load_dword v243, %[l4], %[rs], %[sex1] offen offset:128 sc1\n\t" \
  "s_waitcnt vmcnt(0)\n\t" \
  "v_add_f32 v240, v240, v241\n\t" \
  "v_mul_f32 v240, v240, -%[hm]\n\t" \
  "v_cvt_pk_bf16_f32 v232, v240, v240\n\t" \
  "v_mov_b32 v234, 0\n\t" \
  "v_lshlrev_b32 v233, 16, v232\n\t" \
  "v_mov_b32 v235, 0\n\t" \
  "v_sub_f32 v233, v240, v233\n\t" \
  "v_cvt_pk_bf16_f32 v233, v233, v234\n\t" \
  "ds_write_b128 %[lexq], v[232:235]\n\t" \
  "v_add_f32 v242, v242, v243\n\t" \
  "v_mul_f32 v242, v242, -%[hm]\n\t" \
  "v_cvt_pk_bf16_f32 v236, v242, v242\n\t" \
  "v_mov_b32 v238, 0\n\t" \
  "v_lshlrev_b32 v237, 16, v236\n\t" \
  "v_mov_b32 v239, 0\n\t" \
  "v_sub_f32 v237, v242, v237\n\t" \
  "v_cvt_pk_bf16_f32 v237, v237, v238\n\t" \
  "ds_write_b128 %[lex1], v[236:239]\n\t" \
  "s_waitcnt lgkmcnt(0)\n\t"
#define OWN "v64","v65","v66","v67","v68","v69","v70","v71","v72","v73","v74","v75","v76","v77","v78","v79","v80","v81","v82","v83","v84","v85","v86","v87","v88","v89","v90","v91","v92","v93","v94","v95","v96","v97","v98","v99","v100","v101","v102","v103","v104","v105","v106","v107","v108","v109","v110","v111","v112","v113","v114","v115","v116","v117","v118","v119","v120","v121","v122","v123","v124","v125","v126","v127","v128","v129","v130","v131","v132","v133","v134","v135","v136","v137","v138","v139","v140","v141","v142","v143","v144","v145","v146","v147","v148","v149","v150","v151","v152","v153","v154","v155","v156","v157","v158","v159","v160","v161","v162","v163","v164","v165","v166","v167","v168","v169","v170","v171","v172","v173","v174","v175","v176","v177","v178","v179","v180","v181","v182","v183","v184","v185","v186","v187","v188","v189","v190","v191","v192","v193","v194","v195","v196","v197","v198","v199","v200","v201","v202","v203","v204","v205","v206","v207","v208","v209","v210","v211","v212","v213","v214","v215","v216","v217","v218","v219","v220","v221","v222","v223","v224","v225","v226","v227","v228","v229","v230","v231","v232","v233","v234","v235","v236","v237","v238","v239","v240","v241","v242","v243","v244","v245","v246","v247","v248","v249","v250","v251","v252","v253","v254","v255","a0","a1","a2","a3","a4","a5","a6","a7","a8","a9","a10","a11","a12","a13","a14","a15","a16","a17","a18","a19","a20","a21","a22","a23","a24","a25","a26","a27","a28","a29","a30","a31","a32","a33","a34","a35","a36","a37","a38","a39","a40","a41","a42","a43","a44","a45","a46","a47","a48","a49","a50","a51","a52","a53","a54","a55","a56","a57","a58","a59","a60","a61","a62","a63","a64","a65","a66","a67","a68","a69","a70","a71","a72","a73","a74","a75","a76","a77","a78","a79","a80","a81","a82","a83","a84","a85","a86","a87","a88","a89","a90","a91","a92","a93","a94","a95","a96","a97","a98","a99","a100","a101","a102","a103","a104","a105","a106","a107","a108","a109","a110","a111","a112","a113","a114","a115","a116","a117","a118","a119","a120","a121","a122","a123","a124","a125","a126","a127","a128","a129","a130","a131","a132","a133","a134","a135","a136","a137","a138","a139","a140","a141","a142","a143","a144","a145","a146","a147","a148","a149","a150","a151","a152","a153","a154","a155","a156","a157","a158","a159","a160","a161","a162","a163","a164","a165","a166","a167","a168","a169","a170","a171","a172","a173","a174","a175","a176","a177","a178","a179","a180","a181","a182","a183","a184","a185","a186","a187","a188","a189","a190","a191","a192","a193","a194","a195","a196","a197","a198","a199","a200","a201","a202","a203","a204","a205","a206","a207","a208","a209","a210","a211","a212","a213","a214","a215","a216","a217","a218","a219","a220","a221","a222","a223","a224","a225","a226","a227","a228","a229","a230","a231","a232","a233","a234","a235","a236","a237","a238","a239","a240","a241","a242","a243","a244","a245","a246","a247","a248","a249","a250","a251","a252","a253","a254","a255"


constexpr u32 MB = 1u << 20;
constexpr u32 WS_FLAGS = 4096;
constexpr u32 WS_CQ = 512 * 1024;
constexpr u32 WS_C1 = WS_CQ + 16384;
constexpr u32 WS_KM = WS_C1 + 16384;
constexpr u32 WS_ST = 1 * MB;
constexpr u32 WS_XB = 2 * MB;
constexpr u32 WS_QF = 10 * MB;
constexpr u32 WS_KF = 14 * MB;
constexpr u32 WS_VF = 18 * MB;
constexpr u32 WS_OF = 22 * MB;
constexpr u32 WS_HF = 26 * MB;
constexpr u32 WS_WQ = 40 * MB;
constexpr u32 WS_WO = 42 * MB;
constexpr u32 WS_W1 = 43 * MB;
constexpr u32 WS_W2 = 45 * MB;
constexpr u32 WS_BYTES = 48 * MB;
constexpr int FL_P0A = 0;

constexpr u32 L_K = 0, L_V = 65536, VSTR = 1056;
constexpr u32 L_W = 0;
constexpr u32 L_STG = 98304;
constexpr u32 L_RED = 98304;
constexpr u32 L_TBL = 147456;
constexpr u32 L_CSR = L_TBL + 512;
constexpr u32 L_EXQ = L_CSR + 512;
constexpr u32 L_EX1 = L_EXQ + 3072;
constexpr u32 LDS_BYTES = L_EX1 + 4096;

constexpr int NWQ1 = 10;
constexpr float QSCALE = 0.25f * 1.4426950408889634f;
constexpr float SAFE_BOUND2 = 60.f * 60.f / 1.02f;

struct Params { const u16* x; const u16* wqkv; const u16* wo; const u16* w1; const u16* w2; u16* out; char* ws; };

#define DMA_SEQ(NAME, FLAGS) \
template <int N, int SSTEP, int LSTEP> DI void NAME(rsrc_t rs, u32 voff, u32 soff, u32 ldsaddr) { \
  u32 t; \
  asm volatile("s_nop 4\n\ts_mov_b32 m0, %1\n\ts_mov_b32 %0, %4\n\t" \
               ".rept %c5\n\tbuffer_load_dwordx4 %2, %3, %0 offen " FLAGS "lds\n\ts_add_u32 m0, m0, %c7\n\ts_add_u32 %0, %0, %c6\n\t.endr" \
               : "=&s"(t) : "s"(ldsaddr), "v"(voff), "s"(rs), "s"(soff), "i"(N), "i"(SSTEP), "i"(LSTEP) : "memory", "scc"); }
DMA_SEQ(dma_seq, "sc1 ")
template <int N> DI void dma_kv(rsrc_t rs, u32 voff, u32& gk, u32& gv, u32& mk, u32& mv) {
  asm volatile("s_nop 4\n\t.rept %c6\n\ts_mov_b32 m0, %2\n\ts_add_u32 %2, %2, 4096\n\tbuffer_load_dwordx4 %4, %5, %0 offen sc1 lds\n\ts_add_u32 %0, %0, 4096\n\t"
               "s_mov_b32 m0, %3\n\ts_add_u32 %3, %3, 4224\n\tbuffer_load_dwordx4 %4, %5, %1 offen sc1 lds\n\ts_add_u32 %1, %1, 4096\n\t.endr"
               : "+s"(gk), "+s"(gv), "+s"(mk), "+s"(mv) : "v"(voff), "s"(rs), "i"(N) : "memory", "scc");
}
DI void glds16(const void* g, u32 ldsaddr) { asm volatile("s_nop 4\n\ts_mov_b32 m0, %1\n\ts_nop 0\n\tglobal_load_lds_dwordx4 %0, off" ::"v"(g), "s"(ldsaddr) : "memory"); }
DI void ldsw(u32 addr, u32x4 v) { *(LAS u32x4*)(size_t)addr = v; }
#define VMWAIT0() asm volatile("s_waitcnt vmcnt(0)" ::: "memory")
#define BAR() do { asm volatile("" ::: "memory"); __builtin_amdgcn_s_barrier(); asm volatile("" ::: "memory"); } while (0)

DI int cu_of(int rg, int cg) { return (cg << 4) | ((rg & 1) << 3) | (rg >> 1); }

DI void wait_flags(char* ws, int inst, int cu, u64 tag) {
  const GAS u64* p = (const GAS u64*)(ws + WS_FLAGS + ((u32)inst * 256u + (u32)cu) * 128u);
  u32 lo = (u32)tag, hi = (u32)(tag >> 32); asm volatile("" : "+s"(lo), "+s"(hi));
  for (int spin = 0; spin < (1 << 18); ++spin) {
    const u64 v = __hip_atomic_load(p, __ATOMIC_RELAXED, __HIP_MEMORY_SCOPE_AGENT);
    if (__all((u32)v == lo && (u32)(v >> 32) == hi)) break;
    __builtin_amdgcn_s_sleep(1);
  }
  asm volatile("" ::: "memory");
}
DI void wait_flags5(char* ws, int i0, int cu0, int i1, int cu1, u64 tag) {
  const GAS u64* p = (const GAS u64*)(ws + WS_FLAGS + ((u32)i0 * 256u + (u32)cu0) * 128u);
  const GAS u64* q = (const GAS u64*)(ws + WS_FLAGS + ((u32)i1 * 256u + (u32)cu1) * 128u);
  u32 lo = (u32)tag, hi = (u32)(tag >> 32); asm volatile("" : "+s"(lo), "+s"(hi));
  for (int spin = 0; spin < (1 << 18); ++spin) {
    const u64 v0 = __hip_atomic_load(p, __ATOMIC_RELAXED, __HIP_MEMORY_SCOPE_AGENT), v1 = __hip_atomic_load(p + 1024, __ATOMIC_RELAXED, __HIP_MEMORY_SCOPE_AGENT);
    const u64 v2 = __hip_atomic_load(p + 2048, __ATOMIC_RELAXED, __HIP_MEMORY_SCOPE_AGENT), v3 = __hip_atomic_load(p + 3072, __ATOMIC_RELAXED, __HIP_MEMORY_SCOPE_AGENT);
    const u64 v4 = __hip_atomic_load(q, __ATOMIC_RELAXED, __HIP_MEMORY_SCOPE_AGENT);
    const u64 t = ((u64)hi << 32) | lo;
    if (__all(v0 == t && v1 == t && v2 == t && v3 == t && v4 == t)) break;
    __builtin_amdgcn_s_sleep(1);
  }
  asm volatile("" ::: "memory");
}
template <int N> DI void publish(rsrc_t rs, int inst, int b, u64 tag) {
  const u32 vfl = threadIdx.x == 0 ? 0u : 0x7ffffff0u;
  asm volatile("s_waitcnt vmcnt(%4) lgkmcnt(0)\n\ts_barrier\n\tbuffer_store_dwordx2 %0, %1, %2, %3 offen sc1"
               ::"v"(tag), "v"(vfl), "s"(rs), "s"(WS_FLAGS + ((u32)inst * 256u + (u32)b) * 128u), "i"(N) : "memory");
}


#define IDX() \
  int tid = threadIdx.x, b = blockIdx.x; asm volatile("" : "+v"(tid)); asm volatile("" : "+s"(b)); \
  const int lane = tid & 63, w = __builtin_amdgcn_readfirstlane(tid >> 6), t32 = lane & 31, h = lane >> 5; const u32 l16 = lane * 16; \
  const int rg = (b & 7) * 2 + ((b >> 3) & 1), cg = b >> 4, T64 = 2 * rg + (cg & 1), fg = cg >> 1; \
  const int TTo = 2 * T64 + (w & 1), FTo = 2 * fg + (w >> 1), TTw = 4 * rg + w; \
  (void)t32; (void)h; (void)l16; (void)TTo; (void)FTo; (void)TTw; (void)T64; (void)fg


__global__ __launch_bounds__(256, 1) __attribute__((amdgpu_num_vgpr(64))) void mega(Params p) {
  extern __shared__ __attribute__((aligned(16))) char lds_raw[];
  const u32 lds0 = (u32)(size_t)(LAS char*)lds_raw;
  const rsrc_t rs = __builtin_amdgcn_make_buffer_rsrc(p.ws, 0, WS_BYTES, 0x00020000);
  u32x4 pf_dummy;
  {
    u64 pc; u32 len;
    asm volatile("s_getpc_b64 %0\n.Lmega_pf0:\n\ts_mov_b32 %1, .Lmega_pf1-.Lmega_pf0" : "=s"(pc), "=s"(len));
    u32 off = ((blockIdx.x >> 3) * 256 + threadIdx.x) * 16;
    off = off + 16 <= len ? off : 0;
    asm volatile("global_load_dwordx4 %0, %1, %2" : "=v"(pf_dummy) : "v"(off), "s"(pc & ~15ull) : "memory");
  }
  u64 tag;
  {
    IDX();
    const rsrc_t rx = __builtin_amdgcn_make_buffer_rsrc((void*)p.x, 0, 2048 * 512 * 2, 0x00020000);
    const int i32 = b & 31, bg = b >> 5;
    const u16* cs; int cld, cbytes; u32 cdst; bool cval; int khalf;
    if (i32 < 12) { const int hp = 2 * bg + i32 / 6, t = (i32 % 6) >> 1; khalf = i32 & 1; cs = p.wqkv + 512 * t + 32 * hp; cld = 1536; cbytes = (512 * 1536 - 512 * t - 32 * hp) * 2; cdst = WS_CQ + (hp * 3 + t) * 256 + khalf * 128; cval = true; }
    else { int ft = 8 * bg + ((i32 - 12) >> 1); khalf = i32 & 1; cval = (i32 < 28) && (ft < 56); ft = cval ? ft : 0; cs = p.w1 + 32 * ft; cld = 1792; cbytes = (512 * 1792 - 32 * ft) * 2; cdst = WS_C1 + ft * 256 + khalf * 128; }
    const rsrc_t rc = __builtin_amdgcn_make_buffer_rsrc((void*)cs, 0, cbytes, 0x00020000);
    {
      u32 tc = 0;
      asm volatile(P0_ISSUE_ASM : [tc] "+s"(tc)
                   : [vz] "v"(0u), [vo] "v"((u32)((32 * TTo + t32) * 1024 + (32 * FTo + 4 * h) * 2)), [vc] "v"((u32)(((256 * khalf + 64 * w + (lane >> 2)) * cld + 8 * (lane & 3)) * 2)),
                     [rs] "s"(rs), [rx] "s"(rx), [rc] "s"(rc), [cstep] "s"(32 * cld) : OWN, "scc", "memory");
    }
    u32 da[3], db[3];
#pragma unroll
    for (int s = 0; s < 3; ++s) {
      int i = w + 4 * s; i = i > 10 ? 10 : i;
      const u16* src; int ld, n0, k0;
      if (i < 3) { const int q = i32 * 3 + i, t = q >> 5, ks = q & 31; src = p.wqkv; ld = 1536; n0 = 512 * t + 64 * bg; k0 = 16 * ks;
                   da[s] = WS_WQ + ((2 * bg) * 96 + q) * 1024; db[s] = da[s] + 96 * 1024; }
      else if (i < 4) { src = p.wo; ld = 512; n0 = 64 * (b >> 5); k0 = 16 * (b & 31); da[s] = WS_WO + (2 * (b >> 5) * 32 + (b & 31)) * 1024; db[s] = da[s] + 32 * 1024; }
      else {
        const int q = b * 7 + (i - 4);
        if (q < 896) { const int fp = q >> 5; src = p.w1; ld = 1792; n0 = 64 * fp; k0 = 16 * (q & 31); da[s] = WS_W1 + (2 * fp * 32 + (q & 31)) * 1024; db[s] = da[s] + 32 * 1024; }
        else { const int r = q - 896, fp = r / 112, ks = r - fp * 112; src = p.w2; ld = 512; n0 = 64 * fp; k0 = 16 * ks; da[s] = WS_W2 + (2 * fp * 112 + ks) * 1024; db[s] = da[s] + 112 * 1024; }
      }
      const u16* g = src + (k0 + (lane >> 3)) * ld + n0 + 8 * (lane & 7);
      glds16(g, lds0 + L_STG + (s * 4 + w) * 2048); glds16(g + 8 * ld, lds0 + L_STG + (s * 4 + w) * 2048 + 1024);
    }
    {
      u32 to = 0x7ffffff0u;
      if (tid < 12) to = WS_FLAGS + ((u32)tid * 256u + (u32)b) * 128u;
      else if (tid < 20) to = WS_KM + ((tid >> 2) & 1) * 8192 + (2 * cg + (tid & 1)) * 256 + (((4 * rg) >> 5) + 0) * 128 + 0 * ((tid >> 1) & 1);
      asm volatile("buffer_load_dword v250, %0, %1, 0 offen sc1" ::"v"(to), "s"(rs) : OWN, "memory");
    }
    {
      u32 tg0, tg1;
      asm volatile(P0_COMP_ASM : [tg0] "=&s"(tg0), [tg1] "=&s"(tg1)
                   : [l16] "v"(l16), [vst] "v"(lane < 32 ? lane * 8 : 0x7ffffff0u), [lcs] "v"(lds0 + L_CSR + w * 128 + (lane & 3) * 32), [lcr] "v"(lds0 + L_CSR + t32 * 4),
                     [ltr] "v"(lds0 + L_STG + w * 2048 + (4 * h + ((lane & 15) >> 2)) * 128 + (((lane >> 4) & 1) * 16 + (lane & 3) * 4) * 2),
                     [vcs] "v"((tid < 32 && cval) ? tid * 4 : 0x7ffffff0u), [rs] "s"(rs),
                     [sx] "s"(WS_XB + (TTo * 32 + 2 * FTo) * 1024), [sxs] "s"(WS_ST + (TTo * 16 + FTo) * 256), [scs] "s"(cdst),
                     [d0a] "s"(da[0]), [d0b] "s"(db[0]), [d1a] "s"(da[1]), [d1b] "s"(db[1]), [d2a] "s"(da[2]), [d2b] "s"(db[2])
                   : OWN, "memory");
      asm volatile("" : "+v"(pf_dummy));
      tag = (((u64)tg1 << 32) | tg0) + 1;
    }
    publish<0>(rs, FL_P0A, b, tag);
    const float hm = h ? 0.f : -1.f; const u32 lw0 = lds0 + L_W + l16, oa = ((w & 1) * 32 + (w >> 1)) * 1024, ob = (64 + w) * 1024;
    auto q0_l16 = l16; auto q0_sv = (u32)(t32 * 8 + h * 2048); auto q0_l4 = (u32)(t32 * 4); auto q0_lw0 = lw0; auto q0_lw1 = lw0 + 65536; auto q0_ltw = lds0 + L_TBL + w * 128 + t32 * 4; auto q0_ltr = lds0 + L_TBL + w * 128 + h * 16; auto q0_vv = (u32)((t32 >> 4) * 64) * 1024 + h * 256 + (t32 & 15) * 16; auto q0_vkm = lane == 31 ? 0u : 0x7ffffff0u; auto q0_hm = hm; auto q0_sact = WS_XB + TTw * 32 * 1024; auto q0_sst = WS_ST + TTw * 4096; auto q0_scq = WS_CQ + cg * 768; auto q0_lA = lds0 + L_W + oa; auto q0_gA = WS_WQ + cg * 96 * 1024 + oa; auto q0_lB = lds0 + L_W + ob; auto q0_gB = WS_WQ + cg * 96 * 1024 + ob; auto q0_sq = WS_QF + (2 * cg * 64 + TTw) * 1024; auto q0_sk = WS_KF + (2 * cg * 64 + TTw) * 1024; auto q0_svv = WS_VF + (2 * cg * 64 + TTw) * 1024; auto q0_skm = WS_KM + (2 * cg * 64 + TTw) * 4;
    asm volatile("" : "+s"(q0_sact), "+s"(q0_sst), "+s"(q0_scq), "+s"(q0_lA), "+s"(q0_gA), "+s"(q0_lB), "+s"(q0_gB), "+s"(q0_sq), "+s"(q0_sk), "+s"(q0_svv), "+s"(q0_skm));
    asm volatile("" : "+v"(q0_l16), "+v"(q0_sv), "+v"(q0_l4), "+v"(q0_lw0), "+v"(q0_lw1), "+v"(q0_ltw), "+v"(q0_ltr), "+v"(q0_vv), "+v"(q0_vkm), "+v"(q0_hm));
    wait_flags(p.ws, FL_P0A, lane < 8 ? cu_of(rg, 2 * lane + (w >> 1)) : 32 * (cg >> 1) + (lane & 31), tag);
    {
      u32 t0, t1, sq1, sk1;
      asm volatile(Q0_ASM : [t0] "=&s"(t0), [t1] "=&s"(t1), [sq1] "=&s"(sq1), [sk1] "=&s"(sk1)
                   : [l16] "v"(q0_l16), [sv] "v"(q0_sv), [l4] "v"(q0_l4), [lw0] "v"(q0_lw0), [lw1] "v"(q0_lw1), [ltw] "v"(q0_ltw),
                     [ltr] "v"(q0_ltr), [vv] "v"(q0_vv), [vkm] "v"(q0_vkm), [hm] "v"(q0_hm), [rs] "s"(rs),
                     [sact] "s"(q0_sact), [sst] "s"(q0_sst), [scq] "s"(q0_scq),
                     [lA] "s"(q0_lA), [gA] "s"(q0_gA), [lB] "s"(q0_lB), [gB] "s"(q0_gB),
                     [sq] "s"(q0_sq), [sk] "s"(q0_sk),
                     [svv] "s"(q0_svv), [skm] "s"(q0_skm): OWN, "scc", "memory");
    }
    publish<0>(rs, 1, b, tag);
  }

#pragma unroll 1
  for (int layer = 0; layer < 2; ++layer) {
    if (layer) {
      IDX();
      const u32 lw0 = lds0 + L_W + l16;
      auto q1_l16 = l16; auto q1_sv = (u32)(t32 * 8 + h * 2048); auto q1_lw0 = lw0; auto q1_lw1 = lw0 + 65536; auto q1_lext = lds0 + L_EXQ + l16; auto q1_ltw = lds0 + L_TBL + w * 128 + t32 * 4; auto q1_ltr = lds0 + L_TBL + w * 128 + h * 16; auto q1_vv = (u32)((t32 >> 4) * 64) * 1024 + h * 256 + (t32 & 15) * 16; auto q1_vkm = lane == 31 ? 0u : 0x7ffffff0u; auto q1_hm = h ? 0.f : -1.f; auto q1_sact = WS_XB + 2 * 2 * MB + TTw * 32 * 1024; auto q1_sst = WS_ST + 2 * 262144 + TTw * 4096; auto q1_sq = WS_QF + 2 * MB + (2 * cg * 64 + TTw) * 1024; auto q1_sk = WS_KF + 2 * MB + (2 * cg * 64 + TTw) * 1024; auto q1_svv = WS_VF + 2 * MB + (2 * cg * 64 + TTw) * 1024; auto q1_skm = WS_KM + 8192 + (2 * cg * 64 + TTw) * 4;
      asm volatile("" : "+s"(q1_sact), "+s"(q1_sst), "+s"(q1_sq), "+s"(q1_sk), "+s"(q1_svv), "+s"(q1_skm));
      asm volatile("" : "+v"(q1_l16), "+v"(q1_sv), "+v"(q1_lw0), "+v"(q1_lw1), "+v"(q1_lext), "+v"(q1_ltw), "+v"(q1_ltr), "+v"(q1_vv), "+v"(q1_vkm), "+v"(q1_hm));
      wait_flags(p.ws, 5, cu_of(rg, 2 * (lane & 7) + (w >> 1)), tag);
      u32 t0, sq1, sk1;
      asm volatile(Q1_ASM : [t0] "=&s"(t0), [sq1] "=&s"(sq1), [sk1] "=&s"(sk1)
                   : [l16] "v"(q1_l16), [sv] "v"(q1_sv), [lw0] "v"(q1_lw0), [lw1] "v"(q1_lw1), [lext] "v"(q1_lext), [ltw] "v"(q1_ltw),
                     [ltr] "v"(q1_ltr), [vv] "v"(q1_vv), [vkm] "v"(q1_vkm), [hm] "v"(q1_hm), [rs] "s"(rs),
                     [sact] "s"(q1_sact), [sst] "s"(q1_sst),
                     [sq] "s"(q1_sq), [sk] "s"(q1_sk),
                     [svv] "s"(q1_svv), [skm] "s"(q1_skm): OWN, "scc", "memory");
      publish<0>(rs, 6, b, tag);
    }
    {
      IDX();
      const int hd = 4 * (b & 7) + ((b >> 3) & 3), qt = b >> 5;
      asm volatile(ATT_INIT_ASM ::: OWN);
      {
        u32 c1 = 0x3f803f80u, c0 = 0; asm volatile("" : "+v"(c1), "+v"(c0));
        const u32 ca = lds0 + L_V + (tid < 65 ? tid : 64) * VSTR + 1024;
        ldsw(ca, (u32x4){c1, c1, c1, c1}); ldsw(ca + 16, (u32x4){c0, c0, c0, c0});
      }
      u32 gk = WS_KF + layer * 2 * MB + (hd * 64 + w) * 1024, gv = WS_VF + layer * 2 * MB + (hd * 64 + w) * 1024, mk = lds0 + L_K + w * 1024, mv = lds0 + L_V + w * VSTR;
      asm volatile("" : "+s"(gk), "+s"(gv), "+s"(mk), "+s"(mv));
      auto aq_l16 = l16; auto aq_l4 = (u32)(lane * 4); auto aq_qo = WS_QF + layer * 2 * MB + (hd * 64 + 8 * qt + 2 * w) * 1024; auto aq_ko = WS_KM + layer * 8192 + hd * 256;
      asm volatile("" : "+s"(aq_qo), "+s"(aq_ko));
      asm volatile("" : "+v"(aq_l16), "+v"(aq_l4));
      if (layer == 0) wait_flags5(p.ws, FL_P0A, lane, 1, cu_of(lane & 15, hd >> 1), tag); else wait_flags(p.ws, 6, cu_of(lane & 15, hd >> 1), tag);
      asm volatile(ATT_Q_ASM ::[l16] "v"(aq_l16), [l4] "v"(aq_l4), [rs] "s"(rs), [qo] "s"(aq_qo), [ko] "s"(aq_ko): OWN, "memory");
      dma_kv<5>(rs, l16, gk, gv, mk, mv);
      u32 fast;
      { u32 t0, t1;
        asm volatile(ATT_PRE_ASM : [fast] "=&s"(fast), [t0] "=&s"(t0), [t1] "=&s"(t1) : [bnd] "s"(__float_as_uint(SAFE_BOUND2)) : OWN, "vcc", "scc", "memory"); }
      const int d = t32;
      const u32 va0 = d < 16 ? (u32)(h * 256 + d * 16) : ((d == 16 || d == 20) ? 1024u : 1040u);
      const u32 va1 = d < 16 ? va0 + 512 : va0;
      u32 ka = lds0 + L_K + l16, a0 = lds0 + L_V + va0, a1 = lds0 + L_V + va1;
      u32 gwoa = WS_WO + (FTo * 32 + 8 * w) * 1024, gwob = WS_WO + ((FTo ^ 1) * 32 + 8 * w) * 1024, gw1 = WS_W1 + ((cg < 14 ? cg : 13) * 128 + w * 32) * 1024;
      u32 wa = WS_W2 + (FTo * 112 + 28 * w) * 1024, wb = WS_W2 + ((FTo ^ 1) * 112 + 28 * w) * 1024;
      if (fast) {
        asm volatile(ATTN_ASM
                     : [ka] "+v"(ka), [a0] "+v"(a0), [a1] "+v"(a1), [gk] "+s"(gk), [gv] "+s"(gv), [gwoa] "+s"(gwoa), [gwob] "+s"(gwob), [gw1] "+s"(gw1), [wa] "+s"(wa), [wb] "+s"(wb)
                     : [l16] "v"(l16), [rs] "s"(rs), [mk] "s"(mk), [mv] "s"(mv) : OWN, "scc", "memory");
      } else {
        asm volatile(ATT_SLOWLD_ASM : [gk] "+s"(gk), [gv] "+s"(gv), [gwoa] "+s"(gwoa), [gwob] "+s"(gwob), [gw1] "+s"(gw1), [wa] "+s"(wa), [wb] "+s"(wb) : [l16] "v"(l16), [rs] "s"(rs), [mk] "s"(mk), [mv] "s"(mv) : OWN, "scc", "memory");
        VMWAIT0(); BAR();
        int cnt;
        asm volatile(ATT_SLOW_ASM : [ka] "+v"(ka), [a0] "+v"(a0), [a1] "+v"(a1), [cnt] "=&s"(cnt) :: OWN, "scc", "memory");
      }
      asm volatile(ATT_EPI_ASM ::[l16] "v"(l16), [rs] "s"(rs), [so0] "s"(WS_OF + layer * 2 * MB + ((8 * qt + 2 * w) * 32 + hd) * 1024), [so1] "s"(WS_OF + layer * 2 * MB + ((8 * qt + 2 * w + 1) * 32 + hd) * 1024) : OWN, "memory");
      publish<0>(rs, 2 + 5 * layer, b, tag);
      if (w < 3) asm volatile(W1DUMP_ASM ::[lw] "v"(lds0 + L_W + w * 32 * 1024 + l16) : "memory");
      if (layer == 0)
        asm volatile(P0_TAIL_ASM ::[l4] "v"((u32)(t32 * 4)), [hm] "v"(h ? 0.f : -1.f), [lexq] "v"(lds0 + L_EXQ + (w < 3 ? w : 2) * 1024 + l16), [lex1] "v"(lds0 + L_EX1 + w * 1024 + l16), [rs] "s"(rs),
                     [sexq] "s"(WS_CQ + (cg * 3 + (w < 3 ? w : 2)) * 256), [sex1] "s"(WS_C1 + ((cg < 14 ? cg : 0) * 4 + w) * 256) : OWN, "memory");
    }
    {
      IDX();
      const int qt = T64 >> 2, xo = 2 * layer + 1; const u32 rb = lds0 + L_RED + l16;
      auto o_l16 = l16; auto o_vst = lane < 32 ? lane * 8 : 0x7ffffff0u; auto o_r1 = rb + ((w ^ 1) * 3 + 0) * 4096; auto o_r2 = rb + ((w ^ 2) * 3 + 1) * 4096; auto o_r3 = rb + ((w ^ 3) * 3 + 2) * 4096; auto o_rr = rb + w * 3 * 4096; auto o_sa = WS_OF + layer * 2 * MB + (TTo * 32 + 8 * w) * 1024; auto o_sb = WS_OF + layer * 2 * MB + ((TTo ^ 1) * 32 + 8 * w) * 1024; auto o_sx = WS_XB + xo * 2 * MB + (TTo * 32 + 2 * FTo) * 1024; auto o_sxs = WS_ST + xo * 262144 + (TTo * 16 + FTo) * 256;
      asm volatile("" : "+s"(o_sa), "+s"(o_sb), "+s"(o_sx), "+s"(o_sxs));
      asm volatile("" : "+v"(o_l16), "+v"(o_vst), "+v"(o_r1), "+v"(o_r2), "+v"(o_r3), "+v"(o_rr));
      wait_flags(p.ws, 2 + 5 * layer, (qt << 5) | (((lane & 31) & 3) << 3) | ((lane & 31) >> 2), tag);
      { u32 t0, t1;
        asm volatile(O_ASM : [t0] "=&s"(t0), [t1] "=&s"(t1)
                     : [l16] "v"(o_l16), [vst] "v"(o_vst), [r1] "v"(o_r1), [r2] "v"(o_r2), [r3] "v"(o_r3),
                       [rr] "v"(o_rr), [rs] "s"(rs), [sa] "s"(o_sa), [sb] "s"(o_sb),
                       [sx] "s"(o_sx), [sxs] "s"(o_sxs): OWN, "scc", "memory");
      }
      publish<0>(rs, 3 + 5 * layer, b, tag);
      u32 wa = WS_W2 + (FTo * 112 + 28 * w) * 1024, wb = WS_W2 + ((FTo ^ 1) * 112 + 28 * w) * 1024;
      if (w == 3) asm volatile(W2OD_ASM : [wa] "+s"(wa), [wb] "+s"(wb) : [l16] "v"(l16), [rs] "s"(rs), [lw] "v"(lds0 + L_W + 3 * 32 * 1024 + l16) : OWN, "scc", "memory");
      else asm volatile(W2O_ASM : [wa] "+s"(wa), [wb] "+s"(wb) : [l16] "v"(l16), [rs] "s"(rs) : OWN, "scc", "memory");
      BAR();
    }
    {
      IDX();
      const int xin = 2 * layer + 1;
      if (cg < 14) {
        const u32 lw0 = lds0 + L_W + l16;
        auto f1_l16 = l16; auto f1_sv = (u32)(t32 * 8 + h * 2048); auto f1_lw0 = lw0; auto f1_lw1 = lw0 + 65536; auto f1_lext = lds0 + L_EX1 + l16; auto f1_hm = h ? 0.f : -1.f; auto f1_sact = WS_XB + xin * 2 * MB + TTw * 32 * 1024; auto f1_sst = WS_ST + xin * 262144 + TTw * 4096; auto f1_sst2 = WS_ST + xin * 262144 + TTo * 4096; auto f1_sh0 = WS_HF + layer * 7 * MB + (TTw * 112 + cg * 8) * 1024;
        asm volatile("" : "+s"(f1_sact), "+s"(f1_sst), "+s"(f1_sst2), "+s"(f1_sh0));
        asm volatile("" : "+v"(f1_l16), "+v"(f1_sv), "+v"(f1_lw0), "+v"(f1_lw1), "+v"(f1_lext), "+v"(f1_hm));
        wait_flags(p.ws, 3 + 5 * layer, cu_of(rg, lane & 15), tag);
        u32 t0, sh1;
        asm volatile(F1_ASM : [t0] "=&s"(t0), [sh1] "=&s"(sh1)
                     : [l16] "v"(f1_l16), [sv] "v"(f1_sv), [lw0] "v"(f1_lw0), [lw1] "v"(f1_lw1), [lext] "v"(f1_lext), [hm] "v"(f1_hm), [rs] "s"(rs),
                       [sact] "s"(f1_sact), [sst] "s"(f1_sst), [sst2] "s"(f1_sst2),
                       [sh0] "s"(f1_sh0): OWN, "scc", "memory");
      }
      else {
        wait_flags(p.ws, 3 + 5 * layer, cu_of(rg, lane & 15), tag);
        asm volatile(RSTD2_ASM ::[sv] "v"((u32)(t32 * 8 + h * 2048)), [rs] "s"(rs), [sst2] "s"(WS_ST + xin * 262144 + TTo * 4096) : OWN, "memory");
      }
      publish<0>(rs, 4 + 5 * layer, b, tag);
      u32 wa = WS_W2 + (FTo * 112 + 28 * w) * 1024, wb = WS_W2 + ((FTo ^ 1) * 112 + 28 * w) * 1024;
      asm volatile(W2F_ASM : [wa] "+s"(wa), [wb] "+s"(wb) : [l16] "v"(l16), [rs] "s"(rs) : OWN, "scc", "memory");
      if (layer == 0) { const u32 oa = ((w & 1) * 32 + (w >> 1)) * 1024; dma_seq<NWQ1, 2048, 2048>(rs, l16, WS_WQ + cg * 96 * 1024 + oa, lds0 + L_W + oa); }
    }
    {
      IDX();
      const u32 rb = lds0 + L_RED + l16;
      auto f2_l16 = l16; auto f2_r1 = rb + ((w ^ 1) * 3 + 0) * 4096; auto f2_r2 = rb + ((w ^ 2) * 3 + 1) * 4096; auto f2_r3 = rb + ((w ^ 3) * 3 + 2) * 4096; auto f2_rr = rb + w * 3 * 4096; auto f2_sha = WS_HF + layer * 7 * MB + (TTo * 112 + 28 * w) * 1024; auto f2_shb = WS_HF + layer * 7 * MB + ((TTo ^ 1) * 112 + 28 * w) * 1024;
      asm volatile("" : "+s"(f2_sha), "+s"(f2_shb));
      asm volatile("" : "+v"(f2_l16), "+v"(f2_r1), "+v"(f2_r2), "+v"(f2_r3), "+v"(f2_rr));
      wait_flags(p.ws, 4 + 5 * layer, cu_of(rg, (lane & 15) < 14 ? (lane & 15) : 0), tag);
      { u32 t0, t1;
        asm volatile(F2_ASM : [t0] "=&s"(t0), [t1] "=&s"(t1)
                     : [l16] "v"(f2_l16), [r1] "v"(f2_r1), [r2] "v"(f2_r2), [r3] "v"(f2_r3), [rr] "v"(f2_rr), [rs] "s"(rs),
                       [sha] "s"(f2_sha), [shb] "s"(f2_shb): OWN, "scc", "memory");
      }
      if (layer == 0) {
        asm volatile(STOREX_ASM ::[l16] "v"(l16), [vst] "v"(lane < 32 ? lane * 8 : 0x7ffffff0u), [rs] "s"(rs),
                     [sx] "s"(WS_XB + 2 * 2 * MB + (TTo * 32 + 2 * FTo) * 1024), [sxs] "s"(WS_ST + 2 * 262144 + (TTo * 16 + FTo) * 256) : OWN, "memory");
        publish<0>(rs, 5, b, tag);
        const u32 oa = ((w & 1) * 32 + (w >> 1)) * 1024 + NWQ1 * 2048, ob = (64 + w) * 1024;
        dma_seq<16 - NWQ1, 2048, 2048>(rs, l16, WS_WQ + cg * 96 * 1024 + oa, lds0 + L_W + oa);
        dma_seq<8, 4096, 4096>(rs, l16, WS_WQ + cg * 96 * 1024 + ob, lds0 + L_W + ob);
      } else {
        const rsrc_t ro = __builtin_amdgcn_make_buffer_rsrc((void*)p.out, 0, 2048 * 512 * 2, 0x00020000);
        asm volatile(OUT_ASM ::[vo] "v"((u32)((32 * TTo + t32) * 1024 + (32 * FTo + 8 * h) * 2)), [ro] "s"(ro) : OWN, "memory");
      }
    }
  }
  if (blockIdx.x == 0 && threadIdx.x == 0) __hip_atomic_store((GAS u64*)p.ws, tag, __ATOMIC_RELAXED, __HIP_MEMORY_SCOPE_AGENT);
  asm volatile(".Lmega_pf1:");
}

extern "C" void kernel_launch(void* const* d_in, const int* in_sizes, int n_in, void* d_out, int out_size, void* d_ws, size_t ws_size, hipStream_t stream) {
  Params p;
  p.x = (const u16*)d_in[0]; p.wqkv = (const u16*)d_in[1]; p.wo = (const u16*)d_in[2];
  p.w1 = (const u16*)d_in[4]; p.w2 = (const u16*)d_in[5]; p.out = (u16*)d_out; p.ws = (char*)d_ws;
  (void)hipFuncSetAttribute((const void*)mega, hipFuncAttributeMaxDynamicSharedMemorySize, LDS_BYTES);
  mega<<<256, 256, LDS_BYTES, stream>>>(p);
}
```
